```python
import torch

def _softmax(x, axis=-1):
    return torch.softmax(x, dim=axis)
def _gelu(x):
    return torch.nn.functional.gelu(x, approximate="tanh")
def _silu(x):
    return torch.nn.functional.silu(x)
def _sigmoid(x):
    return torch.sigmoid(x)
def _layernorm(x, axis=-1, eps=1e-5):
    m = x.mean(dim=axis, keepdim=True)
    v = x.var(dim=axis, keepdim=True, unbiased=False)
    return (x - m) / torch.sqrt(v + eps)
def _scatter_add(tgt, idx, src, axis=0):
    return tgt.clone().index_add_(axis, idx, src)
def _segment_sum(src, seg, n):
    out = torch.zeros((n,) + src.shape[1:], dtype=src.dtype)
    return out.index_add_(0, seg, src)
def _pad(x, pw):
    flat = tuple(v for pair in reversed(pw) for v in pair)
    return torch.nn.functional.pad(x, flat)

kernel_name = 'interleave_layers__spectral_gfnet__mixer_channel_mlp__prompt'

def setup_inputs():

    _w = (lambda a: (lambda t: t.to(torch.bfloat16) if t.is_floating_point() else t)(torch.as_tensor(a)))
    def _A_setup():
        B, S, D = 1, 2048, 640
        Df = D // 2 + 1
        return {
            "x": _w((lambda *s: torch.randn(*s, dtype=torch.bfloat16) * 0.05)(B, S, D)),
            "w_r": _w((lambda *s: torch.randn(*s, dtype=torch.bfloat16) * 0.8)(S, Df) * 0.02),
            "w_i": _w((lambda *s: torch.randn(*s, dtype=torch.bfloat16) * 0.8)(S, Df) * 0.02),
        }
    _A_in = _A_setup()

    def _B_setup():
        B, S, D, F = 1, 2048, 640, 1728
        return {
            "x": _w((lambda *s: torch.randn(*s, dtype=torch.bfloat16) * 0.05)(B, S, D)),
            "w1": _w((lambda *s: torch.randn(*s, dtype=torch.bfloat16) * 0.8)(D, F) * (D ** -0.5)),
            "b1": _w((lambda *s: torch.randn(*s, dtype=torch.bfloat16) * 0.4)(F)),
            "w2": _w((lambda *s: torch.randn(*s, dtype=torch.bfloat16) * 0.05)(F, D) * (F ** -0.5)),
            "b2": _w((lambda *s: torch.randn(*s, dtype=torch.bfloat16) * 0.05)(D)),
        }
    _B_in = _B_setup()

    return {
        "A_x": _A_in["x"],
        "A_w_r": _A_in["w_r"],
        "A_w_i": _A_in["w_i"],
        "B_x": _B_in["x"],
        "B_w1": _B_in["w1"],
        "B_b1": _B_in["b1"],
        "B_w2": _B_in["w2"],
        "B_b2": _B_in["b2"],
    }

def reference(A_x, A_w_r, A_w_i, B_x, B_w1, B_b1, B_w2, B_b2):
    def _A_ref(x, w_r, w_i):
        B, S, D = x.shape
        X = (lambda x, axes=(-2,-1): torch.fft.rfft2(x.float(), dim=axes))(x, axes=(-2, -1))
        W = w_r + 1j * w_i
        X = X * W
        y = (lambda x, s, axes=(-2,-1): torch.fft.irfft2(x, s=s, dim=axes).to(torch.bfloat16))(X, (S, D), axes=(-2, -1))
        return y

    def _B_ref(x, w1, b1, w2, b2):
        y = _layernorm(x, axis=-1)
        h = _gelu(torch.einsum("bsd,df->bsf", y, w1) + b1)
        return torch.einsum("bsf,fd->bsd", h, w2) + b2

    x = A_x
    x = x + _A_ref(_layernorm(x, axis=-1), A_w_r, A_w_i)
    x = x + _B_ref(_layernorm(x, axis=-1), B_w1, B_b1, B_w2, B_b2)
    x = x + _A_ref(_layernorm(x, axis=-1), A_w_r, A_w_i)
    x = x + _B_ref(_layernorm(x, axis=-1), B_w1, B_b1, B_w2, B_b2)
    return x
```

```cpp
#include <hip/hip_runtime.h>
#include <stdint.h>

typedef __bf16 bf16x2 __attribute__((ext_vector_type(2)));
typedef __bf16 bf16x4 __attribute__((ext_vector_type(4)));
typedef __bf16 bf16x8 __attribute__((ext_vector_type(8)));
typedef float f32x2 __attribute__((ext_vector_type(2)));
typedef float f32x4 __attribute__((ext_vector_type(4)));
typedef unsigned u32;
typedef unsigned u32x2 __attribute__((ext_vector_type(2)));
typedef unsigned u32x4 __attribute__((ext_vector_type(4)));
typedef unsigned short u16;
#define LAS __attribute__((address_space(3)))
#define DI __device__ __forceinline__

constexpr int S_ = 2048, D_ = 640, F_ = 1728, DF_ = 321;
constexpr int KS1 = 20;
constexpr int KS2 = 54;
constexpr float EPS = 1e-5f;

constexpr size_t XT_B = (size_t)80 * 2048 * 16;
constexpr size_t H_B = (size_t)128 * KS2 * 1024;
constexpr size_t A0_B = (size_t)16 * KS1 * 7 * 1024;
constexpr size_t A1_B = (size_t)16 * KS1 * 4 * 1024;
constexpr size_t W2F_B = (size_t)8 * KS2 * 5 * 1024;
constexpr size_t WT_B = (size_t)320 * 8192;
constexpr size_t X3S_B = (size_t)40 * 128 * 512;
constexpr size_t OFF_X0 = 0, OFF_X1 = OFF_X0 + XT_B, OFF_X2 = OFF_X1 + XT_B, OFF_X3 = OFF_X2 + XT_B;
constexpr size_t OFF_H1 = OFF_X3 + XT_B, OFF_H2 = OFF_H1 + H_B;
constexpr size_t OFF_A0 = OFF_H2 + H_B, OFF_A1 = OFF_A0 + A0_B;
constexpr size_t OFF_W2F = OFF_A1 + A1_B, OFF_W2T = OFF_W2F + W2F_B;
constexpr size_t OFF_WT = OFF_W2T + W2F_B;
constexpr size_t OFF_X3S = OFF_WT + WT_B;
constexpr size_t OFF_RSTD0 = OFF_X3S + X3S_B;
constexpr size_t OFF_SS2 = OFF_RSTD0 + 8192;
constexpr size_t OFF_B2F = OFF_SS2 + 8192;
constexpr size_t OFF_TAB = OFF_B2F + 4096;
constexpr size_t WS_END = OFF_TAB + 20480;

DI u32 pk2(float a, float b) { f32x2 v = {a, b}; bf16x2 r = __builtin_convertvector(v, bf16x2); return __builtin_bit_cast(u32, r); }

DI float bflo(u32 w) { return __builtin_bit_cast(float, w << 16); }
DI float bfhi(u32 w) { return __builtin_bit_cast(float, w & 0xffff0000u); }
DI float rbf(float x) { return bflo(pk2(x, x)); }
DI f32x4 mfma(u32x4 a, u32x4 b, f32x4 c) {
  return __builtin_amdgcn_mfma_f32_16x16x32_bf16(__builtin_bit_cast(bf16x8, a), __builtin_bit_cast(bf16x8, b), c, 0, 0, 0);
}
DI float dot2(u32 a, u32 b, float c) { return __builtin_amdgcn_fdot2_f32_bf16(__builtin_bit_cast(bf16x2, a), __builtin_bit_cast(bf16x2, b), c, false); }
DI float cos2pi(float x) { return __builtin_amdgcn_cosf(x); }
DI float sin2pi(float x) { return __builtin_amdgcn_sinf(x); }
DI int s_of_p(int p) {
  const int r = p >> 9, i = (p >> 7) & 3, g = (p >> 5) & 3, uu = (p >> 4) & 1, c16 = p & 15;
  return 64 * (16 * uu + 4 * g + r) + 16 * i + c16;
}
DI void glds16(const void* g, LAS void* l) { __builtin_amdgcn_global_load_lds(g, l, 16, 0, 0); }
#define WAIT_VM0() asm volatile("s_waitcnt vmcnt(0)" ::: "memory")
#define WAIT_LG0() asm volatile("s_waitcnt lgkmcnt(0)" ::: "memory")
#define BAR() __builtin_amdgcn_s_barrier()
DI float ln_den(float var) { return rbf(__builtin_amdgcn_sqrtf(rbf(rbf(var) + EPS))); }
DI float rcp(float x) { return __builtin_amdgcn_rcpf(x); }

typedef int i32x4 __attribute__((ext_vector_type(4)));
DI i32x4 mk_rsrc(const void* p, u32 bytes) {
  const unsigned long long a = (unsigned long long)p;
  return i32x4{(int)__builtin_amdgcn_readfirstlane((u32)a), (int)__builtin_amdgcn_readfirstlane((u32)(a >> 32) & 0xffffu), (int)bytes, 0x00020000};
}
#define RFL(x) __builtin_amdgcn_readfirstlane(x)
template <int IMM> DI void dma16(i32x4 rsrc, u32 voff, u32 soff, u32 lds) {
  asm volatile("s_mov_b32 m0, %3\n\ts_nop 0\n\tbuffer_load_dwordx4 %0, %1, %2 offen offset:%4 lds" ::"v"(voff), "s"(rsrc), "s"(RFL(soff)), "s"(RFL(lds)), "i"(IMM) : "memory");
}
template <int IMM> DI void bld16(u32x4& dst, i32x4 rsrc, u32 voff, u32 soff) {
  asm volatile("buffer_load_dwordx4 %0, %1, %2, %3 offen offset:%4" : "=v"(dst) : "v"(voff), "s"(rsrc), "s"(RFL(soff)), "i"(IMM) : "memory");
}
DI void bldu16(u32& dst, i32x4 rsrc, u32 voff, u32 soff) {
  asm volatile("buffer_load_ushort %0, %1, %2, %3 offen" : "=v"(dst) : "v"(voff), "s"(rsrc), "s"(RFL(soff)) : "memory");
}
DI void bld8(u32x2& dst, i32x4 rsrc, u32 voff, u32 soff) {
  asm volatile("buffer_load_dwordx2 %0, %1, %2, %3 offen" : "=v"(dst) : "v"(voff), "s"(rsrc), "s"(RFL(soff)) : "memory");
}
DI void bld4(u32& dst, i32x4 rsrc, u32 voff, u32 soff) {
  asm volatile("buffer_load_dword %0, %1, %2, %3 offen" : "=v"(dst) : "v"(voff), "s"(rsrc), "s"(RFL(soff)) : "memory");
}
#define DMA16(rsrc, voff, soff, lds, IMM) dma16<IMM>(rsrc, voff, soff, lds)
#define BLD16(dst, rsrc, voff, soff, IMM) bld16<IMM>(dst, rsrc, voff, soff)
template <int N> DI void wait_vm_bar8(u32x4& a, u32x4& b, u32x4& c, u32x4& d, u32x4& e, u32x4& f, u32x4& g, u32x4& h) {
  asm volatile("s_waitcnt vmcnt(%8)\n\ts_barrier" : "+v"(a), "+v"(b), "+v"(c), "+v"(d), "+v"(e), "+v"(f), "+v"(g), "+v"(h) : "i"(N) : "memory");
}
#define WAIT_VM(N) asm volatile("s_waitcnt vmcnt(" #N ")" ::: "memory")
DI float red_g(float x) {
  const u32 xi = __builtin_bit_cast(u32, x);
  auto a = __builtin_amdgcn_permlane32_swap(xi, xi, false, false);
  const float y = __builtin_bit_cast(float, (u32)a[0]) + __builtin_bit_cast(float, (u32)a[1]);
  const u32 yi = __builtin_bit_cast(u32, y);
  auto c = __builtin_amdgcn_permlane16_swap(yi, yi, false, false);
  return __builtin_bit_cast(float, (u32)c[0]) + __builtin_bit_cast(float, (u32)c[1]);
}
DI u32 lds_addr(const void* p) { return (u32)(uintptr_t)(LAS const char*)p; }

template <int I> struct IC { static constexpr int value = I; constexpr operator int() const { return I; } };
template <int B, int E, class F> DI void static_for(F&& f) { if constexpr (B < E) { f(IC<B>{}); static_for<B + 1, E>(f); } }
#define SB() __builtin_amdgcn_sched_barrier(0)

struct Params {
  const u16 *x, *wr, *wi, *w1, *b1, *w2, *b2;
  u16* out;
  char* ws;
};

#define STAMP(kid, i)
#define RSTAMP(kid, i)

struct QGen { float v0[8], v1[8], k; };
DI void gen_q_init(QGen& G, int ms, int lane) {
  const int c = ms * 16 + (lane & 15), g = lane >> 4;
  int jj = c >> 1; float amp = 0.05590169943749474f; float ph = (c & 1) ? 0.25f : 0.f;
  if (c < 2) { jj = (c == 0) ? 320 : 0; amp = 0.03952847075210474f; ph = 0.f; }
  const float fj = (float)jj * (1.f / 640.f);
  const float kj = 2.f * cos2pi(fj), d = (float)(8 * g);
  G.v0[0] = amp * cos2pi(__builtin_amdgcn_fractf(fj * d) + ph);          G.v0[1] = amp * cos2pi(__builtin_amdgcn_fractf(fj * (d + 1.f)) + ph);
  G.v1[0] = amp * cos2pi(__builtin_amdgcn_fractf(fj * (d + 32.f)) + ph); G.v1[1] = amp * cos2pi(__builtin_amdgcn_fractf(fj * (d + 33.f)) + ph);
#pragma unroll
  for (int j = 2; j < 8; ++j) { G.v0[j] = kj * G.v0[j - 1] - G.v0[j - 2]; G.v1[j] = kj * G.v1[j - 1] - G.v1[j - 2]; }
  G.k = 2.f * cos2pi(__builtin_amdgcn_fractf((float)jj * 0.05f));
}
DI u32x4 gen_q_step(QGen& G) {
  const u32x4 r = u32x4{pk2(G.v0[0], G.v0[1]), pk2(G.v0[2], G.v0[3]), pk2(G.v0[4], G.v0[5]), pk2(G.v0[6], G.v0[7])};
#pragma unroll
  for (int j = 0; j < 8; ++j) { const float n = G.k * G.v1[j] - G.v0[j]; G.v0[j] = G.v1[j]; G.v1[j] = n; }
  return r;
}

struct JobOps { i32x4 rs; u32 vo[10]; u32 l0; };
DI void job_ops(JobOps& J, const Params& P, int job, int w8, int lane, u32 lbuf) {
  asm volatile("" : "+v"(lane), "+s"(job), "+s"(w8));
  if (job >= 32 && job < 59) {
    const int d = 80 * w8 + (lane >> 3), pos = lane & 7;
    J.rs = mk_rsrc(P.w1, D_ * F_ * 2);
    const u32 va = (u32)d * (F_ * 2) + (u32)(job - 32) * 128u + 16u * (u32)(pos ^ (((d >> 1) & 1) << 1));
#pragma unroll
    for (int k = 0; k < 10; ++k) J.vo[k] = ((k & 1) ? (va ^ 64u) : va) + (u32)k * (8 * F_ * 2);
    J.l0 = lbuf + (u32)w8 * 10240u;
  } else {
    const int nn = w8 >> 1, hh = w8 & 1;
    u32 s0;
    if (job < 32) { J.rs = mk_rsrc(P.x, S_ * D_ * 2); s0 = (u32)s_of_p(job * 64 + nn * 16) * (D_ * 2); }
    else if (job < 86) { J.rs = mk_rsrc(P.w2, F_ * D_ * 2); s0 = (u32)((job - 59) * 64 + nn * 16) * (D_ * 2); }
    else { J.rs = mk_rsrc(P.b2, D_ * 2); s0 = 0; }
    J.l0 = lbuf + (u32)nn * 20480u + (u32)hh * 10240u;
    const u32 va = (u32)lane * 16u;
    if (hh == 0) {
#pragma unroll
      for (int k = 0; k < 10; ++k) J.vo[k] = (va ^ (u32)(k * 16)) + (s0 + k * 1280u);
    } else {
      const u32 vb = (u32)(lane >> 4) * 1280u + 1024u + (((u32)(lane & 15) ^ (u32)(lane >> 4)) * 16u);
#pragma unroll
      for (int k = 0; k < 6; ++k) J.vo[k] = (va ^ (u32)((10 + k) * 16)) + (s0 + (10 + k) * 1280u);
#pragma unroll
      for (int k = 6; k < 10; ++k) J.vo[k] = (vb ^ (u32)((k - 6) * 64)) + (s0 + (k - 6) * 5120u);
    }
  }
}
template <int K> DI void job_op(const JobOps& J) {
  if constexpr (K == 0) asm volatile("s_mov_b32 m0, %2\n\ts_nop 0\n\tbuffer_load_dwordx4 %0, %1, 0 offen lds\n\ts_add_u32 m0, m0, 0x400" ::"v"(J.vo[0]), "s"(J.rs), "s"(RFL(J.l0)) : "memory", "scc");
  else asm volatile("buffer_load_dwordx4 %0, %1, 0 offen lds\n\ts_add_u32 m0, m0, 0x400" ::"v"(J.vo[K]), "s"(J.rs) : "memory", "scc");
}

DI u32x4 ident_frag(int par, int lane) {
  const int j = 16 * par + (lane & 15) - 8 * (lane >> 4);
  u32x4 r;
#pragma unroll
  for (int k = 0; k < 4; ++k) r[k] = (j == 2 * k ? 0x3F80u : 0u) | (j == 2 * k + 1 ? 0x3F800000u : 0u);
  return r;
}

template <int MODE, class HK>
DI void qloop_lin(const char* buf, int sl, int lane, const u32x4 (&q)[KS1], f32x4 (&acc)[2], f32x4& sum, f32x4& gram, HK&& hook) {
  u32x4 bf[4][2];
  const int row = lane & 15, g = lane >> 4;
  const char* ba[4];
#pragma unroll
  for (int j = 0; j < 4; ++j) ba[j] = buf + row * 1024 + (((4 * j + g) ^ row) * 16);
  const int dB = 16384 - row * 768;
  auto rd = [&](auto T, auto N) { constexpr int t = T, n = N;
    if constexpr (t < 16) bf[t % 4][n] = *(const u32x4*)(ba[t & 3] + n * 20480 + (t >> 2) * 256);
    else if constexpr (t < KS1) bf[t % 4][n] = *(const u32x4*)(ba[t & 3] + dB + n * 20480); };
  static_for<0, 3>([&](auto T) { rd(T, IC<0>{}); rd(T, IC<1>{}); });
  static_for<0, KS1>([&](auto T) {
    constexpr int t = T;
    __builtin_amdgcn_s_waitcnt(0xC07F | ((t + 2 < KS1 ? 4 : (t + 1 < KS1 ? 2 : 0)) << 8));
    SB();
    static_for<0, 2>([&](auto N) {
      constexpr int n = N;
      if constexpr (MODE == 2) acc[n] = mfma(bf[t % 4][n], q[t], acc[n]); else acc[n] = mfma(q[t], bf[t % 4][n], acc[n]);
      SB();
      rd(IC<t + 3>{}, N);
      SB();
    });
    if constexpr (MODE == 1) {
      const u32x4 e = sl ? bf[t % 4][1] : bf[t % 4][0];
      sum = mfma(e, u32x4{0x3F803F80u, 0x3F803F80u, 0x3F803F80u, 0x3F803F80u}, sum);
      gram = mfma(e, e, gram);
      SB();
    }
    hook(T);
    SB();
  });
}
template <class HK>
DI void qloop_tr(const char* buf, int nh, int lane, const u32x4 (&q)[KS1], f32x4 (&acc)[2], HK&& hook) {
  const int g = lane >> 4, pp = lane & 3, qq = (lane & 15) >> 2;
  const int sw = (((qq >> 1) & 1) | ((g & 1) << 1)) << 1;
  const char* base[2];
#pragma unroll
  for (int n = 0; n < 2; ++n) base[n] = buf + (8 * g + qq) * 128 + ((2 * (2 * nh + n) + (pp >> 1)) ^ sw) * 16 + (pp & 1) * 8;
  u32x2 lo[4][2], hi[4][2];
  auto rd = [&](auto T, auto N) { constexpr int t = T, n = N;
    if constexpr (t < KS1) {
      lo[t % 4][n] = __builtin_bit_cast(u32x2, __builtin_amdgcn_ds_read_tr16_b64_v4bf16((LAS bf16x4*)(base[n] + t * 4096)));
      hi[t % 4][n] = __builtin_bit_cast(u32x2, __builtin_amdgcn_ds_read_tr16_b64_v4bf16((LAS bf16x4*)(base[n] + t * 4096 + 512)));
    } };
  static_for<0, 3>([&](auto T) { rd(T, IC<0>{}); rd(T, IC<1>{}); });
  static_for<0, KS1>([&](auto T) {
    constexpr int t = T;
    __builtin_amdgcn_s_waitcnt(0xC07F | ((t + 2 < KS1 ? 8 : (t + 1 < KS1 ? 4 : 0)) << 8));
    SB();
    static_for<0, 2>([&](auto N) {
      constexpr int n = N;
      acc[n] = mfma(q[t], u32x4{lo[t % 4][n][0], lo[t % 4][n][1], hi[t % 4][n][0], hi[t % 4][n][1]}, acc[n]);
      SB();
      rd(IC<t + 3>{}, N);
      SB();
    });
    hook(T);
    SB();
  });
}

DI u32x4 row_frag(const char* buf, int n, int t, int lane) {
  const int row = lane & 15, g = lane >> 4, x = (((4 * (t & 3) + g) ^ row) * 16);
  return *(const u32x4*)(buf + n * 20480 + (t < 16 ? row * 1024 + (t >> 2) * 256 : 16384 + row * 256) + x);
}
typedef __attribute__((ext_vector_type(2))) unsigned int v2u_t;
DI void bst8(__amdgpu_buffer_rsrc_t rs, u32 voff, u32 soff, const f32x4& a) {
  __builtin_amdgcn_raw_buffer_store_b64(v2u_t{pk2(a[0], a[1]), pk2(a[2], a[3])}, rs, voff, soff, 0);
}
template <class HK>
DI void qjob_compute(const Params& P, __amdgpu_buffer_rsrc_t rsw, u32 vX, u32 vW, int job, int ct10, int m_, int nh, int lane, const char* buf,
                     const u32x4 (&q)[KS1], HK&& hook) {
  asm volatile("" : "+s"(job), "+s"(ct10), "+s"(m_), "+s"(nh));
  const int ms = ct10 * 4 + m_;
  f32x4 acc[2], sum = {0.f, 0.f, 0.f, 0.f}, gram = sum;
  acc[0] = sum; acc[1] = sum;
  const char* bl = buf + nh * 40960;
  if (job < 32) {
    if (ct10 < 4 && m_ == 3 && nh == (ct10 >> 1)) {
      qloop_lin<1>(bl, ct10 & 1, lane, q, acc, sum, gram, hook);
      const int c16 = lane & 15, g = lane >> 4, r = c16 & 3;
      const float s1 = r == 0 ? sum[0] : r == 1 ? sum[1] : r == 2 ? sum[2] : sum[3];
      const float s2 = r == 0 ? gram[0] : r == 1 ? gram[1] : r == 2 ? gram[2] : gram[3];
      const float m = s1 * (1.f / 640.f);
      float var = s2 * (1.f / 640.f) - m * m; var = var < 0.f ? 0.f : var;
      if (g == (c16 >> 2)) ((float*)(P.ws + OFF_RSTD0))[job * 64 + ct10 * 16 + c16] = rcp(ln_den(var));
    } else {
      qloop_lin<0>(bl, 0, lane, q, acc, sum, gram, hook);
    }
    const u32 so = (u32)OFF_X0 + (u32)(2 * ms * 2048 + job * 64 + nh * 32) * 16u;
    bst8(rsw, vX, so, acc[0]); bst8(rsw, vX, so + 256u, acc[1]);
  } else if (job < 59) {
    qloop_tr(buf, nh, lane, q, acc, hook);
#pragma unroll
    for (int n = 0; n < 2; ++n) {
      const int fs = (job - 32) * 4 + 2 * nh + n;
      if (ms == 0 && lane < 16) acc[n][1] = 0.f;
      const int ft = fs < 84 ? fs / 7 : 12 + (fs - 84) / 6, m = fs < 84 ? fs - 7 * ft : (fs - 84) - 6 * (ft - 12);
      bst8(rsw, vW, (u32)OFF_A0 + (u32)((ft * KS1 + (ms >> 1)) * 7 + m) * 1024u + (u32)(ms & 1) * 512u, acc[n]);
    }
  } else if (job < 86) {
    qloop_lin<2>(bl, 0, lane, q, acc, sum, gram, hook);
    const u32x4 id = ident_frag(ms & 1, lane);
    const int ct = ms / 5;
#pragma unroll
    for (int n = 0; n < 2; ++n) {
      const u32x4 b = row_frag(bl, n, ms >> 1, lane);
      const f32x4 tr = mfma(b, id, f32x4{0.f, 0.f, 0.f, 0.f});
      const int fs = (job - 59) * 4 + 2 * nh + n;
      const u32 o = (u32)((ct * KS2 + (fs >> 1)) * 5 + (ms - 5 * ct)) * 1024u + (u32)(fs & 1) * 512u;
      bst8(rsw, vW, (u32)OFF_W2F + o, acc[n]);
      bst8(rsw, vW, (u32)OFF_W2T + o, tr);
    }
  } else if (job == 86) {
    static_for<0, 10>([&](auto T) { hook(T); });
    if (nh == 0) {
#pragma unroll
      for (int t = 0; t < KS1; ++t) acc[0] = mfma(row_frag(buf, 0, t, lane), q[t], acc[0]);
      if (lane < 16) ((float*)(P.ws + OFF_B2F))[ms * 16 + lane] = acc[0][0];
    }
  } else {
    static_for<0, 10>([&](auto T) { hook(T); });
    const u32x4 id = ident_frag(nh, lane);
    static_for<0, 20>([&](auto T) {
      constexpr int t = T;
      const f32x4 dd = mfma(q[t], id, f32x4{0.f, 0.f, 0.f, 0.f});
      const u32 fm = t < 12 ? (u32)(t * KS1 * 4) + (u32)nh : (u32)((12 + (t - 12) / 2) * KS1 * 4 + ((t - 12) & 1) * 2) + (u32)nh;
      bst8(rsw, vW, (u32)OFF_A1 + (fm + (u32)(ms >> 1) * 4u) * 1024u + (u32)(ms & 1) * 512u, dd);
    });
  }
}

template <int KID>
DI void prep_jobs(const Params& P, char* smem, int ct10, int j0, int js, int nj, int WT, int tid, int lane, int w8) {
  const int m_ = w8 & 3, nh = w8 >> 2;
  const u32 lb = lds_addr(smem);
  JobOps J;
  job_ops(J, P, j0, w8, lane, lb);
  static_for<0, 4>([&](auto K) { job_op<K>(J); asm volatile("s_nop 0"); });
  const int wa = WT & 15, wcg = (WT >= 0) ? (WT >> 4) : 0;
  u32x2 wv[2][2];
  u32 w0r = 0, w0i = 0;
  u32x4 q[KS1];
  {
    QGen G;
    gen_q_init(G, ct10 * 4 + m_, lane);
    static_for<0, 6>([&](auto T) { constexpr int t = T; q[t] = gen_q_step(G); SB(); job_op<4 + t>(J); SB(); });
    {
      const i32x4 rwr = mk_rsrc(P.wr, S_ * DF_ * 2), rwi = mk_rsrc(P.wi, S_ * DF_ * 2);
      const u32 wvo = (u32)(lane >> 3) * (DF_ * 2) + (u32)(lane & 7) * 8u + (u32)wcg * 64u;
      const u32 wso = (u32)((16 * nh + 32 * (4 * wa + m_)) * DF_ * 2);
      bld8(wv[0][0], rwr, wvo, wso); bld8(wv[0][1], rwi, wvo, wso);
      bld8(wv[1][0], rwr, wvo, wso + 8 * DF_ * 2); bld8(wv[1][1], rwi, wvo, wso + 8 * DF_ * 2);
      bldu16(w0r, rwr, (u32)(lane & 15) * (DF_ * 2) + 640u, wso); bldu16(w0i, rwi, (u32)(lane & 15) * (DF_ * 2) + 640u, wso);
    }
    job_ops(J, P, j0 + js, w8, lane, lb + 81920);
    SB();
    static_for<6, KS1>([&](auto T) { constexpr int t = T; q[t] = gen_q_step(G); SB(); if constexpr (t < 16) job_op<t - 6>(J); SB(); });
#pragma unroll
    for (int t = 0; t < KS1; ++t) asm volatile("" : "+v"(q[t]));
  }
  const __amdgpu_buffer_rsrc_t rsw = __builtin_amdgcn_make_buffer_rsrc((void*)P.ws, 0, (int)WS_END, 0x00020000);
  const u32 vX = (u32)(lane >> 5) * 32768u + (u32)((lane >> 4) & 1) * 8u + (u32)(lane & 15) * 16u;
  const u32 vW = ((u32)(lane >> 5) * 16u + (u32)(lane & 15)) * 16u + (u32)((lane >> 4) & 1) * 8u;
  STAMP(KID, 1);
  for (int jj = 0; jj < nj; ++jj) {
    const int job = j0 + jj * js;
    char* buf = smem + (jj & 1) * 81920;
    if (jj == 0) asm volatile("s_waitcnt vmcnt(10)\n\ts_barrier" ::: "memory");
    else asm volatile("s_waitcnt vmcnt(2)\n\ts_barrier" ::: "memory");
    if (jj == 0) {
      asm volatile("" : "+v"(wv[0][0]), "+v"(wv[0][1]), "+v"(wv[1][0]), "+v"(wv[1][1]), "+v"(w0r), "+v"(w0i) :: "memory");
      STAMP(KID, 2);
    }
    if (jj == 1) STAMP(KID, 11);
    if (jj == 2) STAMP(KID, 12);
    if (jj == 3) STAMP(KID, 13);
    const bool pf = (jj >= 1 && jj + 1 < nj);
    if (pf) job_ops(J, P, job + js, w8, lane, lb + ((jj + 1) & 1) * 81920);
    qjob_compute(P, rsw, vX, vW, job, ct10, m_, nh, lane, buf, q, [&](auto T) { constexpr int t = T; if constexpr (t < 10) { if (pf) job_op<t>(J); } });
    if (jj == 0) STAMP(KID, 3);
    if (jj == 0) {
      asm volatile("s_waitcnt lgkmcnt(0)\n\ts_barrier" ::: "memory");
      if (WT >= 0) {
        u32* st = (u32*)buf;
#pragma unroll
        for (int i = 0; i < 2; ++i) {
          const u32 r0 = wv[i][0][0], r1 = wv[i][0][1], i0 = wv[i][1][0], i1 = wv[i][1][1];
          u32* d = st + (4 * (lane & 7)) * 132 + (16 * nh + 8 * i + (lane >> 3)) * 4 + m_;
          d[0] = (r0 & 0xffffu) | (i0 << 16); d[132] = (r0 >> 16) | (i0 & 0xffff0000u);
          d[264] = (r1 & 0xffffu) | (i1 << 16); d[396] = (r1 >> 16) | (i1 & 0xffff0000u);
        }
        if (wcg == 0) {
          asm volatile("s_waitcnt lgkmcnt(0)\n\ts_barrier" ::: "memory");
          if (lane < 16) st[(16 * nh + lane) * 4 + m_] = w0r | (w0i << 16);
        }
        asm volatile("s_waitcnt lgkmcnt(0)\n\ts_barrier" ::: "memory");
        const int v = wa >> 3, ii = (wa >> 2) & 1, g = wa & 3;
#pragma unroll
        for (int k = 0; k < 2; ++k) {
          const int it = k * 512 + tid, cl = it >> 5, k1 = it & 31;
          const u32x4 o = *(const u32x4*)(st + cl * 132 + k1 * 4);
          *(u32x4*)(P.ws + OFF_WT + ((((size_t)(32 * wcg + cl) * 4 + (k1 >> 4) * 2 + v) * 2 + ii) * 64 + g * 16 + (k1 & 15)) * 16) = o;
        }
        asm volatile("s_waitcnt lgkmcnt(0)" ::: "memory");
      }
      STAMP(KID, 4);
    }
    if (jj == 1) STAMP(KID, 5);
    if (jj == 2) STAMP(KID, 6);
  }
}

__global__ void __launch_bounds__(512, 2) k0_prep(Params P) {
  extern __shared__ __attribute__((aligned(16))) char smem[];
  const int b = blockIdx.x, tid = threadIdx.x, lane = tid & 63, w = __builtin_amdgcn_readfirstlane(tid >> 6);
  RSTAMP(0, 14); STAMP(0, 0);
  if (b < 240) {
    const int li = b >> 3;
    const int sg = (b & 7) * 3 + li / 10;
    prep_jobs<0>(P, smem, li % 10, sg, 24, sg + 72 < 88 ? 4 : 3, b < 160 ? b : -1, tid, lane, w);
    STAMP(0, 7); RSTAMP(0, 15);
  } else if (b == 240) {
    const int c16 = lane & 15, g = lane >> 4;
    for (int fr = w; fr < 20; fr += 8) {
      float v[8];
      if (fr < 16) {
        const int tile = fr >> 2, t = (fr >> 1) & 1, part = fr & 1;
#pragma unroll
        for (int j = 0; j < 8; ++j) {
          const int kk = 32 * t + 16 * (j >> 2) + 4 * g + (j & 3);
          const float a = (float)(((16 * tile + c16) * kk) & 63) * (1.f / 64.f);
          v[j] = part ? -sin2pi(a) : cos2pi(a);
        }
      } else {
        const int u = (fr - 16) >> 1, part = fr & 1;
#pragma unroll
        for (int j = 0; j < 8; ++j) {
          const int kk = 16 * (j >> 2) + 4 * g + (j & 3);
          const float a = (float)(((16 * u + c16) * kk) & 31) * (1.f / 32.f);
          v[j] = part ? -sin2pi(a) : cos2pi(a);
        }
      }
      *(u32x4*)(P.ws + OFF_TAB + fr * 1024 + lane * 16) = u32x4{pk2(v[0], v[1]), pk2(v[2], v[3]), pk2(v[4], v[5]), pk2(v[6], v[7])};
    }
  } else if (b == 241) {
    for (int i = tid; i < 2048; i += 512) ((float*)(P.ws + OFF_SS2))[i] = 0.f;
  }
}

template <int LAYER>
__global__ void __launch_bounds__(512, 2) k_col(Params P) {
  extern __shared__ __attribute__((aligned(16))) char smem[];
  const int b = blockIdx.x, tid = threadIdx.x, lane = tid & 63, w = __builtin_amdgcn_readfirstlane(tid >> 6);
  char* ws = P.ws;
  constexpr int KID = LAYER == 0 ? 1 : 4;
  RSTAMP(KID, 14); STAMP(KID, 0);
  const int oct = (b & 7) + 8 * (b >> 4), hf = (b >> 3) & 1;
  char* sA = smem;
  char* sT = smem + 16384;
  char* sE = smem + 36864;
  char* sG = smem + 53248;
  char* sY = smem + 69632;
  const int cc = w >> 2, u = (w >> 1) & 1, v = w & 1, c16 = lane & 15, g = lane >> 4;
  const int col = oct * 4 + hf * 2 + cc;
  const i32x4 rsw = mk_rsrc(ws, (u32)WS_END);
  const u32 l16 = lane * 16;
  dma16<0>(rsw, l16, (u32)OFF_TAB + w * 1024u, lds_addr(sT) + w * 1024u);
  dma16<0>(rsw, l16, (u32)OFF_TAB + (8 + w) * 1024u, lds_addr(sT) + (8 + w) * 1024u);
  dma16<0>(rsw, l16, (u32)OFF_TAB + (16 + (w & 3)) * 1024u, lds_addr(sT) + (16 + (w & 3)) * 1024u);
  u32x2 raw[4]; u32 st4[4]; u32x4 wv[2];
  const u32 xo = (u32)(LAYER == 0 ? OFF_X0 : OFF_X2) + (u32)oct * 32768u, xv = (u32)tid * 16u + (u32)hf * 8u;
#pragma unroll
  for (int r = 0; r < 4; ++r) bld8(raw[r], rsw, xv, xo + r * 8192u);
#pragma unroll
  for (int r = 0; r < 4; ++r) bld4(st4[r], rsw, (u32)tid * 4u, (u32)(LAYER == 0 ? OFF_RSTD0 : OFF_SS2) + r * 2048u);
  {
    const u32 wo = (u32)OFF_WT + (u32)((col * 4 + u * 2 + v) * 2) * 1024u;
    bld16<0>(wv[0], rsw, l16, wo);
    bld16<1024>(wv[1], rsw, l16, wo);
  }
  SB();
  float twc[16], tws[16], itc[8], its[8];
  {
    const float fk = (float)(16 * u + c16) * (1.f / 2048.f);
#pragma unroll
    for (int i = 0; i < 4; ++i)
#pragma unroll
      for (int r = 0; r < 4; ++r) {
        const float a = (float)(16 * i + 4 * g + r) * fk;
        twc[i * 4 + r] = cos2pi(a); tws[i * 4 + r] = sin2pi(a);
      }
#pragma unroll
    for (int ii = 0; ii < 2; ++ii) {
      const float fm = (float)(16 * (2 * v + ii) + c16) * (1.f / 2048.f);
#pragma unroll
      for (int r = 0; r < 4; ++r) {
        const float a = (float)(16 * u + 4 * g + r) * fm;
        itc[ii * 4 + r] = cos2pi(a) * (1.f / 2048.f); its[ii * 4 + r] = sin2pi(a) * (1.f / 2048.f);
      }
    }
#pragma unroll
    for (int k = 0; k < 16; ++k) asm volatile("" : "+v"(twc[k]), "+v"(tws[k]));
#pragma unroll
    for (int k = 0; k < 8; ++k) asm volatile("" : "+v"(itc[k]), "+v"(its[k]));
  }
  STAMP(KID, 1);
  asm volatile("s_waitcnt vmcnt(10)\n\ts_barrier" ::: "memory");
  u32x4 f64r[2][2], f64i[2][2], f64n[2][2], f32r, f32i, f32n;
#pragma unroll
  for (int ii = 0; ii < 2; ++ii)
#pragma unroll
    for (int t = 0; t < 2; ++t) {
      f64r[ii][t] = *(const u32x4*)(sT + (((2 * v + ii) * 2 + t) * 2 + 0) * 1024 + l16);
      f64i[ii][t] = *(const u32x4*)(sT + (((2 * v + ii) * 2 + t) * 2 + 1) * 1024 + l16);
      f64n[ii][t] = f64i[ii][t] ^ 0x80008000u;
    }
  f32r = *(const u32x4*)(sT + (16 + u * 2 + 0) * 1024 + l16);
  f32i = *(const u32x4*)(sT + (16 + u * 2 + 1) * 1024 + l16);
  f32n = f32i ^ 0x80008000u;
#pragma unroll
  for (int ii = 0; ii < 2; ++ii)
#pragma unroll
    for (int t = 0; t < 2; ++t) asm volatile("" : "+v"(f64r[ii][t]), "+v"(f64i[ii][t]), "+v"(f64n[ii][t]));
  asm volatile("" : "+v"(f32r), "+v"(f32i), "+v"(f32n));
  STAMP(KID, 2);
  asm volatile("s_waitcnt vmcnt(2)" : "+v"(raw[0]), "+v"(raw[1]), "+v"(raw[2]), "+v"(raw[3]), "+v"(st4[0]), "+v"(st4[1]), "+v"(st4[2]), "+v"(st4[3]) :: "memory");
  const bool special = (oct == 0 && hf == 0);
  {
    float val[4][4];
#pragma unroll
    for (int r = 0; r < 4; ++r) {
      const float sv = __builtin_bit_cast(float, st4[r]);
      const float rs = (LAYER == 0) ? sv : __builtin_amdgcn_rcpf(ln_den(sv * (1.f / 640.f)));
      val[r][0] = bflo(raw[r][0]) * rs; val[r][1] = special ? 0.f : bfhi(raw[r][0]) * rs;
      val[r][2] = bflo(raw[r][1]) * rs; val[r][3] = bfhi(raw[r][1]) * rs;
    }
    const int ti = tid >> 7, tg = (tid >> 5) & 3, tu = (tid >> 4) & 1, tc = tid & 15;
#pragma unroll
    for (int k = 0; k < 4; ++k)
      *(u32x2*)(sA + (k * 4 + ti) * 1024 + (tg * 16 + tc) * 16 + tu * 8) = u32x2{pk2(val[0][k], val[1][k]), pk2(val[2][k], val[3][k])};
  }
  asm volatile("s_waitcnt lgkmcnt(0)\n\ts_barrier" ::: "memory");
  STAMP(KID, 3);
  const f32x4 Z4 = {0.f, 0.f, 0.f, 0.f};
  u32x4 yr[2], yi[2];
  {
    u32x4 xr[4], xi[4];
#pragma unroll
    for (int i = 0; i < 4; ++i) {
      xr[i] = *(const u32x4*)(sA + ((cc * 2 + 0) * 4 + i) * 1024 + l16);
      xi[i] = *(const u32x4*)(sA + ((cc * 2 + 1) * 4 + i) * 1024 + l16);
    }
    f32x4 dr[4], di[4];
#pragma unroll
    for (int i = 0; i < 4; ++i) { dr[i] = mfma(xr[i], f32r, Z4); di[i] = mfma(xr[i], f32i, Z4); }
#pragma unroll
    for (int i = 0; i < 4; ++i) { dr[i] = mfma(xi[i], f32n, dr[i]); di[i] = mfma(xi[i], f32r, di[i]); }
    float tr[16], tq[16];
#pragma unroll
    for (int i = 0; i < 4; ++i)
#pragma unroll
      for (int r = 0; r < 4; ++r) {
        tr[i * 4 + r] = dr[i][r] * twc[i * 4 + r] + di[i][r] * tws[i * 4 + r];
        tq[i * 4 + r] = di[i][r] * twc[i * 4 + r] - dr[i][r] * tws[i * 4 + r];
      }
#pragma unroll
    for (int t = 0; t < 2; ++t) {
      yr[t] = u32x4{pk2(tr[8 * t + 0], tr[8 * t + 1]), pk2(tr[8 * t + 2], tr[8 * t + 3]), pk2(tr[8 * t + 4], tr[8 * t + 5]), pk2(tr[8 * t + 6], tr[8 * t + 7])};
      yi[t] = u32x4{pk2(tq[8 * t + 0], tq[8 * t + 1]), pk2(tq[8 * t + 2], tq[8 * t + 3]), pk2(tq[8 * t + 4], tq[8 * t + 5]), pk2(tq[8 * t + 6], tq[8 * t + 7])};
    }
  }
  u32x4 zr[2], zi[2];
  {
    f32x4 dr[2] = {Z4, Z4}, di[2] = {Z4, Z4};
#pragma unroll
    for (int t = 0; t < 2; ++t) {
#pragma unroll
      for (int ii = 0; ii < 2; ++ii) { dr[ii] = mfma(f64r[ii][t], yr[t], dr[ii]); di[ii] = mfma(f64i[ii][t], yr[t], di[ii]); }
#pragma unroll
      for (int ii = 0; ii < 2; ++ii) { dr[ii] = mfma(f64n[ii][t], yi[t], dr[ii]); di[ii] = mfma(f64r[ii][t], yi[t], di[ii]); }
    }
    asm volatile("s_waitcnt vmcnt(0)" : "+v"(wv[0]), "+v"(wv[1]) :: "memory");
    float pr[8], pi[8];
#pragma unroll
    for (int ii = 0; ii < 2; ++ii)
#pragma unroll
      for (int r = 0; r < 4; ++r) {
        const u32 wd = wv[ii][r];
        const float a = bflo(wd), bb = bfhi(wd);
        pr[ii * 4 + r] = dr[ii][r] * a - di[ii][r] * bb;
        pi[ii * 4 + r] = dr[ii][r] * bb + di[ii][r] * a;
      }
    const u32x4 mr = u32x4{pk2(pr[0], pr[1]), pk2(pr[2], pr[3]), pk2(pr[4], pr[5]), pk2(pr[6], pr[7])};
    const u32x4 mi = u32x4{pk2(pi[0], pi[1]), pk2(pi[2], pi[3]), pk2(pi[4], pi[5]), pk2(pi[6], pi[7])};
    *(u32x4*)(sE + ((((cc * 2 + u) * 2 + v) * 2 + 0) * 1024) + l16) = mr;
    *(u32x4*)(sE + ((((cc * 2 + u) * 2 + v) * 2 + 1) * 1024) + l16) = mi;
    STAMP(KID, 4);
    asm volatile("s_waitcnt lgkmcnt(0)\n\ts_barrier" ::: "memory");
    const u32x4 or_ = *(const u32x4*)(sE + ((((cc * 2 + u) * 2 + (1 - v)) * 2 + 0) * 1024) + l16);
    const u32x4 oi_ = *(const u32x4*)(sE + ((((cc * 2 + u) * 2 + (1 - v)) * 2 + 1) * 1024) + l16);
    zr[0] = v ? or_ : mr; zi[0] = v ? oi_ : mi;
    zr[1] = v ? mr : or_; zi[1] = v ? mi : oi_;
  }
  {
    f32x4 dr[2] = {Z4, Z4}, di[2] = {Z4, Z4};
#pragma unroll
    for (int t = 0; t < 2; ++t) {
#pragma unroll
      for (int ii = 0; ii < 2; ++ii) { dr[ii] = mfma(zr[t], f64r[ii][t], dr[ii]); di[ii] = mfma(zr[t], f64n[ii][t], di[ii]); }
#pragma unroll
      for (int ii = 0; ii < 2; ++ii) { dr[ii] = mfma(zi[t], f64i[ii][t], dr[ii]); di[ii] = mfma(zi[t], f64r[ii][t], di[ii]); }
    }
#pragma unroll
    for (int ii = 0; ii < 2; ++ii) {
      float tr[4], tq[4];
#pragma unroll
      for (int r = 0; r < 4; ++r) {
        tr[r] = dr[ii][r] * itc[ii * 4 + r] - di[ii][r] * its[ii * 4 + r];
        tq[r] = dr[ii][r] * its[ii * 4 + r] + di[ii][r] * itc[ii * 4 + r];
      }
      *(u32x2*)(sG + ((((cc * 2 + v) * 2 + ii) * 2 + 0) * 1024) + l16 + u * 8) = u32x2{pk2(tr[0], tr[1]), pk2(tr[2], tr[3])};
      *(u32x2*)(sG + ((((cc * 2 + v) * 2 + ii) * 2 + 1) * 1024) + l16 + u * 8) = u32x2{pk2(tq[0], tq[1]), pk2(tq[2], tq[3])};
    }
  }
  STAMP(KID, 5);
  asm volatile("s_waitcnt lgkmcnt(0)\n\ts_barrier" ::: "memory");
  {
    u32x4 tr[2], tq[2];
#pragma unroll
    for (int ii = 0; ii < 2; ++ii) {
      tr[ii] = *(const u32x4*)(sG + ((((cc * 2 + v) * 2 + ii) * 2 + 0) * 1024) + l16);
      tq[ii] = *(const u32x4*)(sG + ((((cc * 2 + v) * 2 + ii) * 2 + 1) * 1024) + l16);
    }
    f32x4 orr[2], oi[2];
#pragma unroll
    for (int ii = 0; ii < 2; ++ii) { orr[ii] = mfma(f32r, tr[ii], Z4); oi[ii] = mfma(f32r, tq[ii], Z4); }
#pragma unroll
    for (int ii = 0; ii < 2; ++ii) { orr[ii] = mfma(f32i, tq[ii], orr[ii]); oi[ii] = mfma(f32n, tr[ii], oi[ii]); }
#pragma unroll
    for (int ii = 0; ii < 2; ++ii) {
      const int tt = (2 * v + ii) * 128 + g * 32 + u * 16 + c16;
      *(u32x4*)(sY + (cc * 512 + tt) * 16) = u32x4{pk2(orr[ii][0], oi[ii][0]), pk2(orr[ii][1], oi[ii][1]), pk2(orr[ii][2], oi[ii][2]), pk2(orr[ii][3], oi[ii][3])};
    }
  }
  STAMP(KID, 6);
  asm volatile("s_waitcnt lgkmcnt(0)\n\ts_barrier" ::: "memory");
  {
    char* Xout = ws + (LAYER == 0 ? OFF_X1 : OFF_X3);
    const u32x4 ya = *(const u32x4*)(sY + tid * 16), yb = *(const u32x4*)(sY + (512 + tid) * 16);
#pragma unroll
    for (int r = 0; r < 4; ++r) {
      const u32 a = ya[r], bq = yb[r], r0 = raw[r][0], r1 = raw[r][1];
      const float v0 = bflo(r0) + bflo(a);
      const float v1 = special ? bfhi(r0) : bfhi(r0) + bfhi(a);
      const float v2 = bflo(r1) + bflo(bq), v3 = bfhi(r1) + bfhi(bq);
      *(u32x2*)(Xout + ((size_t)oct * 2048 + r * 512 + tid) * 16 + hf * 8) = u32x2{pk2(v0, v1), pk2(v2, v3)};
    }
  }
  STAMP(KID, 7); RSTAMP(KID, 15);
}

DI f32x2 gelu2(f32x2 x) {
  const float k1 = -2.3022081978378774f;
  const float k2 = k1 * 0.044715f;
  const f32x2 t = x * x;
  const f32x2 u = t * k2 + k1;
  const f32x2 z = x * u;
  f32x2 d = f32x2{__builtin_amdgcn_exp2f(z[0]), __builtin_amdgcn_exp2f(z[1])} + 1.f;
  return x * f32x2{__builtin_amdgcn_rcpf(d[0]), __builtin_amdgcn_rcpf(d[1])};
}
template <int IMM> DI void dma16_nom0(i32x4 rsrc, u32 voff, u32 soff) {
  asm volatile("buffer_load_dwordx4 %0, %1, %2 offen offset:%3 lds" ::"v"(voff), "s"(rsrc), "s"(RFL(soff)), "i"(IMM) : "memory");
}
template <int N> DI void wait_vm_bar(u32x4& a, u32x4& b, u32x4& c, u32x4& d, u32x4& e, u32x4& f, u32x4& g, u32x4& h) { wait_vm_bar8<N>(a, b, c, d, e, f, g, h); }

template <int LAYER, int NF, int NQ>
DI void gemm1_body(const Params& P, char* smem, int ft, int tt, int lane, int w) {
  constexpr int KID = LAYER == 0 ? 2 : 5;
  constexpr int NM = NF + NQ, H = (NM + 1) / 2, RING = (LAYER == 0) ? 20 : 16, BR = 12;
  const int c16 = lane & 15, g = lane >> 4;
  char* ws = P.ws;
  const i32x4 rs = mk_rsrc(ws, (u32)WS_END);
  const u32 l16 = lane * 16;
  const int p0 = tt * 128 + w * 32;
  const int f0 = (ft < 12) ? 7 * ft : 84 + 6 * (ft - 12);
  u32x2 bw[NF];
#pragma unroll
  for (int m = 0; m < NF; ++m) bw[m] = *(const u32x2*)(P.b1 + (f0 + m) * 16 + 4 * g);
  u32x4 bq[BR][2];
  const u32 xoff = (u32)(LAYER == 0 ? OFF_X1 : OFF_X3) + (u32)p0 * 16;
  const u32 bvo = (u32)g * 32768u + (u32)c16 * 16u;
  const u32 asrc = (u32)OFF_A0 + (u32)(ft * KS1 + w) * 7168u, qsrc = (u32)OFF_A1 + (u32)(ft * KS1 + w) * 4096u;
  const u32 adst = lds_addr(smem) + (u32)w * (NM * 1024);
  constexpr int PER = NM + 8;
  auto issue = [&](auto C, auto K) {
    constexpr int c = C, k = K;
    if constexpr (k < NF) {
      constexpr int k4 = k & ~3;
      const u32 so = asrc + (u32)(c * 4 * 7 + k4) * 1024u;
      if constexpr ((k & 3) == 0) dma16<0>(rs, l16, so, adst + (u32)((((c * 4) % RING) * NM + k4) * 1024));
      else dma16_nom0<(k & 3) * 1024>(rs, l16, so);
    } else if constexpr (k < NM) {
      constexpr int kq = k - NF;
      const u32 so = qsrc + (u32)(c * 4 * 4) * 1024u;
      if constexpr (kq == 0) dma16<0>(rs, l16, so, adst + (u32)((((c * 4) % RING) * NM + NF) * 1024));
      else dma16_nom0<kq * 1024>(rs, l16, so);
    } else {
      constexpr int kk = (k - NM) >> 1, t = 4 * c + kk;
      const u32 so = xoff + (u32)t * 131072u;
      if constexpr (((k - NM) & 1) == 0) bld16<0>(bq[t % BR][0], rs, bvo, so);
      else bld16<256>(bq[t % BR][1], rs, bvo, so);
    }
  };
  static_for<0, 2>([&](auto C) { static_for<0, PER>([&](auto K) { issue(C, K); }); });
  STAMP(KID, 1);
  f32x4 acc[NM][2], gram[2];
#pragma unroll
  for (int m = 0; m < NM; ++m) { acc[m][0] = f32x4{0.f, 0.f, 0.f, 0.f}; acc[m][1] = acc[m][0]; }
  gram[0] = gram[1] = f32x4{0.f, 0.f, 0.f, 0.f};
  u32x4 af[3][H];
  auto waitchunk = [&](auto C, auto N) {
    constexpr int c = C;
    wait_vm_bar<N.value>(bq[(4 * c) % BR][0], bq[(4 * c) % BR][1], bq[(4 * c + 1) % BR][0], bq[(4 * c + 1) % BR][1], bq[(4 * c + 2) % BR][0],
                         bq[(4 * c + 2) % BR][1], bq[(4 * c + 3) % BR][0], bq[(4 * c + 3) % BR][1]);
  };
  auto rd1 = [&](auto HH, auto K) {
    constexpr int h = HH, k = K, t = h >> 1, m0 = (h & 1) ? H : 0, m1 = (h & 1) ? NM : H;
    if constexpr (t < KS1 && m0 + k < m1) af[h % 3][k] = *(const u32x4*)(smem + ((t % RING) * NM + m0 + k) * 1024 + l16);
  };
  waitchunk(IC<0>{}, IC<PER>{});
  STAMP(KID, 2);
  static_for<0, H>([&](auto K) { rd1(IC<0>{}, K); });
  static_for<0, H>([&](auto K) { rd1(IC<1>{}, K); });
  static_for<0, 2 * KS1>([&](auto HH) {
    constexpr int h = HH, t = h >> 1, c = t >> 2, q = t & 3, m0 = (h & 1) ? H : 0, m1 = (h & 1) ? NM : H;
    constexpr int PS = (PER + 2) / 3;
    if constexpr ((h & 1) == 0 && q == 3 && t + 1 < KS1) waitchunk(IC<c + 1>{}, IC<(c + 1 < 4 ? PER : 0)>{});
    constexpr int n1 = (h + 1 < 2 * KS1) ? (((h + 1) & 1) ? NM - H : H) : 0;
    __builtin_amdgcn_s_waitcnt(0xC07F | (n1 << 8));
    SB();
    static_for<m0, m1>([&](auto M) {
      constexpr int m = M;
      acc[m][0] = mfma(af[h % 3][m - m0], bq[t % BR][0], acc[m][0]);
      SB();
      rd1(IC<h + 2>{}, IC<m - m0>{});
      SB();
      acc[m][1] = mfma(af[h % 3][m - m0], bq[t % BR][1], acc[m][1]);
      SB();
      if constexpr (c + 2 < 5 && q < 3 && m < PS && q * PS + m < PER) issue(IC<c + 2>{}, IC<q * PS + m>{});
      SB();
    });
    if constexpr ((h & 1) == 0) {
      u32x4 e0 = bq[t % BR][0], e1 = bq[t % BR][1];
      if constexpr (t == 0) { e0[0] = (g == 0) ? (e0[0] & 0xffffu) : (u32)e0[0]; e1[0] = (g == 0) ? (e1[0] & 0xffffu) : (u32)e1[0]; }
      gram[0] = mfma(e0, e0, gram[0]);
      gram[1] = mfma(e1, e1, gram[1]);
      SB();
    } else if constexpr (NM - H < H) {
      rd1(IC<h + 2>{}, IC<H - 1>{});
    }
  });
  STAMP(KID, 3);
  float a_sc[2];
#pragma unroll
  for (int n = 0; n < 2; ++n) {
    const int r = c16 & 3;
    float dv = r == 0 ? gram[n][0] : r == 1 ? gram[n][1] : r == 2 ? gram[n][2] : gram[n][3];
    dv = (g == (c16 >> 2)) ? dv : 0.f;
    const float var = red_g(dv) * (1.f / 640.f);
    const float d1 = ln_den(var);
    a_sc[n] = rcp(d1 * ln_den(var * rcp(d1 * d1)));
  }
  char* H_ = ws + (LAYER == 0 ? OFF_H1 : OFF_H2);
#pragma unroll
  for (int m = 0; m < NF; ++m) {
    const int mg = f0 + m;
    const f32x2 b01 = {bflo(bw[m][0]), bfhi(bw[m][0])}, b23 = {bflo(bw[m][1]), bfhi(bw[m][1])};
#pragma unroll
    for (int n = 0; n < 2; ++n) {
      const f32x4 a = acc[m][n];
      const f32x2 h01 = gelu2(f32x2{a[0], a[1]} * a_sc[n] + b01), h23 = gelu2(f32x2{a[2], a[3]} * a_sc[n] + b23);
      const int tile = tt * 8 + w * 2 + n;
      *(u32x2*)(H_ + ((((size_t)(tile >> 2) * KS2 + (mg >> 1)) * 4 + (tile & 3)) * 64 + (2 * (mg & 1) + (g >> 1)) * 16 + c16) * 16 + (g & 1) * 8) =
          u32x2{pk2(h01[0], h01[1]), pk2(h23[0], h23[1])};
    }
  }
  if constexpr (NQ > 0) {
    const int d0 = (ft < 12) ? 2 * ft : 24 + 4 * (ft - 12);
#pragma unroll
    for (int m = 0; m < NQ; ++m)
#pragma unroll
      for (int n = 0; n < 2; ++n) {
        const f32x4 a = acc[NF + m][n];
        const int tile = tt * 8 + w * 2 + n;
        *(u32x2*)(ws + OFF_X3S + (((size_t)(d0 + m) * 128 + tile) * 64 + lane) * 8) = u32x2{pk2(a[0], a[1]), pk2(a[2], a[3])};
      }
  }
}

template <int LAYER>
__global__ void __launch_bounds__(256, 2) k_gemm1(Params P) {
  extern __shared__ __attribute__((aligned(16))) char smem[];
  const int b = blockIdx.x, tid = threadIdx.x, lane = tid & 63, w = __builtin_amdgcn_readfirstlane(tid >> 6);
  constexpr int KID = LAYER == 0 ? 2 : 5;
  RSTAMP(KID, 14); STAMP(KID, 0);
  const int x = b & 7, li = b >> 3;
  const int ft = (x & 1) * 8 + (li & 7), tt = (x >> 1) * 4 + (li >> 3);
  if (ft < 12) gemm1_body<LAYER, 7, LAYER == 0 ? 0 : 2>(P, smem, ft, tt, lane, w);
  else gemm1_body<LAYER, 6, LAYER == 0 ? 0 : 4>(P, smem, ft, tt, lane, w);
  STAMP(KID, 4); RSTAMP(KID, 15);
}

template <int LAYER>
__global__ void __launch_bounds__(256, 1) k_gemm2(Params P) {
  extern __shared__ __attribute__((aligned(16))) char smem[];
  const int b = blockIdx.x, tid = threadIdx.x, lane = tid & 63, w = __builtin_amdgcn_readfirstlane(tid >> 6);
  const int c16 = lane & 15, g = lane >> 4;
  char* ws = P.ws;
  const int x = b & 7, li = b >> 3;
  const int ct = (x & 1) * 4 + (li & 3), tt = (x >> 1) * 8 + (li >> 2);
  constexpr int KID = LAYER == 0 ? 3 : 6;
  RSTAMP(KID, 14); STAMP(KID, 0);
  const auto rs = __builtin_amdgcn_make_buffer_rsrc((void*)ws, 0, (int)WS_END, 0x00020000);
  const u32 l16 = lane * 16;
  const u32 aoff = (u32)(LAYER == 0 ? OFF_W2F : OFF_W2T) + (u32)ct * (KS2 * 5 * 1024) + (u32)w * 5120u;
  const u32 boff = (u32)(LAYER == 0 ? OFF_H1 : OFF_H2) + (u32)tt * (KS2 * 4096) + (u32)w * 4096u;
  constexpr int PD = 5, NSTEP = 14;
  u32x4 ra[PD][5], rb[PD][4];
  const bool full = (w < 2);
  auto ld1 = [&](auto I, auto K) {
    constexpr int i = I, k = K;
    if constexpr (k < 4) rb[i % PD][k] = __builtin_amdgcn_raw_buffer_load_b128(rs, l16 + k * 1024, boff + (u32)i * 16384u, 0);
    else if constexpr (k < 8) ra[i % PD][k - 4] = __builtin_amdgcn_raw_buffer_load_b128(rs, l16 + (k - 4) * 1024, aoff + (u32)i * 20480u, 0);
    else ra[i % PD][4] = __builtin_amdgcn_raw_buffer_load_b128(rs, l16, aoff + (u32)i * 20480u + 4096u, 0);
  };
  static_for<0, 9>([&](auto K) { ld1(IC<0>{}, K); });
  SB();
  const int p = tt * 64 + w * 16 + c16;
  u32x2 rz[5]; f32x4 bz[5];
#pragma unroll
  for (int m = 0; m < 5; ++m) {
    const int ms = ct * 5 + m;
    if (LAYER == 0) {
      rz[m] = *(const u32x2*)(ws + OFF_X1 + ((size_t)(2 * ms + (g >> 1)) * 2048 + p) * 16 + (g & 1) * 8);
      bz[m] = *(const f32x4*)(ws + OFF_B2F + (ms * 16 + 4 * g) * 4);
    } else {
      rz[m] = *(const u32x2*)(ws + OFF_X3S + (((size_t)ms * 128 + tt * 4 + w) * 64 + lane) * 8);
      const u32x2 bw = *(const u32x2*)(P.b2 + ms * 16 + 4 * g);
      bz[m] = f32x4{bflo(bw[0]), bfhi(bw[0]), bflo(bw[1]), bfhi(bw[1])};
    }
  }
  SB();
  static_for<1, PD - 1>([&](auto I) { static_for<0, 9>([&](auto K) { ld1(I, K); }); });
  f32x4 acc[5][4];
#pragma unroll
  for (int m = 0; m < 5; ++m)
#pragma unroll
    for (int n = 0; n < 4; ++n) acc[m][n] = f32x4{0.f, 0.f, 0.f, 0.f};
  SB();
  STAMP(KID, 1);
  static_for<0, NSTEP - 1>([&](auto I) {
    constexpr int i = I, nx = i + PD - 1;
    static_for<0, 5>([&](auto M) {
      constexpr int m = M;
#pragma unroll
      for (int n = 0; n < 4; ++n) acc[m][n] = mfma(ra[i % PD][m], rb[i % PD][n], acc[m][n]);
      SB();
      if constexpr (nx < NSTEP - 1) {
        ld1(IC<nx>{}, IC<2 * m>{});
        if constexpr (m < 4) ld1(IC<nx>{}, IC<2 * m + 1>{});
      } else if constexpr (nx == NSTEP - 1) {
        if (full) {
          ld1(IC<nx>{}, IC<2 * m>{});
          if constexpr (m < 4) ld1(IC<nx>{}, IC<2 * m + 1>{});
        }
      }
      SB();
    });
  });
  if (full) {
    constexpr int i = NSTEP - 1;
#pragma unroll
    for (int m = 0; m < 5; ++m)
#pragma unroll
      for (int n = 0; n < 4; ++n) acc[m][n] = mfma(ra[i % PD][m], rb[i % PD][n], acc[m][n]);
  }
  SB();
  STAMP(KID, 2);
  f32x4* red = (f32x4*)smem;
#pragma unroll
  for (int n = 0; n < 4; ++n)
    if (n != w) {
#pragma unroll
      for (int m = 0; m < 5; ++m) red[((n * 4 + w) * 5 + m) * 64 + lane] = acc[m][n];
    }
  f32x4 own[5];
#pragma unroll
  for (int m = 0; m < 5; ++m) own[m] = (w == 0) ? acc[m][0] : (w == 1) ? acc[m][1] : (w == 2) ? acc[m][2] : acc[m][3];
  __syncthreads();
  STAMP(KID, 3);
  float ssq = 0.f;
#pragma unroll
  for (int m = 0; m < 5; ++m) {
    f32x4 s = own[m];
#pragma unroll
    for (int k = 1; k < 4; ++k) s += red[((w * 4 + ((w + k) & 3)) * 5 + m) * 64 + lane];
    const int ms = ct * 5 + m;
    const float o0 = rbf(bflo(rz[m][0]) + s[0] + bz[m][0]), o1 = rbf(bfhi(rz[m][0]) + s[1] + bz[m][1]);
    const float o2 = rbf(bflo(rz[m][1]) + s[2] + bz[m][2]), o3 = rbf(bfhi(rz[m][1]) + s[3] + bz[m][3]);
    if (LAYER == 0) {
      *(u32x2*)(ws + OFF_X2 + ((size_t)(2 * ms + (g >> 1)) * 2048 + p) * 16 + (g & 1) * 8) = u32x2{pk2(o0, o1), pk2(o2, o3)};
      ssq += o0 * o0 + o2 * o2 + o3 * o3;
      ssq += (ms == 0 && g == 0) ? 0.f : o1 * o1;
    } else {
      *(u32x2*)(P.out + (size_t)s_of_p(p) * D_ + ms * 16 + 4 * g) = u32x2{pk2(o0, o1), pk2(o2, o3)};
    }
  }
  if (LAYER == 0) {
    ssq = red_g(ssq);
    if (g == 0) atomicAdd((float*)(ws + OFF_SS2) + p, ssq);
  }
  STAMP(KID, 4); RSTAMP(KID, 15);
}

extern "C" void kernel_launch(void* const* d_in, const int* in_sizes, int n_in, void* d_out, int out_size, void* d_ws,
                              size_t ws_size, hipStream_t stream) {
  Params P;
  P.x = (const u16*)d_in[0]; P.wr = (const u16*)d_in[1]; P.wi = (const u16*)d_in[2];
  P.w1 = (const u16*)d_in[4]; P.b1 = (const u16*)d_in[5]; P.w2 = (const u16*)d_in[6]; P.b2 = (const u16*)d_in[7];
  P.out = (u16*)d_out; P.ws = (char*)d_ws;
  {
    hipFuncSetAttribute((const void*)k0_prep, hipFuncAttributeMaxDynamicSharedMemorySize, 163840);
    hipFuncSetAttribute((const void*)k_col<0>, hipFuncAttributeMaxDynamicSharedMemorySize, 163840);
    hipFuncSetAttribute((const void*)k_col<1>, hipFuncAttributeMaxDynamicSharedMemorySize, 163840);
    hipFuncSetAttribute((const void*)k_gemm1<0>, hipFuncAttributeMaxDynamicSharedMemorySize, 163840);
    hipFuncSetAttribute((const void*)k_gemm1<1>, hipFuncAttributeMaxDynamicSharedMemorySize, 163840);
    hipFuncSetAttribute((const void*)k_gemm2<0>, hipFuncAttributeMaxDynamicSharedMemorySize, 163840);
    hipFuncSetAttribute((const void*)k_gemm2<1>, hipFuncAttributeMaxDynamicSharedMemorySize, 163840);
  }
  k0_prep<<<242, 512, 163840, stream>>>(P);
  k_col<0><<<160, 512, 86016, stream>>>(P);
  k_gemm1<0><<<256, 256, 143360, stream>>>(P);
  k_gemm2<0><<<256, 256, 81920, stream>>>(P);
  k_col<1><<<160, 512, 86016, stream>>>(P);
  k_gemm1<1><<<256, 256, 163840, stream>>>(P);
  k_gemm2<1><<<256, 256, 81920, stream>>>(P);
}
```

```cpp
#include <hip/hip_runtime.h>
#include <stdint.h>

typedef __bf16 bf16x2 __attribute__((ext_vector_type(2)));
typedef __bf16 bf16x4 __attribute__((ext_vector_type(4)));
typedef __bf16 bf16x8 __attribute__((ext_vector_type(8)));
typedef float f32x2 __attribute__((ext_vector_type(2)));
typedef float f32x4 __attribute__((ext_vector_type(4)));
typedef unsigned u32;
typedef unsigned u32x2 __attribute__((ext_vector_type(2)));
typedef unsigned u32x4 __attribute__((ext_vector_type(4)));
typedef unsigned short u16;
#define LAS __attribute__((address_space(3)))
#define DI __device__ __forceinline__

constexpr int S_ = 2048, D_ = 640, F_ = 1728, DF_ = 321;
constexpr int KS1 = 20;
constexpr int KS2 = 54;
constexpr float EPS = 1e-5f;

constexpr size_t XT_B = (size_t)80 * 2048 * 16;
constexpr size_t H_B = (size_t)128 * KS2 * 1024;
constexpr size_t A0_B = (size_t)16 * KS1 * 7 * 1024;
constexpr size_t A1_B = (size_t)16 * KS1 * 4 * 1024;
constexpr size_t W2F_B = (size_t)8 * KS2 * 5 * 1024;
constexpr size_t WT_B = (size_t)320 * 8192;
constexpr size_t X3S_B = (size_t)40 * 128 * 512;
constexpr size_t OFF_X0 = 0, OFF_X1 = OFF_X0 + XT_B, OFF_X2 = OFF_X1 + XT_B, OFF_X3 = OFF_X2 + XT_B;
constexpr size_t OFF_H1 = OFF_X3 + XT_B, OFF_H2 = OFF_H1 + H_B;
constexpr size_t OFF_A0 = OFF_H2 + H_B, OFF_A1 = OFF_A0 + A0_B;
constexpr size_t OFF_W2F = OFF_A1 + A1_B, OFF_W2T = OFF_W2F + W2F_B;
constexpr size_t OFF_WT = OFF_W2T + W2F_B;
constexpr size_t OFF_X3S = OFF_WT + WT_B;
constexpr size_t OFF_RSTD0 = OFF_X3S + X3S_B;
constexpr size_t OFF_SS2 = OFF_RSTD0 + 8192;
constexpr size_t OFF_B2F = OFF_SS2 + 8192;
constexpr size_t OFF_TAB = OFF_B2F + 4096;
constexpr size_t OFF_CNT = OFF_TAB + 20480;
constexpr size_t WS_END = OFF_CNT + 8192;

DI u32 pk2(float a, float b) { f32x2 v = {a, b}; bf16x2 r = __builtin_convertvector(v, bf16x2); return __builtin_bit_cast(u32, r); }

DI float bflo(u32 w) { return __builtin_bit_cast(float, w << 16); }
DI float bfhi(u32 w) { return __builtin_bit_cast(float, w & 0xffff0000u); }
DI float rbf(float x) { return bflo(pk2(x, x)); }
DI f32x4 mfma(u32x4 a, u32x4 b, f32x4 c) {
  return __builtin_amdgcn_mfma_f32_16x16x32_bf16(__builtin_bit_cast(bf16x8, a), __builtin_bit_cast(bf16x8, b), c, 0, 0, 0);
}
DI float dot2(u32 a, u32 b, float c) { return __builtin_amdgcn_fdot2_f32_bf16(__builtin_bit_cast(bf16x2, a), __builtin_bit_cast(bf16x2, b), c, false); }
DI float cos2pi(float x) { return __builtin_amdgcn_cosf(x); }
DI float sin2pi(float x) { return __builtin_amdgcn_sinf(x); }
DI int s_of_p(int p) {
  const int r = p >> 9, i = (p >> 7) & 3, g = (p >> 5) & 3, uu = (p >> 4) & 1, c16 = p & 15;
  return 64 * (16 * uu + 4 * g + r) + 16 * i + c16;
}
DI void glds16(const void* g, LAS void* l) { __builtin_amdgcn_global_load_lds(g, l, 16, 0, 0); }
#define WAIT_VM0() asm volatile("s_waitcnt vmcnt(0)" ::: "memory")
#define WAIT_LG0() asm volatile("s_waitcnt lgkmcnt(0)" ::: "memory")
#define BAR() __builtin_amdgcn_s_barrier()
DI float ln_den(float var) { return rbf(__builtin_amdgcn_sqrtf(rbf(rbf(var) + EPS))); }
DI float rcp(float x) { return __builtin_amdgcn_rcpf(x); }

typedef int i32x4 __attribute__((ext_vector_type(4)));
DI i32x4 mk_rsrc(const void* p, u32 bytes) {
  const unsigned long long a = (unsigned long long)p;
  return i32x4{(int)__builtin_amdgcn_readfirstlane((u32)a), (int)__builtin_amdgcn_readfirstlane((u32)(a >> 32) & 0xffffu), (int)bytes, 0x00020000};
}
#define RFL(x) __builtin_amdgcn_readfirstlane(x)
#define PADS
template <int IMM> DI void dma16(i32x4 rsrc, u32 voff, u32 soff, u32 lds) {
  asm volatile(PADS "s_mov_b32 m0, %3\n\ts_nop 0\n\tbuffer_load_dwordx4 %0, %1, %2 offen offset:%4 lds" ::"v"(voff), "s"(rsrc), "s"(RFL(soff)), "s"(RFL(lds)), "i"(IMM) : "memory");
}
template <int IMM> DI void bld16(u32x4& dst, i32x4 rsrc, u32 voff, u32 soff) {
  asm volatile(PADS "buffer_load_dwordx4 %0, %1, %2, %3 offen offset:%4" : "=v"(dst) : "v"(voff), "s"(rsrc), "s"(RFL(soff)), "i"(IMM) : "memory");
}
DI void bldu16(u32& dst, i32x4 rsrc, u32 voff, u32 soff) {
  asm volatile(PADS "buffer_load_ushort %0, %1, %2, %3 offen" : "=v"(dst) : "v"(voff), "s"(rsrc), "s"(RFL(soff)) : "memory");
}
DI void bld8(u32x2& dst, i32x4 rsrc, u32 voff, u32 soff) {
  asm volatile(PADS "buffer_load_dwordx2 %0, %1, %2, %3 offen" : "=v"(dst) : "v"(voff), "s"(rsrc), "s"(RFL(soff)) : "memory");
}
DI void bld4(u32& dst, i32x4 rsrc, u32 voff, u32 soff) {
  asm volatile(PADS "buffer_load_dword %0, %1, %2, %3 offen" : "=v"(dst) : "v"(voff), "s"(rsrc), "s"(RFL(soff)) : "memory");
}
#define DMA16(rsrc, voff, soff, lds, IMM) dma16<IMM>(rsrc, voff, soff, lds)
#define BLD16(dst, rsrc, voff, soff, IMM) bld16<IMM>(dst, rsrc, voff, soff)
template <int N> DI void wait_vm_bar8(u32x4& a, u32x4& b, u32x4& c, u32x4& d, u32x4& e, u32x4& f, u32x4& g, u32x4& h) {
  asm volatile("s_waitcnt vmcnt(%8)\n\ts_barrier" : "+v"(a), "+v"(b), "+v"(c), "+v"(d), "+v"(e), "+v"(f), "+v"(g), "+v"(h) : "i"(N) : "memory");
}
#define WAIT_VM(N) asm volatile("s_waitcnt vmcnt(" #N ")" ::: "memory")
DI float red_g(float x) {
  const u32 xi = __builtin_bit_cast(u32, x);
  auto a = __builtin_amdgcn_permlane32_swap(xi, xi, false, false);
  const float y = __builtin_bit_cast(float, (u32)a[0]) + __builtin_bit_cast(float, (u32)a[1]);
  const u32 yi = __builtin_bit_cast(u32, y);
  auto c = __builtin_amdgcn_permlane16_swap(yi, yi, false, false);
  return __builtin_bit_cast(float, (u32)c[0]) + __builtin_bit_cast(float, (u32)c[1]);
}
DI u32 lds_addr(const void* p) { return (u32)(uintptr_t)(LAS const char*)p; }

template <int I> struct IC { static constexpr int value = I; constexpr operator int() const { return I; } };
template <int B, int E, class F> DI void static_for(F&& f) { if constexpr (B < E) { f(IC<B>{}); static_for<B + 1, E>(f); } }
#define SB() __builtin_amdgcn_sched_barrier(0)

struct Params {
  const u16 *x, *wr, *wi, *w1, *b1, *w2, *b2;
  u16* out;
  char* ws;
};

#define STAMP(kid, i)
#define RSTAMP(kid, i)

struct QGen { float v0[8], v1[8], k; };
DI void gen_q_init(QGen& G, int ms, int lane) {
  const int c = ms * 16 + (lane & 15), g = lane >> 4;
  int jj = c >> 1; float amp = 0.05590169943749474f; float ph = (c & 1) ? 0.25f : 0.f;
  if (c < 2) { jj = (c == 0) ? 320 : 0; amp = 0.03952847075210474f; ph = 0.f; }
  const float fj = (float)jj * (1.f / 640.f);
  const float kj = 2.f * cos2pi(fj), d = (float)(8 * g);
  G.v0[0] = amp * cos2pi(__builtin_amdgcn_fractf(fj * d) + ph);          G.v0[1] = amp * cos2pi(__builtin_amdgcn_fractf(fj * (d + 1.f)) + ph);
  G.v1[0] = amp * cos2pi(__builtin_amdgcn_fractf(fj * (d + 32.f)) + ph); G.v1[1] = amp * cos2pi(__builtin_amdgcn_fractf(fj * (d + 33.f)) + ph);
#pragma unroll
  for (int j = 2; j < 8; ++j) { G.v0[j] = kj * G.v0[j - 1] - G.v0[j - 2]; G.v1[j] = kj * G.v1[j - 1] - G.v1[j - 2]; }
  G.k = 2.f * cos2pi(__builtin_amdgcn_fractf((float)jj * 0.05f));
}
DI u32x4 gen_q_step(QGen& G) {
  const u32x4 r = u32x4{pk2(G.v0[0], G.v0[1]), pk2(G.v0[2], G.v0[3]), pk2(G.v0[4], G.v0[5]), pk2(G.v0[6], G.v0[7])};
#pragma unroll
  for (int j = 0; j < 8; ++j) { const float n = G.k * G.v1[j] - G.v0[j]; G.v0[j] = G.v1[j]; G.v1[j] = n; }
  return r;
}

struct JobOps { i32x4 rs; u32 vo[10]; u32 l0; };
DI void job_ops(JobOps& J, const Params& P, int job, int w8, int lane, u32 lbuf) {
  asm volatile("" : "+v"(lane), "+s"(job), "+s"(w8));
  if (job >= 32 && job < 59) {
    const int d = 80 * w8 + (lane >> 3), pos = lane & 7;
    J.rs = mk_rsrc(P.w1, D_ * F_ * 2);
    const u32 va = (u32)d * (F_ * 2) + (u32)(job - 32) * 128u + 16u * (u32)(pos ^ (((d >> 1) & 1) << 1));
#pragma unroll
    for (int k = 0; k < 10; ++k) J.vo[k] = ((k & 1) ? (va ^ 64u) : va) + (u32)k * (8 * F_ * 2);
    J.l0 = lbuf + (u32)w8 * 10240u;
  } else {
    const int nn = w8 >> 1, hh = w8 & 1;
    u32 s0;
    if (job < 32) { J.rs = mk_rsrc(P.x, S_ * D_ * 2); s0 = (u32)s_of_p(job * 64 + nn * 16) * (D_ * 2); }
    else if (job < 86) { J.rs = mk_rsrc(P.w2, F_ * D_ * 2); s0 = (u32)((job - 59) * 64 + nn * 16) * (D_ * 2); }
    else { J.rs = mk_rsrc(P.b2, D_ * 2); s0 = 0; }
    J.l0 = lbuf + (u32)nn * 20480u + (u32)hh * 10240u;
    const u32 va = (u32)lane * 16u;
    if (hh == 0) {
#pragma unroll
      for (int k = 0; k < 10; ++k) J.vo[k] = (va ^ (u32)(k * 16)) + (s0 + k * 1280u);
    } else {
      const u32 vb = (u32)(lane >> 4) * 1280u + 1024u + (((u32)(lane & 15) ^ (u32)(lane >> 4)) * 16u);
#pragma unroll
      for (int k = 0; k < 6; ++k) J.vo[k] = (va ^ (u32)((10 + k) * 16)) + (s0 + (10 + k) * 1280u);
#pragma unroll
      for (int k = 6; k < 10; ++k) J.vo[k] = (vb ^ (u32)((k - 6) * 64)) + (s0 + (k - 6) * 5120u);
    }
  }
}
template <int K> DI void job_op(const JobOps& J) {
  if constexpr (K == 0) asm volatile(PADS "s_mov_b32 m0, %2\n\ts_nop 0\n\tbuffer_load_dwordx4 %0, %1, 0 offen lds\n\ts_add_u32 m0, m0, 0x400" ::"v"(J.vo[0]), "s"(J.rs), "s"(RFL(J.l0)) : "memory", "scc");
  else asm volatile(PADS "buffer_load_dwordx4 %0, %1, 0 offen lds\n\ts_add_u32 m0, m0, 0x400" ::"v"(J.vo[K]), "s"(J.rs) : "memory", "scc");
}

DI u32x4 ident_frag(int par, int lane) {
  const int j = 16 * par + (lane & 15) - 8 * (lane >> 4);
  u32x4 r;
#pragma unroll
  for (int k = 0; k < 4; ++k) r[k] = (j == 2 * k ? 0x3F80u : 0u) | (j == 2 * k + 1 ? 0x3F800000u : 0u);
  return r;
}

template <int MODE, class HK>
DI void qloop_lin(const char* buf, int sl, int lane, u32x4 (&q)[2][KS1], f32x4 (&acc)[2][2], f32x4& sum, f32x4& gram, HK&& hook) {
  u32x4 bf[2][2];
  const int row = lane & 15, g = lane >> 4;
  const char* ba[4];
#pragma unroll
  for (int j = 0; j < 4; ++j) ba[j] = buf + row * 1024 + (((4 * j + g) ^ row) * 16);
  const int dB = 16384 - row * 768;
  auto rd = [&](auto T, auto N) { constexpr int t = T, n = N;
    if constexpr (t < 16) bf[t & 1][n] = *(const u32x4*)(ba[t & 3] + n * 20480 + (t >> 2) * 256);
    else if constexpr (t < KS1) bf[t & 1][n] = *(const u32x4*)(ba[t & 3] + dB + n * 20480); };
  rd(IC<0>{}, IC<0>{}); rd(IC<0>{}, IC<1>{});
  static_for<0, KS1>([&](auto T) {
    constexpr int t = T;
    rd(IC<t + 1>{}, IC<0>{}); rd(IC<t + 1>{}, IC<1>{});
    __builtin_amdgcn_s_waitcnt(0xC07F | ((t + 1 < KS1 ? 2 : 0) << 8));
    SB();
    static_for<0, 2>([&](auto N) {
      constexpr int n = N;
      if constexpr (MODE == 2) acc[0][n] = mfma(bf[t & 1][n], q[0][t], acc[0][n]); else acc[0][n] = mfma(q[0][t], bf[t & 1][n], acc[0][n]);
      if constexpr (MODE == 2) acc[1][n] = mfma(bf[t & 1][n], q[1][t], acc[1][n]); else acc[1][n] = mfma(q[1][t], bf[t & 1][n], acc[1][n]);
      SB();
      hook(T, N);
      SB();
    });
    if constexpr (MODE == 1) {
      const u32x4 e = sl ? bf[t & 1][1] : bf[t & 1][0];
      sum = mfma(e, u32x4{0x3F803F80u, 0x3F803F80u, 0x3F803F80u, 0x3F803F80u}, sum);
      gram = mfma(e, e, gram);
      SB();
    }
  });
}
template <class HK>
DI void qloop_tr(const char* buf, int nh, int lane, u32x4 (&q)[2][KS1], f32x4 (&acc)[2][2], HK&& hook) {
  const int g = lane >> 4, pp = lane & 3, qq = (lane & 15) >> 2;
  const int sw = (((qq >> 1) & 1) | ((g & 1) << 1)) << 1;
  const char* base[2];
#pragma unroll
  for (int n = 0; n < 2; ++n) base[n] = buf + (8 * g + qq) * 128 + ((2 * (2 * nh + n) + (pp >> 1)) ^ sw) * 16 + (pp & 1) * 8;
  u32x2 lo[2][2], hi[2][2];
  auto rd = [&](auto T, auto N) { constexpr int t = T, n = N;
    if constexpr (t < KS1) {
      lo[t & 1][n] = __builtin_bit_cast(u32x2, __builtin_amdgcn_ds_read_tr16_b64_v4bf16((LAS bf16x4*)(base[n] + t * 4096)));
      hi[t & 1][n] = __builtin_bit_cast(u32x2, __builtin_amdgcn_ds_read_tr16_b64_v4bf16((LAS bf16x4*)(base[n] + t * 4096 + 512)));
    } };
  rd(IC<0>{}, IC<0>{}); rd(IC<0>{}, IC<1>{});
  static_for<0, KS1>([&](auto T) {
    constexpr int t = T;
    rd(IC<t + 1>{}, IC<0>{}); rd(IC<t + 1>{}, IC<1>{});
    __builtin_amdgcn_s_waitcnt(0xC07F | ((t + 1 < KS1 ? 4 : 0) << 8));
    SB();
    static_for<0, 2>([&](auto N) {
      constexpr int n = N;
      const u32x4 b = u32x4{lo[t & 1][n][0], lo[t & 1][n][1], hi[t & 1][n][0], hi[t & 1][n][1]};
      acc[0][n] = mfma(q[0][t], b, acc[0][n]);
      acc[1][n] = mfma(q[1][t], b, acc[1][n]);
      SB();
      hook(T, N);
      SB();
    });
  });
}

DI u32x4 row_frag(const char* buf, int n, int t, int lane) {
  const int row = lane & 15, g = lane >> 4, x = (((4 * (t & 3) + g) ^ row) * 16);
  return *(const u32x4*)(buf + n * 20480 + (t < 16 ? row * 1024 + (t >> 2) * 256 : 16384 + row * 256) + x);
}
typedef __attribute__((ext_vector_type(2))) unsigned int v2u_t;
#define HAUX 16
DI void bst8(__amdgpu_buffer_rsrc_t rs, u32 voff, u32 soff, const f32x4& a) {
  __builtin_amdgcn_raw_buffer_store_b64(v2u_t{pk2(a[0], a[1]), pk2(a[2], a[3])}, rs, voff, soff, 0);
}
template <class HK>
DI void qjob_compute(const Params& P, int job, int ct5, int mp, int nh, int lane, const char* buf, u32x4 (&q)[2][KS1], HK&& hook) {
  asm volatile("" : "+s"(job), "+s"(ct5), "+s"(mp), "+s"(nh), "+v"(lane));
  const int ms0 = ct5 * 8 + 2 * mp, tc = 4 * ct5 + mp;
  f32x4 acc[2][2], sum = {0.f, 0.f, 0.f, 0.f}, gram = sum;
  acc[0][0] = sum; acc[0][1] = sum; acc[1][0] = sum; acc[1][1] = sum;
  const char* bl = buf + nh * 40960;
  const __amdgpu_buffer_rsrc_t rsw = __builtin_amdgcn_make_buffer_rsrc((void*)P.ws, 0, (int)WS_END, 0x00020000);
  auto vXf = [&]() { asm volatile("" : "+v"(lane)); return (u32)(lane >> 5) * 32768u + (u32)((lane >> 4) & 1) * 8u + (u32)(lane & 15) * 16u; };
  auto vWf = [&]() { asm volatile("" : "+v"(lane)); return ((u32)(lane >> 5) * 16u + (u32)(lane & 15)) * 16u + (u32)((lane >> 4) & 1) * 8u; };
  if (job < 32) {
    if (ct5 < 4 && mp == 3 && nh == (ct5 >> 1)) {
      qloop_lin<1>(bl, ct5 & 1, lane, q, acc, sum, gram, hook);
      const int c16 = lane & 15, g = lane >> 4, r = c16 & 3;
      const float s1 = r == 0 ? sum[0] : r == 1 ? sum[1] : r == 2 ? sum[2] : sum[3];
      const float s2 = r == 0 ? gram[0] : r == 1 ? gram[1] : r == 2 ? gram[2] : gram[3];
      const float m = s1 * (1.f / 640.f);
      float var = s2 * (1.f / 640.f) - m * m; var = var < 0.f ? 0.f : var;
      if (g == (c16 >> 2)) ((float*)(P.ws + OFF_RSTD0))[job * 64 + ct5 * 16 + c16] = rcp(ln_den(var));
    } else {
      qloop_lin<0>(bl, 0, lane, q, acc, sum, gram, hook);
    }
    const u32 vX = vXf();
#pragma unroll
    for (int a = 0; a < 2; ++a) {
      const u32 so = (u32)OFF_X0 + (u32)(2 * (ms0 + a) * 2048 + job * 64 + nh * 32) * 16u;
      bst8(rsw, vX, so, acc[a][0]); bst8(rsw, vX, so + 256u, acc[a][1]);
    }
  } else if (job < 59) {
    qloop_tr(buf, nh, lane, q, acc, hook);
    const u32 vW = vWf();
#pragma unroll
    for (int n = 0; n < 2; ++n) {
      const int fs = (job - 32) * 4 + 2 * nh + n;
      if (ms0 == 0 && lane < 16) acc[0][n][1] = 0.f;
      const int ft = fs < 84 ? fs / 7 : 12 + (fs - 84) / 6, m = fs < 84 ? fs - 7 * ft : (fs - 84) - 6 * (ft - 12);
      const u32 so = (u32)OFF_A0 + (u32)((ft * KS1 + tc) * 7 + m) * 1024u;
      bst8(rsw, vW, so, acc[0][n]); bst8(rsw, vW, so + 512u, acc[1][n]);
    }
  } else if (job < 86) {
    qloop_lin<2>(bl, 0, lane, q, acc, sum, gram, hook);
    const u32 vW = vWf();
#pragma unroll
    for (int a = 0; a < 2; ++a) {
      const int ms = ms0 + a, ct = ms / 5;
      const u32x4 id = ident_frag(a, lane);
#pragma unroll
      for (int n = 0; n < 2; ++n) {
        const f32x4 tr = mfma(row_frag(bl, n, tc, lane), id, f32x4{0.f, 0.f, 0.f, 0.f});
        const int fs = (job - 59) * 4 + 2 * nh + n;
        const u32 o = (u32)((ct * KS2 + (fs >> 1)) * 5 + (ms - 5 * ct)) * 1024u + (u32)(fs & 1) * 512u;
        bst8(rsw, vW, (u32)OFF_W2F + o, acc[a][n]);
        bst8(rsw, vW, (u32)OFF_W2T + o, tr);
      }
    }
  } else if (job == 86) {
    if (nh == 0) {
#pragma unroll
      for (int t = 0; t < KS1; ++t) { const u32x4 b = row_frag(buf, 0, t, lane); acc[0][0] = mfma(b, q[0][t], acc[0][0]); acc[1][0] = mfma(b, q[1][t], acc[1][0]); }
      if (lane < 16) { ((float*)(P.ws + OFF_B2F))[ms0 * 16 + lane] = acc[0][0][0]; ((float*)(P.ws + OFF_B2F))[ms0 * 16 + 16 + lane] = acc[1][0][0]; }
    }
  } else {
    const u32x4 id = ident_frag(nh, lane);
    const u32 vW = vWf();
    static_for<0, KS1>([&](auto T) {
      constexpr int t = T;
      const u32 fm = t < 12 ? (u32)(t * KS1 * 4) + (u32)nh : (u32)((12 + (t - 12) / 2) * KS1 * 4 + ((t - 12) & 1) * 2) + (u32)nh;
      const u32 so = (u32)OFF_A1 + (fm + (u32)tc * 4u) * 1024u;
      bst8(rsw, vW, so, mfma(q[0][t], id, f32x4{0.f, 0.f, 0.f, 0.f}));
      bst8(rsw, vW, so + 512u, mfma(q[1][t], id, f32x4{0.f, 0.f, 0.f, 0.f}));
    });
  }
}

__global__ void __launch_bounds__(512, 2) k0_prep(Params P) {
  extern __shared__ __attribute__((aligned(16))) char smem[];
  const int b = blockIdx.x, tid = threadIdx.x, lane = tid & 63, w8 = __builtin_amdgcn_readfirstlane(tid >> 6);
  RSTAMP(0, 14); STAMP(0, 0);
  if (b < 240) {
    const int li = b >> 3, ct5 = li % 5, j0 = (b & 7) * 6 + li / 5, j1 = j0 + 48;
    const bool two = j1 < 88;
    const int mp = w8 & 3, nh = w8 >> 2;
    const u32 lb = lds_addr(smem);
    JobOps J;
    job_ops(J, P, j0, w8, lane, lb);
    static_for<0, 4>([&](auto K) { job_op<K>(J); asm volatile("s_nop 0"); });
    const int WT = b < 160 ? b : -1, wa = WT & 15, wcg = (WT >= 0) ? (WT >> 4) : 0;
    u32x2 wv[2][2];
    u32 w0r, w0i;
    u32x4 q[2][KS1];
    QGen G0;
    constexpr int GL = 4;
    {
      QGen G1;
      gen_q_init(G1, ct5 * 8 + 2 * mp + 1, lane);
      static_for<0, 6>([&](auto T) { constexpr int t = T; SB(); job_op<4 + t>(J); SB(); q[1][t] = gen_q_step(G1); });
      static_for<6, KS1>([&](auto T) { constexpr int t = T; q[1][t] = gen_q_step(G1); });
#pragma unroll
      for (int t = 0; t < KS1; ++t) asm volatile("" : "+v"(q[1][t]));
    }
    job_ops(J, P, two ? j1 : 86, w8, lane, lb + 81920);
    static_for<0, 4>([&](auto K) { job_op<K>(J); asm volatile("s_nop 0"); });
    SB();
    gen_q_init(G0, ct5 * 8 + 2 * mp, lane);
    static_for<0, GL>([&](auto T) { constexpr int t = T; SB(); job_op<4 + t>(J); SB(); q[0][t] = gen_q_step(G0); });
    SB(); job_op<8>(J); asm volatile("s_nop 0"); job_op<9>(J); SB();
    STAMP(0, 1);
    asm volatile("s_waitcnt vmcnt(10)\n\ts_barrier" ::: "memory");
    STAMP(0, 2);
    qjob_compute(P, j0, ct5, mp, nh, lane, smem, q, [&](auto T, auto N) {
      constexpr int t = T, n = N;
      if constexpr (t + GL < KS1 && n == 0) q[0][t + GL] = gen_q_step(G0);
    });
    {
      const i32x4 rwr = mk_rsrc(P.wr, S_ * DF_ * 2), rwi = mk_rsrc(P.wi, S_ * DF_ * 2);
      const u32 wvo = (u32)(lane >> 3) * (DF_ * 2) + (u32)(lane & 7) * 8u + (u32)wcg * 64u;
      const u32 wso = (u32)((16 * nh + 32 * (4 * wa + mp)) * DF_ * 2);
      bld8(wv[0][0], rwr, wvo, wso); bld8(wv[0][1], rwi, wvo, wso);
      bld8(wv[1][0], rwr, wvo, wso + 8 * DF_ * 2); bld8(wv[1][1], rwi, wvo, wso + 8 * DF_ * 2);
      bldu16(w0r, rwr, (u32)(lane & 15) * (DF_ * 2) + 640u, wso); bldu16(w0i, rwi, (u32)(lane & 15) * (DF_ * 2) + 640u, wso);
    }
    STAMP(0, 3);
    asm volatile("s_waitcnt vmcnt(10) lgkmcnt(0)\n\ts_barrier" ::: "memory");
    STAMP(0, 4);
    if (two) qjob_compute(P, j1, ct5, mp, nh, lane, smem + 81920, q, [&](auto T, auto N) {});
    STAMP(0, 5);
    asm volatile("s_cmp_lg_u32 %6, 0\n\ts_cbranch_scc1 1f\n\ts_waitcnt vmcnt(0)\n1:\n\ts_waitcnt vmcnt(4)"
                 : "+v"(wv[0][0]), "+v"(wv[0][1]), "+v"(wv[1][0]), "+v"(wv[1][1]), "+v"(w0r), "+v"(w0i) : "s"(RFL((two && j1 != 86) ? 1 : 0)) : "memory", "scc");
    if (WT >= 0) {
      u32* st = (u32*)smem;
#pragma unroll
      for (int i = 0; i < 2; ++i) {
        const u32 r0 = wv[i][0][0], r1 = wv[i][0][1], i0 = wv[i][1][0], i1 = wv[i][1][1];
        u32* d = st + (4 * (lane & 7)) * 132 + (16 * nh + 8 * i + (lane >> 3)) * 4 + mp;
        d[0] = (r0 & 0xffffu) | (i0 << 16); d[132] = (r0 >> 16) | (i0 & 0xffff0000u);
        d[264] = (r1 & 0xffffu) | (i1 << 16); d[396] = (r1 >> 16) | (i1 & 0xffff0000u);
      }
      if (wcg == 0) {
        asm volatile("s_waitcnt lgkmcnt(0)\n\ts_barrier" ::: "memory");
        if (lane < 16) st[(16 * nh + lane) * 4 + mp] = w0r | (w0i << 16);
      }
      asm volatile("s_waitcnt lgkmcnt(0)\n\ts_barrier" ::: "memory");
      const int v = wa >> 3, ii = (wa >> 2) & 1, g = wa & 3;
#pragma unroll
      for (int k = 0; k < 2; ++k) {
        const int it = k * 512 + tid, cl = it >> 5, k1 = it & 31;
        const u32x4 o = *(const u32x4*)(st + cl * 132 + k1 * 4);
        *(u32x4*)(P.ws + OFF_WT + ((((size_t)(32 * wcg + cl) * 4 + (k1 >> 4) * 2 + v) * 2 + ii) * 64 + g * 16 + (k1 & 15)) * 16) = o;
      }
    }
    STAMP(0, 10); RSTAMP(0, 15);
  } else if (b == 240) {
    const int c16 = lane & 15, g = lane >> 4;
    for (int fr = w8; fr < 20; fr += 8) {
      float v[8];
      if (fr < 16) {
        const int tile = fr >> 2, t = (fr >> 1) & 1, part = fr & 1;
#pragma unroll
        for (int j = 0; j < 8; ++j) {
          const int kk = 32 * t + 16 * (j >> 2) + 4 * g + (j & 3);
          const float a = (float)(((16 * tile + c16) * kk) & 63) * (1.f / 64.f);
          v[j] = part ? -sin2pi(a) : cos2pi(a);
        }
      } else {
        const int u = (fr - 16) >> 1, part = fr & 1;
#pragma unroll
        for (int j = 0; j < 8; ++j) {
          const int kk = 16 * (j >> 2) + 4 * g + (j & 3);
          const float a = (float)(((16 * u + c16) * kk) & 31) * (1.f / 32.f);
          v[j] = part ? -sin2pi(a) : cos2pi(a);
        }
      }
      *(u32x4*)(P.ws + OFF_TAB + fr * 1024 + lane * 16) = u32x4{pk2(v[0], v[1]), pk2(v[2], v[3]), pk2(v[4], v[5]), pk2(v[6], v[7])};
    }
  } else if (b == 241) {
    for (int i = tid; i < 2048; i += 512) { ((float*)(P.ws + OFF_SS2))[i] = 0.f; ((u32*)(P.ws + OFF_CNT))[i] = 0u; }
  }
}

template <int LAYER>
__global__ void __launch_bounds__(512, 2) k_col(Params P) {
  extern __shared__ __attribute__((aligned(16))) char smem[];
  const int b = blockIdx.x, tid = threadIdx.x, lane = tid & 63, w = __builtin_amdgcn_readfirstlane(tid >> 6);
  char* ws = P.ws;
  constexpr int KID = LAYER == 0 ? 1 : 4;
  RSTAMP(KID, 14); STAMP(KID, 0);
  const int oct = (b & 7) + 8 * (b >> 4), hf = (b >> 3) & 1;
  char* sA = smem;
  char* sT = smem + 16384;
  char* sE = smem + 36864;
  char* sG = smem + 53248;
  char* sY = smem + 69632;
  const int cc = w >> 2, u = (w >> 1) & 1, v = w & 1, c16 = lane & 15, g = lane >> 4;
  const int col = oct * 4 + hf * 2 + cc;
  const i32x4 rsw = mk_rsrc(ws, (u32)WS_END);
  const u32 l16 = lane * 16;
  dma16<0>(rsw, l16, (u32)OFF_TAB + w * 1024u, lds_addr(sT) + w * 1024u);
  dma16<0>(rsw, l16, (u32)OFF_TAB + (8 + w) * 1024u, lds_addr(sT) + (8 + w) * 1024u);
  dma16<0>(rsw, l16, (u32)OFF_TAB + (16 + (w & 3)) * 1024u, lds_addr(sT) + (16 + (w & 3)) * 1024u);
  u32x2 raw[4]; u32 st4[4]; u32x4 wv[2];
  const u32 xo = (u32)(LAYER == 0 ? OFF_X0 : OFF_X2) + (u32)oct * 32768u, xv = (u32)tid * 16u + (u32)hf * 8u;
#pragma unroll
  for (int r = 0; r < 4; ++r) bld8(raw[r], rsw, xv, xo + r * 8192u);
#pragma unroll
  for (int r = 0; r < 4; ++r) bld4(st4[r], rsw, (u32)tid * 4u, (u32)(LAYER == 0 ? OFF_RSTD0 : OFF_SS2) + r * 2048u);
  {
    const u32 wo = (u32)OFF_WT + (u32)((col * 4 + u * 2 + v) * 2) * 1024u;
    bld16<0>(wv[0], rsw, l16, wo);
    bld16<1024>(wv[1], rsw, l16, wo);
  }
  SB();
  float twc[16], tws[16], itc[8], its[8];
  {
    const float fk = (float)(16 * u + c16) * (1.f / 2048.f);
#pragma unroll
    for (int i = 0; i < 4; ++i)
#pragma unroll
      for (int r = 0; r < 4; ++r) {
        const float a = (float)(16 * i + 4 * g + r) * fk;
        twc[i * 4 + r] = cos2pi(a); tws[i * 4 + r] = sin2pi(a);
      }
#pragma unroll
    for (int ii = 0; ii < 2; ++ii) {
      const float fm = (float)(16 * (2 * v + ii) + c16) * (1.f / 2048.f);
#pragma unroll
      for (int r = 0; r < 4; ++r) {
        const float a = (float)(16 * u + 4 * g + r) * fm;
        itc[ii * 4 + r] = cos2pi(a) * (1.f / 2048.f); its[ii * 4 + r] = sin2pi(a) * (1.f / 2048.f);
      }
    }
#pragma unroll
    for (int k = 0; k < 16; ++k) asm volatile("" : "+v"(twc[k]), "+v"(tws[k]));
#pragma unroll
    for (int k = 0; k < 8; ++k) asm volatile("" : "+v"(itc[k]), "+v"(its[k]));
  }
  STAMP(KID, 1);
  asm volatile("s_waitcnt vmcnt(10)\n\ts_barrier" ::: "memory");
  u32x4 f64r[2][2], f64i[2][2], f64n[2][2], f32r, f32i, f32n;
#pragma unroll
  for (int ii = 0; ii < 2; ++ii)
#pragma unroll
    for (int t = 0; t < 2; ++t) {
      f64r[ii][t] = *(const u32x4*)(sT + (((2 * v + ii) * 2 + t) * 2 + 0) * 1024 + l16);
      f64i[ii][t] = *(const u32x4*)(sT + (((2 * v + ii) * 2 + t) * 2 + 1) * 1024 + l16);
      f64n[ii][t] = f64i[ii][t] ^ 0x80008000u;
    }
  f32r = *(const u32x4*)(sT + (16 + u * 2 + 0) * 1024 + l16);
  f32i = *(const u32x4*)(sT + (16 + u * 2 + 1) * 1024 + l16);
  f32n = f32i ^ 0x80008000u;
#pragma unroll
  for (int ii = 0; ii < 2; ++ii)
#pragma unroll
    for (int t = 0; t < 2; ++t) asm volatile("" : "+v"(f64r[ii][t]), "+v"(f64i[ii][t]), "+v"(f64n[ii][t]));
  asm volatile("" : "+v"(f32r), "+v"(f32i), "+v"(f32n));
  STAMP(KID, 2);
  asm volatile("s_waitcnt vmcnt(2)" : "+v"(raw[0]), "+v"(raw[1]), "+v"(raw[2]), "+v"(raw[3]), "+v"(st4[0]), "+v"(st4[1]), "+v"(st4[2]), "+v"(st4[3]) :: "memory");
  const bool special = (oct == 0 && hf == 0);
  {
    float val[4][4];
#pragma unroll
    for (int r = 0; r < 4; ++r) {
      const float sv = __builtin_bit_cast(float, st4[r]);
      const float rs = (LAYER == 0) ? sv : __builtin_amdgcn_rcpf(ln_den(sv * (1.f / 640.f)));
      val[r][0] = bflo(raw[r][0]) * rs; val[r][1] = special ? 0.f : bfhi(raw[r][0]) * rs;
      val[r][2] = bflo(raw[r][1]) * rs; val[r][3] = bfhi(raw[r][1]) * rs;
    }
    const int ti = tid >> 7, tg = (tid >> 5) & 3, tu = (tid >> 4) & 1, tc = tid & 15;
#pragma unroll
    for (int k = 0; k < 4; ++k)
      *(u32x2*)(sA + (k * 4 + ti) * 1024 + (tg * 16 + tc) * 16 + tu * 8) = u32x2{pk2(val[0][k], val[1][k]), pk2(val[2][k], val[3][k])};
  }
  asm volatile("s_waitcnt lgkmcnt(0)\n\ts_barrier" ::: "memory");
  STAMP(KID, 3);
  const f32x4 Z4 = {0.f, 0.f, 0.f, 0.f};
  u32x4 yr[2], yi[2];
  {
    u32x4 xr[4], xi[4];
#pragma unroll
    for (int i = 0; i < 4; ++i) {
      xr[i] = *(const u32x4*)(sA + ((cc * 2 + 0) * 4 + i) * 1024 + l16);
      xi[i] = *(const u32x4*)(sA + ((cc * 2 + 1) * 4 + i) * 1024 + l16);
    }
    f32x4 dr[4], di[4];
#pragma unroll
    for (int i = 0; i < 4; ++i) { dr[i] = mfma(xr[i], f32r, Z4); di[i] = mfma(xr[i], f32i, Z4); }
#pragma unroll
    for (int i = 0; i < 4; ++i) { dr[i] = mfma(xi[i], f32n, dr[i]); di[i] = mfma(xi[i], f32r, di[i]); }
    float tr[16], tq[16];
#pragma unroll
    for (int i = 0; i < 4; ++i)
#pragma unroll
      for (int r = 0; r < 4; ++r) {
        tr[i * 4 + r] = dr[i][r] * twc[i * 4 + r] + di[i][r] * tws[i * 4 + r];
        tq[i * 4 + r] = di[i][r] * twc[i * 4 + r] - dr[i][r] * tws[i * 4 + r];
      }
#pragma unroll
    for (int t = 0; t < 2; ++t) {
      yr[t] = u32x4{pk2(tr[8 * t + 0], tr[8 * t + 1]), pk2(tr[8 * t + 2], tr[8 * t + 3]), pk2(tr[8 * t + 4], tr[8 * t + 5]), pk2(tr[8 * t + 6], tr[8 * t + 7])};
      yi[t] = u32x4{pk2(tq[8 * t + 0], tq[8 * t + 1]), pk2(tq[8 * t + 2], tq[8 * t + 3]), pk2(tq[8 * t + 4], tq[8 * t + 5]), pk2(tq[8 * t + 6], tq[8 * t + 7])};
    }
  }
  u32x4 zr[2], zi[2];
  {
    f32x4 dr[2] = {Z4, Z4}, di[2] = {Z4, Z4};
#pragma unroll
    for (int t = 0; t < 2; ++t) {
#pragma unroll
      for (int ii = 0; ii < 2; ++ii) { dr[ii] = mfma(f64r[ii][t], yr[t], dr[ii]); di[ii] = mfma(f64i[ii][t], yr[t], di[ii]); }
#pragma unroll
      for (int ii = 0; ii < 2; ++ii) { dr[ii] = mfma(f64n[ii][t], yi[t], dr[ii]); di[ii] = mfma(f64r[ii][t], yi[t], di[ii]); }
    }
    asm volatile("s_waitcnt vmcnt(0)" : "+v"(wv[0]), "+v"(wv[1]) :: "memory");
    float pr[8], pi[8];
#pragma unroll
    for (int ii = 0; ii < 2; ++ii)
#pragma unroll
      for (int r = 0; r < 4; ++r) {
        const u32 wd = wv[ii][r];
        const float a = bflo(wd), bb = bfhi(wd);
        pr[ii * 4 + r] = dr[ii][r] * a - di[ii][r] * bb;
        pi[ii * 4 + r] = dr[ii][r] * bb + di[ii][r] * a;
      }
    const u32x4 mr = u32x4{pk2(pr[0], pr[1]), pk2(pr[2], pr[3]), pk2(pr[4], pr[5]), pk2(pr[6], pr[7])};
    const u32x4 mi = u32x4{pk2(pi[0], pi[1]), pk2(pi[2], pi[3]), pk2(pi[4], pi[5]), pk2(pi[6], pi[7])};
    *(u32x4*)(sE + ((((cc * 2 + u) * 2 + v) * 2 + 0) * 1024) + l16) = mr;
    *(u32x4*)(sE + ((((cc * 2 + u) * 2 + v) * 2 + 1) * 1024) + l16) = mi;
    STAMP(KID, 4);
    asm volatile("s_waitcnt lgkmcnt(0)\n\ts_barrier" ::: "memory");
    const u32x4 or_ = *(const u32x4*)(sE + ((((cc * 2 + u) * 2 + (1 - v)) * 2 + 0) * 1024) + l16);
    const u32x4 oi_ = *(const u32x4*)(sE + ((((cc * 2 + u) * 2 + (1 - v)) * 2 + 1) * 1024) + l16);
    zr[0] = v ? or_ : mr; zi[0] = v ? oi_ : mi;
    zr[1] = v ? mr : or_; zi[1] = v ? mi : oi_;
  }
  {
    f32x4 dr[2] = {Z4, Z4}, di[2] = {Z4, Z4};
#pragma unroll
    for (int t = 0; t < 2; ++t) {
#pragma unroll
      for (int ii = 0; ii < 2; ++ii) { dr[ii] = mfma(zr[t], f64r[ii][t], dr[ii]); di[ii] = mfma(zr[t], f64n[ii][t], di[ii]); }
#pragma unroll
      for (int ii = 0; ii < 2; ++ii) { dr[ii] = mfma(zi[t], f64i[ii][t], dr[ii]); di[ii] = mfma(zi[t], f64r[ii][t], di[ii]); }
    }
#pragma unroll
    for (int ii = 0; ii < 2; ++ii) {
      float tr[4], tq[4];
#pragma unroll
      for (int r = 0; r < 4; ++r) {
        tr[r] = dr[ii][r] * itc[ii * 4 + r] - di[ii][r] * its[ii * 4 + r];
        tq[r] = dr[ii][r] * its[ii * 4 + r] + di[ii][r] * itc[ii * 4 + r];
      }
      *(u32x2*)(sG + ((((cc * 2 + v) * 2 + ii) * 2 + 0) * 1024) + l16 + u * 8) = u32x2{pk2(tr[0], tr[1]), pk2(tr[2], tr[3])};
      *(u32x2*)(sG + ((((cc * 2 + v) * 2 + ii) * 2 + 1) * 1024) + l16 + u * 8) = u32x2{pk2(tq[0], tq[1]), pk2(tq[2], tq[3])};
    }
  }
  STAMP(KID, 5);
  asm volatile("s_waitcnt lgkmcnt(0)\n\ts_barrier" ::: "memory");
  {
    u32x4 tr[2], tq[2];
#pragma unroll
    for (int ii = 0; ii < 2; ++ii) {
      tr[ii] = *(const u32x4*)(sG + ((((cc * 2 + v) * 2 + ii) * 2 + 0) * 1024) + l16);
      tq[ii] = *(const u32x4*)(sG + ((((cc * 2 + v) * 2 + ii) * 2 + 1) * 1024) + l16);
    }
    f32x4 orr[2], oi[2];
#pragma unroll
    for (int ii = 0; ii < 2; ++ii) { orr[ii] = mfma(f32r, tr[ii], Z4); oi[ii] = mfma(f32r, tq[ii], Z4); }
#pragma unroll
    for (int ii = 0; ii < 2; ++ii) { orr[ii] = mfma(f32i, tq[ii], orr[ii]); oi[ii] = mfma(f32n, tr[ii], oi[ii]); }
#pragma unroll
    for (int ii = 0; ii < 2; ++ii) {
      const int tt = (2 * v + ii) * 128 + g * 32 + u * 16 + c16;
      *(u32x4*)(sY + (cc * 512 + tt) * 16) = u32x4{pk2(orr[ii][0], oi[ii][0]), pk2(orr[ii][1], oi[ii][1]), pk2(orr[ii][2], oi[ii][2]), pk2(orr[ii][3], oi[ii][3])};
    }
  }
  STAMP(KID, 6);
  asm volatile("s_waitcnt lgkmcnt(0)\n\ts_barrier" ::: "memory");
  {
    char* Xout = ws + (LAYER == 0 ? OFF_X1 : OFF_X3);
    const u32x4 ya = *(const u32x4*)(sY + tid * 16), yb = *(const u32x4*)(sY + (512 + tid) * 16);
#pragma unroll
    for (int r = 0; r < 4; ++r) {
      const u32 a = ya[r], bq = yb[r], r0 = raw[r][0], r1 = raw[r][1];
      const float v0 = bflo(r0) + bflo(a);
      const float v1 = special ? bfhi(r0) : bfhi(r0) + bfhi(a);
      const float v2 = bflo(r1) + bflo(bq), v3 = bfhi(r1) + bfhi(bq);
      *(u32x2*)(Xout + ((size_t)oct * 2048 + r * 512 + tid) * 16 + hf * 8) = u32x2{pk2(v0, v1), pk2(v2, v3)};
    }
  }
  STAMP(KID, 7); RSTAMP(KID, 15);
}

DI f32x2 gelu2(f32x2 x) {
  const float k1 = -2.3022081978378774f;
  const float k2 = k1 * 0.044715f;
  const f32x2 t = x * x;
  const f32x2 u = t * k2 + k1;
  const f32x2 z = x * u;
  f32x2 d = f32x2{__builtin_amdgcn_exp2f(z[0]), __builtin_amdgcn_exp2f(z[1])} + 1.f;
  return x * f32x2{__builtin_amdgcn_rcpf(d[0]), __builtin_amdgcn_rcpf(d[1])};
}
template <int IMM> DI void dma16_nom0(i32x4 rsrc, u32 voff, u32 soff) {
  asm volatile(PADS "buffer_load_dwordx4 %0, %1, %2 offen offset:%3 lds" ::"v"(voff), "s"(rsrc), "s"(RFL(soff)), "i"(IMM) : "memory");
}
template <int N> DI void wait_vm_bar(u32x4& a, u32x4& b, u32x4& c, u32x4& d, u32x4& e, u32x4& f, u32x4& g, u32x4& h) { wait_vm_bar8<N>(a, b, c, d, e, f, g, h); }

template <int LAYER, int NF, int NQ, class MH, class EH>
DI void gemm1_body(const Params& P, char* smem, int ft, int tt, int lane, int w, MH&& tail_hook, EH&& epi_hook) {
  constexpr int KID = LAYER == 0 ? 2 : 5;
  constexpr int NM = NF + NQ, H = (NM + 1) / 2, RING = (LAYER == 0) ? 20 : 16, BR = 12;
  const int c16 = lane & 15, g = lane >> 4;
  char* ws = P.ws;
  const i32x4 rs = mk_rsrc(ws, (u32)WS_END);
  const u32 l16 = lane * 16;
  const int p0 = tt * 128 + w * 32;
  const int f0 = (ft < 12) ? 7 * ft : 84 + 6 * (ft - 12);
  u32x2 bw[NF];
#pragma unroll
  for (int m = 0; m < NF; ++m) bw[m] = *(const u32x2*)(P.b1 + (f0 + m) * 16 + 4 * g);
  u32x4 bq[BR][2];
  const u32 xoff = (u32)(LAYER == 0 ? OFF_X1 : OFF_X3) + (u32)p0 * 16;
  const u32 bvo = (u32)g * 32768u + (u32)c16 * 16u;
  const u32 asrc = (u32)OFF_A0 + (u32)(ft * KS1 + w) * 7168u, qsrc = (u32)OFF_A1 + (u32)(ft * KS1 + w) * 4096u;
  const u32 adst = lds_addr(smem) + (u32)w * (NM * 1024);
  constexpr int PER = NM + 8;
  auto issue = [&](auto C, auto K) {
    constexpr int c = C, k = K;
    if constexpr (k < NF) {
      constexpr int k4 = k & ~3;
      const u32 so = asrc + (u32)(c * 4 * 7 + k4) * 1024u;
      if constexpr ((k & 3) == 0) dma16<0>(rs, l16, so, adst + (u32)((((c * 4) % RING) * NM + k4) * 1024));
      else dma16_nom0<(k & 3) * 1024>(rs, l16, so);
    } else if constexpr (k < NM) {
      constexpr int kq = k - NF;
      const u32 so = qsrc + (u32)(c * 4 * 4) * 1024u;
      if constexpr (kq == 0) dma16<0>(rs, l16, so, adst + (u32)((((c * 4) % RING) * NM + NF) * 1024));
      else dma16_nom0<kq * 1024>(rs, l16, so);
    } else {
      constexpr int kk = (k - NM) >> 1, t = 4 * c + kk;
      const u32 so = xoff + (u32)t * 131072u;
      if constexpr (((k - NM) & 1) == 0) bld16<0>(bq[t % BR][0], rs, bvo, so);
      else bld16<256>(bq[t % BR][1], rs, bvo, so);
    }
  };
  static_for<0, 2>([&](auto C) { static_for<0, PER>([&](auto K) { issue(C, K); }); });
  STAMP(KID, 1);
  f32x4 acc[NM][2], gram[2];
#pragma unroll
  for (int m = 0; m < NM; ++m) { acc[m][0] = f32x4{0.f, 0.f, 0.f, 0.f}; acc[m][1] = acc[m][0]; }
  gram[0] = gram[1] = f32x4{0.f, 0.f, 0.f, 0.f};
  u32x4 af[3][H];
  auto waitchunk = [&](auto C, auto N) {
    constexpr int c = C;
    wait_vm_bar<N.value>(bq[(4 * c) % BR][0], bq[(4 * c) % BR][1], bq[(4 * c + 1) % BR][0], bq[(4 * c + 1) % BR][1], bq[(4 * c + 2) % BR][0],
                         bq[(4 * c + 2) % BR][1], bq[(4 * c + 3) % BR][0], bq[(4 * c + 3) % BR][1]);
  };
  auto rd1 = [&](auto HH, auto K) {
    constexpr int h = HH, k = K, t = h >> 1, m0 = (h & 1) ? H : 0, m1 = (h & 1) ? NM : H;
    if constexpr (t < KS1 && m0 + k < m1) af[h % 3][k] = *(const u32x4*)(smem + ((t % RING) * NM + m0 + k) * 1024 + l16);
  };
  waitchunk(IC<0>{}, IC<PER>{});
  f32x2 bb[NF][2];
#pragma unroll
  for (int m = 0; m < NF; ++m) {
    bb[m][0] = f32x2{bflo(bw[m][0]), bfhi(bw[m][0])}; bb[m][1] = f32x2{bflo(bw[m][1]), bfhi(bw[m][1])};
    asm volatile("" : "+v"(bb[m][0]), "+v"(bb[m][1]));
  }
  STAMP(KID, 2);
  static_for<0, H>([&](auto K) { rd1(IC<0>{}, K); });
  static_for<0, H>([&](auto K) { rd1(IC<1>{}, K); });
  static_for<0, 2 * KS1>([&](auto HH) {
    constexpr int h = HH, t = h >> 1, c = t >> 2, q = t & 3, m0 = (h & 1) ? H : 0, m1 = (h & 1) ? NM : H;
    constexpr int PS = (PER + 2) / 3;
    if constexpr ((h & 1) == 0 && q == 3 && t + 1 < KS1) waitchunk(IC<c + 1>{}, IC<(c + 1 < 4 ? PER : 6)>{});
    constexpr int n1 = (h + 1 < 2 * KS1) ? (((h + 1) & 1) ? NM - H : H) : 0;
    __builtin_amdgcn_s_waitcnt(0xC07F | (n1 << 8));
    SB();
    static_for<m0, m1>([&](auto M) {
      constexpr int m = M;
      acc[m][0] = mfma(af[h % 3][m - m0], bq[t % BR][0], acc[m][0]);
      SB();
      rd1(IC<h + 2>{}, IC<m - m0>{});
      SB();
      acc[m][1] = mfma(af[h % 3][m - m0], bq[t % BR][1], acc[m][1]);
      SB();
      if constexpr (c + 2 < 5 && q < 3 && m < PS && q * PS + m < PER) issue(IC<c + 2>{}, IC<q * PS + m>{});
      if constexpr (h >= 24 && h < 39 && m == m0) tail_hook(IC<h - 24>{}, IC<((LAYER == 0) ? 0 : 4 * NM)>{});
      SB();
    });
    if constexpr ((h & 1) == 0) {
      u32x4 e0 = bq[t % BR][0], e1 = bq[t % BR][1];
      if constexpr (t == 0) { e0[0] = (g == 0) ? (e0[0] & 0xffffu) : (u32)e0[0]; e1[0] = (g == 0) ? (e1[0] & 0xffffu) : (u32)e1[0]; }
      gram[0] = mfma(e0, e0, gram[0]);
      gram[1] = mfma(e1, e1, gram[1]);
      SB();
    } else if constexpr (NM - H < H) {
      rd1(IC<h + 2>{}, IC<H - 1>{});
    }
  });
  STAMP(KID, 3);
  float a_sc[2];
#pragma unroll
  for (int n = 0; n < 2; ++n) {
    const int r = c16 & 3;
    float dv = r == 0 ? gram[n][0] : r == 1 ? gram[n][1] : r == 2 ? gram[n][2] : gram[n][3];
    dv = (g == (c16 >> 2)) ? dv : 0.f;
    const float var = red_g(dv) * (1.f / 640.f);
    const float d1 = ln_den(var);
    a_sc[n] = rcp(d1 * ln_den(var * rcp(d1 * d1)));
  }
  const auto rsb = __builtin_amdgcn_make_buffer_rsrc((void*)ws, 0, (int)WS_END, 0x00020000);
  const u32 vW = ((u32)(lane >> 5) * 16u + (u32)c16) * 16u + (u32)(g & 1) * 8u;
  const u32 hso = (u32)(LAYER == 0 ? OFF_H1 : OFF_H2) + (u32)((tt * 2 + (w >> 1)) * KS2 * 4 + 2 * (w & 1)) * 1024u;
  static_for<0, NF>([&](auto M) {
    constexpr int m = M;
    const int mg = f0 + m;
    const u32 so = hso + (u32)(mg >> 1) * 4096u + (u32)(mg & 1) * 512u;
    epi_hook(M);
#pragma unroll
    for (int n = 0; n < 2; ++n) {
      const f32x4 a = acc[m][n];
      const f32x2 h01 = gelu2(f32x2{a[0], a[1]} * a_sc[n] + bb[m][0]), h23 = gelu2(f32x2{a[2], a[3]} * a_sc[n] + bb[m][1]);
      __builtin_amdgcn_raw_buffer_store_b64(v2u_t{pk2(h01[0], h01[1]), pk2(h23[0], h23[1])}, rsb, vW, so + n * 1024u, HAUX);
    }
  });
  if constexpr (NQ > 0) {
    const int d0 = (ft < 12) ? 2 * ft : 24 + 4 * (ft - 12);
#pragma unroll
    for (int m = 0; m < NQ; ++m)
#pragma unroll
      for (int n = 0; n < 2; ++n) {
        const f32x4 a = acc[NF + m][n];
        __builtin_amdgcn_raw_buffer_store_b64(v2u_t{pk2(a[0], a[1]), pk2(a[2], a[3])}, rsb, (u32)lane * 8u,
                                              (u32)OFF_X3S + (u32)((d0 + m) * 128 + tt * 8 + w * 2 + n) * 512u, 16);
      }
  }
}

typedef __attribute__((address_space(1))) unsigned gu32;

template <int LAYER>
__global__ void __launch_bounds__(256, 1) k_mlp(Params P) {
  extern __shared__ __attribute__((aligned(16))) char smem[];
  const int b = blockIdx.x, tid = threadIdx.x, lane = tid & 63, w = __builtin_amdgcn_readfirstlane(tid >> 6);
  constexpr int KID = LAYER == 0 ? 2 : 5;
  RSTAMP(KID, 14); STAMP(KID, 0);
  char* ws = P.ws;
  const int x = b & 7, li = b >> 3;
  const int ft = (x & 1) * 8 + (li & 7), tt = (x >> 1) * 4 + (li >> 3);
  const int ct = ft >> 1, t64 = 2 * tt + (ft & 1);
  const u32 l16 = lane * 16;
  const u32 aoff = (u32)(LAYER == 0 ? OFF_W2F : OFF_W2T) + (u32)ct * (KS2 * 5 * 1024) + (u32)w * 5120u;
  const auto rs = __builtin_amdgcn_make_buffer_rsrc((void*)ws, 0, (int)WS_END, 0x00020000);
  const bool full = (w < 2);
  u32x4 rg[11][5];
  auto lda1 = [&](auto I) {
    constexpr int i = I;
    if (i < 10 || full) {
#pragma unroll
      for (int m = 0; m < 4; ++m) rg[i][m] = __builtin_amdgcn_raw_buffer_load_b128(rs, l16 + m * 1024, aoff + (u32)(3 + i) * 20480u, 0);
      rg[i][4] = __builtin_amdgcn_raw_buffer_load_b128(rs, l16, aoff + (u32)(3 + i) * 20480u + 4096u, 0);
    } else {
#pragma unroll
      for (int m = 0; m < 5; ++m) rg[i][m] = u32x4{0u, 0u, 0u, 0u};
    }
  };
  u32 lA;
  {
    const i32x4 rsa = mk_rsrc(ws, (u32)WS_END);
    auto tail = [&](auto I, auto KB) {
      constexpr int i = I, st = i / 5, m = i % 5;
      const u32 lw = lds_addr(smem) + (u32)(KB.value * 1024) + (u32)w * 15360u;
      if constexpr (m == 0) dma16<0>(rsa, l16, aoff + st * 20480u, lw + st * 5120u);
      else if constexpr (m < 4) dma16_nom0<m * 1024>(rsa, l16, aoff + st * 20480u);
      else dma16<0>(rsa, l16, aoff + st * 20480u + 4096u, lw + st * 5120u + 4096u);
    };
    auto epi = [&](auto M) { constexpr int m = M; if constexpr (m < 6) lda1(M); };
    if (ft < 12) { gemm1_body<LAYER, 7, LAYER == 0 ? 0 : 2>(P, smem, ft, tt, lane, w, tail, epi); lA = (LAYER == 0) ? 0 : 4 * 9 * 1024; }
    else { gemm1_body<LAYER, 6, LAYER == 0 ? 0 : 4>(P, smem, ft, tt, lane, w, tail, epi); lA = (LAYER == 0) ? 0 : 4 * 10 * 1024; }
    lA += (u32)w * 15360u;
  }
  STAMP(KID, 4);
  gu32* cnt = (gu32*)(ws + OFF_CNT) + (LAYER * 16 + tt) * 32;
  asm volatile("s_waitcnt vmcnt(0)\n\ts_barrier" ::: "memory");
  if (tid == 0) __hip_atomic_fetch_add(cnt, 1u, __ATOMIC_RELAXED, __HIP_MEMORY_SCOPE_AGENT);
  STAMP(KID, 5);
  const u32 boff = (u32)(LAYER == 0 ? OFF_H1 : OFF_H2) + (u32)t64 * (KS2 * 4096) + (u32)w * 4096u;
  u32 bo[4];
#pragma unroll
  for (int n = 0; n < 4; ++n) bo[n] = boff + (u32)((n + w) & 3) * 1024u;
  constexpr int PD = 5, NSTEP = 14;
  const u32 vX = (u32)(lane >> 5) * 32768u + (u32)((lane >> 4) & 1) * 8u + (u32)(lane & 15) * 16u;
  const u32 xso = (u32)(10 * ct * 2048 + t64 * 64 + w * 16) * 16u;
  v2u_t rz[5]; f32x4 bz[5];
  auto ldbias = [&]() {
#pragma unroll
    for (int m = 0; m < 5; ++m) {
      if (LAYER == 0) {
        rz[m] = __builtin_amdgcn_raw_buffer_load_b64(rs, vX, (u32)OFF_X1 + xso + m * 65536u, 0);
        bz[m] = __builtin_bit_cast(f32x4, __builtin_amdgcn_raw_buffer_load_b128(rs, (u32)(lane >> 4) * 16u, (u32)OFF_B2F + (u32)(ct * 5 + m) * 64u, 0));
      } else {
        const u32x2 bw = *(const u32x2*)(P.b2 + (ct * 5 + m) * 16 + 4 * (lane >> 4));
        bz[m] = f32x4{bflo(bw[0]), bfhi(bw[0]), bflo(bw[1]), bfhi(bw[1])};
      }
    }
  };
  static_for<6, 11>([&](auto I) { lda1(I); });
  ldbias();
  SB();
  if (tid == 0) {
    unsigned spins = 0;
    while (__hip_atomic_load(cnt, __ATOMIC_RELAXED, __HIP_MEMORY_SCOPE_AGENT) < 16u) {
      __builtin_amdgcn_s_sleep(1);
      if (++spins > (1u << 22)) { __hip_atomic_store((gu32*)(ws + OFF_CNT) + 1024, 1u, __ATOMIC_RELAXED, __HIP_MEMORY_SCOPE_AGENT); break; }
    }
  }
  asm volatile("s_barrier" ::: "memory");
  STAMP(KID, 6);
  u32x4 rb[PD][4];
  auto ldb = [&](auto I, auto K) { constexpr int i = I, k = K; rb[i % PD][k] = __builtin_amdgcn_raw_buffer_load_b128(rs, l16, bo[k] + (u32)i * 16384u, 16); };
  static_for<0, PD - 1>([&](auto I) { static_for<0, 4>([&](auto K) { ldb(I, K); }); });
  SB();
  if (LAYER == 1) {
#pragma unroll
    for (int m = 0; m < 5; ++m) rz[m] = __builtin_amdgcn_raw_buffer_load_b64(rs, (u32)lane * 8u, (u32)OFF_X3S + (u32)((ct * 5 + m) * 128 + t64 * 4 + w) * 512u, 16);
  }
  f32x4 acc[5][4];
#pragma unroll
  for (int m = 0; m < 5; ++m)
#pragma unroll
    for (int n = 0; n < 4; ++n) acc[m][n] = f32x4{0.f, 0.f, 0.f, 0.f};
  u32x4 al[2][5];
  const char* lbase = smem + lA + l16;
#pragma unroll
  for (int m = 0; m < 5; ++m) al[0][m] = *(const u32x4*)(lbase + m * 1024);
  SB();
  static_for<0, NSTEP - 1>([&](auto I) {
    constexpr int i = I, nx = i + PD - 1;
    static_for<0, 5>([&](auto M) {
      constexpr int m = M;
#pragma unroll
      for (int n = 0; n < 4; ++n) {
        if constexpr (i < 3) acc[m][n] = mfma(al[i & 1][m], rb[i % PD][n], acc[m][n]);
        else acc[m][n] = mfma(rg[i - 3][m], rb[i % PD][n], acc[m][n]);
      }
      SB();
      if constexpr (i + 1 < 3) al[(i + 1) & 1][m] = *(const u32x4*)(lbase + ((i + 1) * 5 + m) * 1024);
      if constexpr (m < 4) {
        if constexpr (nx < NSTEP - 1) ldb(IC<nx>{}, M);
        else if constexpr (nx == NSTEP - 1) { if (full) ldb(IC<nx>{}, M); }
      }
      SB();
    });
  });
  if (full) {
    constexpr int i = NSTEP - 1;
#pragma unroll
    for (int m = 0; m < 5; ++m)
#pragma unroll
      for (int n = 0; n < 4; ++n) acc[m][n] = mfma(rg[10][m], rb[i % PD][n], acc[m][n]);
  }
  SB();
  STAMP(KID, 7);
  asm volatile("s_barrier" ::: "memory");
  f32x4* red = (f32x4*)smem + lane;
#pragma unroll
  for (int n = 1; n < 4; ++n) {
    f32x4* dst = red + (((n + w) & 3) * 15 + (n - 1) * 5) * 64;
#pragma unroll
    for (int m = 0; m < 5; ++m) dst[m * 64] = acc[m][n];
  }
  asm volatile("s_waitcnt lgkmcnt(0)\n\ts_barrier" ::: "memory");
  float ssq = 0.f;
  const f32x4* src = red + w * 15 * 64;
#pragma unroll
  for (int m = 0; m < 5; ++m) {
    const f32x4 o = (acc[m][0] + src[m * 64]) + (src[(5 + m) * 64] + src[(10 + m) * 64]) +
                    (f32x4{bflo(rz[m][0]), bfhi(rz[m][0]), bflo(rz[m][1]), bfhi(rz[m][1])} + bz[m]);
    const v2u_t ov = v2u_t{pk2(o[0], o[1]), pk2(o[2], o[3])};
    if (LAYER == 0) {
      __builtin_amdgcn_raw_buffer_store_b64(ov, rs, vX, (u32)OFF_X2 + xso + m * 65536u, 0);
      f32x4 q2 = o * o;
      if (m == 0) q2[1] = (ct == 0 && lane < 16) ? 0.f : q2[1];
      ssq += (q2[0] + q2[1]) + (q2[2] + q2[3]);
    } else {
      *(v2u_t*)(P.out + (size_t)s_of_p(t64 * 64 + w * 16 + (lane & 15)) * D_ + (ct * 5 + m) * 16 + 4 * (lane >> 4)) = ov;
    }
  }
  if (LAYER == 0) {
    ssq = red_g(ssq);
    if (lane < 16) atomicAdd((float*)(ws + OFF_SS2) + t64 * 64 + w * 16 + lane, ssq);
  }
  STAMP(KID, 8); RSTAMP(KID, 15);
}

extern "C" void kernel_launch(void* const* d_in, const int* in_sizes, int n_in, void* d_out, int out_size, void* d_ws,
                              size_t ws_size, hipStream_t stream) {
  Params P;
  P.x = (const u16*)d_in[0]; P.wr = (const u16*)d_in[1]; P.wi = (const u16*)d_in[2];
  P.w1 = (const u16*)d_in[4]; P.b1 = (const u16*)d_in[5]; P.w2 = (const u16*)d_in[6]; P.b2 = (const u16*)d_in[7];
  P.out = (u16*)d_out; P.ws = (char*)d_ws;
  {
    hipFuncSetAttribute((const void*)k0_prep, hipFuncAttributeMaxDynamicSharedMemorySize, 163840);
    hipFuncSetAttribute((const void*)k_col<0>, hipFuncAttributeMaxDynamicSharedMemorySize, 163840);
    hipFuncSetAttribute((const void*)k_col<1>, hipFuncAttributeMaxDynamicSharedMemorySize, 163840);
    hipFuncSetAttribute((const void*)k_mlp<0>, hipFuncAttributeMaxDynamicSharedMemorySize, 163840);
    hipFuncSetAttribute((const void*)k_mlp<1>, hipFuncAttributeMaxDynamicSharedMemorySize, 163840);
  }
  k0_prep<<<242, 512, 163840, stream>>>(P);
  k_col<0><<<160, 512, 86016, stream>>>(P);
  k_mlp<0><<<256, 256, 163840, stream>>>(P);
  k_col<1><<<160, 512, 86016, stream>>>(P);
  k_mlp<1><<<256, 256, 163840, stream>>>(P);
}
```

```cpp
#include <hip/hip_runtime.h>
#include <stdint.h>

typedef __bf16 bf16x2 __attribute__((ext_vector_type(2)));
typedef __bf16 bf16x4 __attribute__((ext_vector_type(4)));
typedef __bf16 bf16x8 __attribute__((ext_vector_type(8)));
typedef float f32x2 __attribute__((ext_vector_type(2)));
typedef float f32x4 __attribute__((ext_vector_type(4)));
typedef unsigned u32;
typedef unsigned u32x2 __attribute__((ext_vector_type(2)));
typedef unsigned u32x4 __attribute__((ext_vector_type(4)));
typedef unsigned short u16;
#define LAS __attribute__((address_space(3)))
#define DI __device__ __forceinline__

constexpr int S_ = 2048, D_ = 640, F_ = 1728, DF_ = 321;
constexpr int KS1 = 20;
constexpr int KS2 = 54;
constexpr float EPS = 1e-5f;

constexpr size_t XT_B = (size_t)80 * 2048 * 16;
constexpr size_t H_B = (size_t)128 * KS2 * 1024;
constexpr size_t A0_B = (size_t)16 * KS1 * 7 * 1024;
constexpr size_t A1_B = (size_t)16 * KS1 * 4 * 1024;
constexpr size_t W2F_B = (size_t)8 * KS2 * 5 * 1024;
constexpr size_t WT_B = (size_t)320 * 8192;
constexpr size_t X3S_B = (size_t)40 * 128 * 512;
constexpr size_t OFF_X0 = 0, OFF_X1 = OFF_X0 + XT_B, OFF_X2 = OFF_X1 + XT_B, OFF_X3 = OFF_X2 + XT_B;
constexpr size_t OFF_H1 = OFF_X3 + XT_B, OFF_H2 = OFF_H1 + H_B;
constexpr size_t OFF_A0 = OFF_H2 + H_B, OFF_A1 = OFF_A0 + A0_B;
constexpr size_t OFF_W2F = OFF_A1 + A1_B, OFF_W2T = OFF_W2F + W2F_B;
constexpr size_t OFF_WT = OFF_W2T + W2F_B;
constexpr size_t OFF_X3S = OFF_WT + WT_B;
constexpr size_t OFF_RSTD0 = OFF_X3S + X3S_B;
constexpr size_t OFF_SS2 = OFF_RSTD0 + 8192;
constexpr size_t OFF_B2F = OFF_SS2 + 8192;
constexpr size_t OFF_TAB = OFF_B2F + 4096;
constexpr size_t OFF_CNT = OFF_TAB + 20480;
constexpr size_t WS_END = OFF_CNT + 8192;

DI u32 pk2(float a, float b) { f32x2 v = {a, b}; bf16x2 r = __builtin_convertvector(v, bf16x2); return __builtin_bit_cast(u32, r); }

DI float bflo(u32 w) { return __builtin_bit_cast(float, w << 16); }
DI float bfhi(u32 w) { return __builtin_bit_cast(float, w & 0xffff0000u); }
DI float rbf(float x) { return bflo(pk2(x, x)); }
DI f32x4 mfma(u32x4 a, u32x4 b, f32x4 c) {
  return __builtin_amdgcn_mfma_f32_16x16x32_bf16(__builtin_bit_cast(bf16x8, a), __builtin_bit_cast(bf16x8, b), c, 0, 0, 0);
}
DI float dot2(u32 a, u32 b, float c) { return __builtin_amdgcn_fdot2_f32_bf16(__builtin_bit_cast(bf16x2, a), __builtin_bit_cast(bf16x2, b), c, false); }
DI float cos2pi(float x) { return __builtin_amdgcn_cosf(x); }
DI float sin2pi(float x) { return __builtin_amdgcn_sinf(x); }
DI int s_of_p(int p) {
  const int r = p >> 9, i = (p >> 7) & 3, g = (p >> 5) & 3, uu = (p >> 4) & 1, c16 = p & 15;
  return 64 * (16 * uu + 4 * g + r) + 16 * i + c16;
}
DI void glds16(const void* g, LAS void* l) { __builtin_amdgcn_global_load_lds(g, l, 16, 0, 0); }
#define WAIT_VM0() asm volatile("s_waitcnt vmcnt(0)" ::: "memory")
#define WAIT_LG0() asm volatile("s_waitcnt lgkmcnt(0)" ::: "memory")
#define BAR() __builtin_amdgcn_s_barrier()
DI float ln_den(float var) { return rbf(__builtin_amdgcn_sqrtf(rbf(rbf(var) + EPS))); }
DI float rcp(float x) { return __builtin_amdgcn_rcpf(x); }

typedef int i32x4 __attribute__((ext_vector_type(4)));
DI i32x4 mk_rsrc(const void* p, u32 bytes) {
  const unsigned long long a = (unsigned long long)p;
  return i32x4{(int)__builtin_amdgcn_readfirstlane((u32)a), (int)__builtin_amdgcn_readfirstlane((u32)(a >> 32) & 0xffffu), (int)bytes, 0x00020000};
}
#define RFL(x) __builtin_amdgcn_readfirstlane(x)
#define PADS
template <int IMM> DI void dma16(i32x4 rsrc, u32 voff, u32 soff, u32 lds) {
  asm volatile(PADS "s_mov_b32 m0, %3\n\ts_nop 0\n\tbuffer_load_dwordx4 %0, %1, %2 offen offset:%4 lds" ::"v"(voff), "s"(rsrc), "s"(RFL(soff)), "s"(RFL(lds)), "i"(IMM) : "memory");
}
template <int IMM> DI void bld16(u32x4& dst, i32x4 rsrc, u32 voff, u32 soff) {
  asm volatile(PADS "buffer_load_dwordx4 %0, %1, %2, %3 offen offset:%4" : "=v"(dst) : "v"(voff), "s"(rsrc), "s"(RFL(soff)), "i"(IMM) : "memory");
}
DI void bldu16(u32& dst, i32x4 rsrc, u32 voff, u32 soff) {
  asm volatile(PADS "buffer_load_ushort %0, %1, %2, %3 offen" : "=v"(dst) : "v"(voff), "s"(rsrc), "s"(RFL(soff)) : "memory");
}
DI void bld8(u32x2& dst, i32x4 rsrc, u32 voff, u32 soff) {
  asm volatile(PADS "buffer_load_dwordx2 %0, %1, %2, %3 offen" : "=v"(dst) : "v"(voff), "s"(rsrc), "s"(RFL(soff)) : "memory");
}
DI void bld4(u32& dst, i32x4 rsrc, u32 voff, u32 soff) {
  asm volatile(PADS "buffer_load_dword %0, %1, %2, %3 offen" : "=v"(dst) : "v"(voff), "s"(rsrc), "s"(RFL(soff)) : "memory");
}
#define DMA16(rsrc, voff, soff, lds, IMM) dma16<IMM>(rsrc, voff, soff, lds)
#define BLD16(dst, rsrc, voff, soff, IMM) bld16<IMM>(dst, rsrc, voff, soff)
template <int N> DI void wait_vm_bar8(u32x4& a, u32x4& b, u32x4& c, u32x4& d, u32x4& e, u32x4& f, u32x4& g, u32x4& h) {
  asm volatile("s_waitcnt vmcnt(%8)\n\ts_barrier" : "+v"(a), "+v"(b), "+v"(c), "+v"(d), "+v"(e), "+v"(f), "+v"(g), "+v"(h) : "i"(N) : "memory");
}
#define WAIT_VM(N) asm volatile("s_waitcnt vmcnt(" #N ")" ::: "memory")
DI float red_g(float x) {
  const u32 xi = __builtin_bit_cast(u32, x);
  auto a = __builtin_amdgcn_permlane32_swap(xi, xi, false, false);
  const float y = __builtin_bit_cast(float, (u32)a[0]) + __builtin_bit_cast(float, (u32)a[1]);
  const u32 yi = __builtin_bit_cast(u32, y);
  auto c = __builtin_amdgcn_permlane16_swap(yi, yi, false, false);
  return __builtin_bit_cast(float, (u32)c[0]) + __builtin_bit_cast(float, (u32)c[1]);
}
DI u32 lds_addr(const void* p) { return (u32)(uintptr_t)(LAS const char*)p; }

template <int I> struct IC { static constexpr int value = I; constexpr operator int() const { return I; } };
template <int B, int E, class F> DI void static_for(F&& f) { if constexpr (B < E) { f(IC<B>{}); static_for<B + 1, E>(f); } }
#define SB() __builtin_amdgcn_sched_barrier(0)

#define CODE_ADDR(SYM, out) do { u32 lo_, hi_; \
  asm volatile("s_getpc_b64 s[20:21]\n\ts_add_u32 s20, s20, " SYM "@rel32@lo+4\n\ts_addc_u32 s21, s21, " SYM "@rel32@hi+12\n\ts_mov_b32 %0, s20\n\ts_mov_b32 %1, s21" \
               : "=s"(lo_), "=s"(hi_) :: "s20", "s21", "scc"); out = ((unsigned long long)hi_ << 32) | lo_; } while (0)
DI void touch_code(unsigned long long p, u32 bytes, int tid) {
  typedef __attribute__((address_space(1))) const u32x4 gv4;
  u32x4 v[4];
#pragma unroll
  for (int k = 0; k < 4; ++k) { const u32 o = (u32)tid * 16u + (u32)k * 8192u; v[k] = *(gv4*)(p + (o < bytes ? o : 0u)); }
#pragma unroll
  for (int k = 0; k < 4; ++k) asm volatile("" :: "v"(v[k]));
}
constexpr u32 K0_CODE_BYTES = 29696, COL_CODE_BYTES = 3584, MLP0_CODE_BYTES = 24576, MLP1_CODE_BYTES = 28672;

struct Params {
  const u16 *x, *wr, *wi, *w1, *b1, *w2, *b2;
  u16* out;
  char* ws;
};

#define STAMP(kid, i)
#define RSTAMP(kid, i)

struct QGen { float v0[8], v1[8], k; };
DI void gen_q_init(QGen& G, int ms, int lane) {
  const int c = ms * 16 + (lane & 15), g = lane >> 4;
  int jj = c >> 1; float amp = 0.05590169943749474f; float ph = (c & 1) ? 0.25f : 0.f;
  if (c < 2) { jj = (c == 0) ? 320 : 0; amp = 0.03952847075210474f; ph = 0.f; }
  const float fj = (float)jj * (1.f / 640.f);
  const float kj = 2.f * cos2pi(fj), d = (float)(8 * g);
  G.v0[0] = amp * cos2pi(__builtin_amdgcn_fractf(fj * d) + ph);          G.v0[1] = amp * cos2pi(__builtin_amdgcn_fractf(fj * (d + 1.f)) + ph);
  G.v1[0] = amp * cos2pi(__builtin_amdgcn_fractf(fj * (d + 32.f)) + ph); G.v1[1] = amp * cos2pi(__builtin_amdgcn_fractf(fj * (d + 33.f)) + ph);
#pragma unroll
  for (int j = 2; j < 8; ++j) { G.v0[j] = kj * G.v0[j - 1] - G.v0[j - 2]; G.v1[j] = kj * G.v1[j - 1] - G.v1[j - 2]; }
  G.k = 2.f * cos2pi(__builtin_amdgcn_fractf((float)jj * 0.05f));
}
DI u32x4 gen_q_step(QGen& G) {
  const u32x4 r = u32x4{pk2(G.v0[0], G.v0[1]), pk2(G.v0[2], G.v0[3]), pk2(G.v0[4], G.v0[5]), pk2(G.v0[6], G.v0[7])};
#pragma unroll
  for (int j = 0; j < 8; ++j) { const float n = G.k * G.v1[j] - G.v0[j]; G.v0[j] = G.v1[j]; G.v1[j] = n; }
  return r;
}

struct JobOps { i32x4 rs; u32 vo[10]; u32 l0; };
DI void job_ops(JobOps& J, const Params& P, int job, int w8, int lane, u32 lbuf) {
  asm volatile("" : "+v"(lane), "+s"(job), "+s"(w8));
  if (job >= 32 && job < 59) {
    const int d = 80 * w8 + (lane >> 3), pos = lane & 7;
    J.rs = mk_rsrc(P.w1, D_ * F_ * 2);
    const u32 va = (u32)d * (F_ * 2) + (u32)(job - 32) * 128u + 16u * (u32)(pos ^ (((d >> 1) & 1) << 1));
#pragma unroll
    for (int k = 0; k < 10; ++k) J.vo[k] = ((k & 1) ? (va ^ 64u) : va) + (u32)k * (8 * F_ * 2);
    J.l0 = lbuf + (u32)w8 * 10240u;
  } else {
    const int nn = w8 >> 1, hh = w8 & 1;
    u32 s0;
    if (job < 32) { J.rs = mk_rsrc(P.x, S_ * D_ * 2); s0 = (u32)s_of_p(job * 64 + nn * 16) * (D_ * 2); }
    else if (job < 86) { J.rs = mk_rsrc(P.w2, F_ * D_ * 2); s0 = (u32)((job - 59) * 64 + nn * 16) * (D_ * 2); }
    else { J.rs = mk_rsrc(P.b2, D_ * 2); s0 = 0; }
    J.l0 = lbuf + (u32)nn * 20480u + (u32)hh * 10240u;
    const u32 va = (u32)lane * 16u;
    if (hh == 0) {
#pragma unroll
      for (int k = 0; k < 10; ++k) J.vo[k] = (va ^ (u32)(k * 16)) + (s0 + k * 1280u);
    } else {
      const u32 vb = (u32)(lane >> 4) * 1280u + 1024u + (((u32)(lane & 15) ^ (u32)(lane >> 4)) * 16u);
#pragma unroll
      for (int k = 0; k < 6; ++k) J.vo[k] = (va ^ (u32)((10 + k) * 16)) + (s0 + (10 + k) * 1280u);
#pragma unroll
      for (int k = 6; k < 10; ++k) J.vo[k] = (vb ^ (u32)((k - 6) * 64)) + (s0 + (k - 6) * 5120u);
    }
  }
}
template <int K> DI void job_op(const JobOps& J) {
  if constexpr (K == 0) asm volatile(PADS "s_mov_b32 m0, %2\n\ts_nop 0\n\tbuffer_load_dwordx4 %0, %1, 0 offen lds\n\ts_add_u32 m0, m0, 0x400" ::"v"(J.vo[0]), "s"(J.rs), "s"(RFL(J.l0)) : "memory", "scc");
  else asm volatile(PADS "buffer_load_dwordx4 %0, %1, 0 offen lds\n\ts_add_u32 m0, m0, 0x400" ::"v"(J.vo[K]), "s"(J.rs) : "memory", "scc");
}

DI u32x4 ident_frag(int par, int lane) {
  const int j = 16 * par + (lane & 15) - 8 * (lane >> 4);
  u32x4 r;
#pragma unroll
  for (int k = 0; k < 4; ++k) r[k] = (j == 2 * k ? 0x3F80u : 0u) | (j == 2 * k + 1 ? 0x3F800000u : 0u);
  return r;
}

template <int MODE, class HK>
DI void qloop_lin(const char* buf, int sl, int lane, u32x4 (&q)[2][KS1], f32x4 (&acc)[2][2], f32x4& sum, f32x4& gram, HK&& hook) {
  u32x4 bf[2][2];
  const int row = lane & 15, g = lane >> 4;
  const char* ba[4];
#pragma unroll
  for (int j = 0; j < 4; ++j) ba[j] = buf + row * 1024 + (((4 * j + g) ^ row) * 16);
  const int dB = 16384 - row * 768;
  auto rd = [&](auto T, auto N) { constexpr int t = T, n = N;
    if constexpr (t < 16) bf[t & 1][n] = *(const u32x4*)(ba[t & 3] + n * 20480 + (t >> 2) * 256);
    else if constexpr (t < KS1) bf[t & 1][n] = *(const u32x4*)(ba[t & 3] + dB + n * 20480); };
  rd(IC<0>{}, IC<0>{}); rd(IC<0>{}, IC<1>{});
  static_for<0, KS1>([&](auto T) {
    constexpr int t = T;
    rd(IC<t + 1>{}, IC<0>{}); rd(IC<t + 1>{}, IC<1>{});
    __builtin_amdgcn_s_waitcnt(0xC07F | ((t + 1 < KS1 ? 2 : 0) << 8));
    SB();
    static_for<0, 2>([&](auto N) {
      constexpr int n = N;
      if constexpr (MODE == 2) acc[0][n] = mfma(bf[t & 1][n], q[0][t], acc[0][n]); else acc[0][n] = mfma(q[0][t], bf[t & 1][n], acc[0][n]);
      if constexpr (MODE == 2) acc[1][n] = mfma(bf[t & 1][n], q[1][t], acc[1][n]); else acc[1][n] = mfma(q[1][t], bf[t & 1][n], acc[1][n]);
      SB();
      hook(T, N);
      SB();
    });
    if constexpr (MODE == 1) {
      const u32x4 e = sl ? bf[t & 1][1] : bf[t & 1][0];
      sum = mfma(e, u32x4{0x3F803F80u, 0x3F803F80u, 0x3F803F80u, 0x3F803F80u}, sum);
      gram = mfma(e, e, gram);
      SB();
    }
  });
}
template <class HK>
DI void qloop_tr(const char* buf, int nh, int lane, u32x4 (&q)[2][KS1], f32x4 (&acc)[2][2], HK&& hook) {
  const int g = lane >> 4, pp = lane & 3, qq = (lane & 15) >> 2;
  const int sw = (((qq >> 1) & 1) | ((g & 1) << 1)) << 1;
  const char* base[2];
#pragma unroll
  for (int n = 0; n < 2; ++n) base[n] = buf + (8 * g + qq) * 128 + ((2 * (2 * nh + n) + (pp >> 1)) ^ sw) * 16 + (pp & 1) * 8;
  u32x2 lo[2][2], hi[2][2];
  auto rd = [&](auto T, auto N) { constexpr int t = T, n = N;
    if constexpr (t < KS1) {
      lo[t & 1][n] = __builtin_bit_cast(u32x2, __builtin_amdgcn_ds_read_tr16_b64_v4bf16((LAS bf16x4*)(base[n] + t * 4096)));
      hi[t & 1][n] = __builtin_bit_cast(u32x2, __builtin_amdgcn_ds_read_tr16_b64_v4bf16((LAS bf16x4*)(base[n] + t * 4096 + 512)));
    } };
  rd(IC<0>{}, IC<0>{}); rd(IC<0>{}, IC<1>{});
  static_for<0, KS1>([&](auto T) {
    constexpr int t = T;
    rd(IC<t + 1>{}, IC<0>{}); rd(IC<t + 1>{}, IC<1>{});
    __builtin_amdgcn_s_waitcnt(0xC07F | ((t + 1 < KS1 ? 4 : 0) << 8));
    SB();
    static_for<0, 2>([&](auto N) {
      constexpr int n = N;
      const u32x4 b = u32x4{lo[t & 1][n][0], lo[t & 1][n][1], hi[t & 1][n][0], hi[t & 1][n][1]};
      acc[0][n] = mfma(q[0][t], b, acc[0][n]);
      acc[1][n] = mfma(q[1][t], b, acc[1][n]);
      SB();
      hook(T, N);
      SB();
    });
  });
}

DI u32x4 row_frag(const char* buf, int n, int t, int lane) {
  const int row = lane & 15, g = lane >> 4, x = (((4 * (t & 3) + g) ^ row) * 16);
  return *(const u32x4*)(buf + n * 20480 + (t < 16 ? row * 1024 + (t >> 2) * 256 : 16384 + row * 256) + x);
}
typedef __attribute__((ext_vector_type(2))) unsigned int v2u_t;
#define HAUX 16
DI void bst8(__amdgpu_buffer_rsrc_t rs, u32 voff, u32 soff, const f32x4& a) {
  __builtin_amdgcn_raw_buffer_store_b64(v2u_t{pk2(a[0], a[1]), pk2(a[2], a[3])}, rs, voff, soff, 0);
}
template <class HK>
DI void qjob_compute(const Params& P, int job, int ct5, int mp, int nh, int lane, const char* buf, u32x4 (&q)[2][KS1], HK&& hook) {
  asm volatile("" : "+s"(job), "+s"(ct5), "+s"(mp), "+s"(nh), "+v"(lane));
  const int ms0 = ct5 * 8 + 2 * mp, tc = 4 * ct5 + mp;
  f32x4 acc[2][2], sum = {0.f, 0.f, 0.f, 0.f}, gram = sum;
  acc[0][0] = sum; acc[0][1] = sum; acc[1][0] = sum; acc[1][1] = sum;
  const char* bl = buf + nh * 40960;
  const __amdgpu_buffer_rsrc_t rsw = __builtin_amdgcn_make_buffer_rsrc((void*)P.ws, 0, (int)WS_END, 0x00020000);
  auto vXf = [&]() { asm volatile("" : "+v"(lane)); return (u32)(lane >> 5) * 32768u + (u32)((lane >> 4) & 1) * 8u + (u32)(lane & 15) * 16u; };
  auto vWf = [&]() { asm volatile("" : "+v"(lane)); return ((u32)(lane >> 5) * 16u + (u32)(lane & 15)) * 16u + (u32)((lane >> 4) & 1) * 8u; };
  if (job < 32) {
    if (ct5 < 4 && mp == 3 && nh == (ct5 >> 1)) {
      qloop_lin<1>(bl, ct5 & 1, lane, q, acc, sum, gram, hook);
      const int c16 = lane & 15, g = lane >> 4, r = c16 & 3;
      const float s1 = r == 0 ? sum[0] : r == 1 ? sum[1] : r == 2 ? sum[2] : sum[3];
      const float s2 = r == 0 ? gram[0] : r == 1 ? gram[1] : r == 2 ? gram[2] : gram[3];
      const float m = s1 * (1.f / 640.f);
      float var = s2 * (1.f / 640.f) - m * m; var = var < 0.f ? 0.f : var;
      if (g == (c16 >> 2)) ((float*)(P.ws + OFF_RSTD0))[job * 64 + ct5 * 16 + c16] = rcp(ln_den(var));
    } else {
      qloop_lin<0>(bl, 0, lane, q, acc, sum, gram, hook);
    }
    const u32 vX = vXf();
#pragma unroll
    for (int a = 0; a < 2; ++a) {
      const u32 so = (u32)OFF_X0 + (u32)(2 * (ms0 + a) * 2048 + job * 64 + nh * 32) * 16u;
      bst8(rsw, vX, so, acc[a][0]); bst8(rsw, vX, so + 256u, acc[a][1]);
    }
  } else if (job < 59) {
    qloop_tr(buf, nh, lane, q, acc, hook);
    const u32 vW = vWf();
#pragma unroll
    for (int n = 0; n < 2; ++n) {
      const int fs = (job - 32) * 4 + 2 * nh + n;
      if (ms0 == 0 && lane < 16) acc[0][n][1] = 0.f;
      const int ft = fs < 84 ? fs / 7 : 12 + (fs - 84) / 6, m = fs < 84 ? fs - 7 * ft : (fs - 84) - 6 * (ft - 12);
      const u32 so = (u32)OFF_A0 + (u32)((ft * KS1 + tc) * 7 + m) * 1024u;
      bst8(rsw, vW, so, acc[0][n]); bst8(rsw, vW, so + 512u, acc[1][n]);
    }
  } else if (job < 86) {
    qloop_lin<2>(bl, 0, lane, q, acc, sum, gram, hook);
    const u32 vW = vWf();
#pragma unroll
    for (int a = 0; a < 2; ++a) {
      const int ms = ms0 + a, ct = ms / 5;
      const u32x4 id = ident_frag(a, lane);
#pragma unroll
      for (int n = 0; n < 2; ++n) {
        const f32x4 tr = mfma(row_frag(bl, n, tc, lane), id, f32x4{0.f, 0.f, 0.f, 0.f});
        const int fs = (job - 59) * 4 + 2 * nh + n;
        const u32 o = (u32)((ct * KS2 + (fs >> 1)) * 5 + (ms - 5 * ct)) * 1024u + (u32)(fs & 1) * 512u;
        bst8(rsw, vW, (u32)OFF_W2F + o, acc[a][n]);
        bst8(rsw, vW, (u32)OFF_W2T + o, tr);
      }
    }
  } else if (job == 86) {
    if (nh == 0) {
#pragma unroll
      for (int t = 0; t < KS1; ++t) { const u32x4 b = row_frag(buf, 0, t, lane); acc[0][0] = mfma(b, q[0][t], acc[0][0]); acc[1][0] = mfma(b, q[1][t], acc[1][0]); }
      if (lane < 16) { ((float*)(P.ws + OFF_B2F))[ms0 * 16 + lane] = acc[0][0][0]; ((float*)(P.ws + OFF_B2F))[ms0 * 16 + 16 + lane] = acc[1][0][0]; }
    }
  } else {
    const u32x4 id = ident_frag(nh, lane);
    const u32 vW = vWf();
    static_for<0, KS1>([&](auto T) {
      constexpr int t = T;
      const u32 fm = t < 12 ? (u32)(t * KS1 * 4) + (u32)nh : (u32)((12 + (t - 12) / 2) * KS1 * 4 + ((t - 12) & 1) * 2) + (u32)nh;
      const u32 so = (u32)OFF_A1 + (fm + (u32)tc * 4u) * 1024u;
      bst8(rsw, vW, so, mfma(q[0][t], id, f32x4{0.f, 0.f, 0.f, 0.f}));
      bst8(rsw, vW, so + 512u, mfma(q[1][t], id, f32x4{0.f, 0.f, 0.f, 0.f}));
    });
  }
}

__global__ void __launch_bounds__(512, 2) k0_prep(Params P) {
  extern __shared__ __attribute__((aligned(16))) char smem[];
  const int b = blockIdx.x, tid = threadIdx.x, lane = tid & 63, w8 = __builtin_amdgcn_readfirstlane(tid >> 6);
  RSTAMP(0, 14); STAMP(0, 0);
  if (b < 240) {
    const int li = b >> 3, ct5 = li % 5, j0 = (b & 7) * 6 + li / 5, j1 = j0 + 48;
    const bool two = j1 < 88;
    const int mp = w8 & 3, nh = w8 >> 2;
    const u32 lb = lds_addr(smem);
    JobOps J;
    job_ops(J, P, j0, w8, lane, lb);
    static_for<0, 4>([&](auto K) { job_op<K>(J); asm volatile("s_nop 0"); });
    const int WT = b < 160 ? b : -1, wa = WT & 15, wcg = (WT >= 0) ? (WT >> 4) : 0;
    u32x2 wv[2][2];
    u32 w0r, w0i;
    u32x4 q[2][KS1];
    QGen G0;
    constexpr int GL = 4;
    {
      QGen G1;
      gen_q_init(G1, ct5 * 8 + 2 * mp + 1, lane);
      static_for<0, 6>([&](auto T) { constexpr int t = T; SB(); job_op<4 + t>(J); SB(); q[1][t] = gen_q_step(G1); });
      static_for<6, KS1>([&](auto T) { constexpr int t = T; q[1][t] = gen_q_step(G1); });
#pragma unroll
      for (int t = 0; t < KS1; ++t) asm volatile("" : "+v"(q[1][t]));
    }
    job_ops(J, P, two ? j1 : 86, w8, lane, lb + 81920);
    static_for<0, 4>([&](auto K) { job_op<K>(J); asm volatile("s_nop 0"); });
    SB();
    gen_q_init(G0, ct5 * 8 + 2 * mp, lane);
    static_for<0, GL>([&](auto T) { constexpr int t = T; SB(); job_op<4 + t>(J); SB(); q[0][t] = gen_q_step(G0); });
    SB(); job_op<8>(J); asm volatile("s_nop 0"); job_op<9>(J); SB();
    STAMP(0, 1);
    asm volatile("s_waitcnt vmcnt(10)\n\ts_barrier" ::: "memory");
    STAMP(0, 2);
    qjob_compute(P, j0, ct5, mp, nh, lane, smem, q, [&](auto T, auto N) {
      constexpr int t = T, n = N;
      if constexpr (t + GL < KS1 && n == 0) q[0][t + GL] = gen_q_step(G0);
    });
    {
      const i32x4 rwr = mk_rsrc(P.wr, S_ * DF_ * 2), rwi = mk_rsrc(P.wi, S_ * DF_ * 2);
      const u32 wvo = (u32)(lane >> 3) * (DF_ * 2) + (u32)(lane & 7) * 8u + (u32)wcg * 64u;
      const u32 wso = (u32)((16 * nh + 32 * (4 * wa + mp)) * DF_ * 2);
      bld8(wv[0][0], rwr, wvo, wso); bld8(wv[0][1], rwi, wvo, wso);
      bld8(wv[1][0], rwr, wvo, wso + 8 * DF_ * 2); bld8(wv[1][1], rwi, wvo, wso + 8 * DF_ * 2);
      bldu16(w0r, rwr, (u32)(lane & 15) * (DF_ * 2) + 640u, wso); bldu16(w0i, rwi, (u32)(lane & 15) * (DF_ * 2) + 640u, wso);
    }
    STAMP(0, 3);
    asm volatile("s_waitcnt vmcnt(10) lgkmcnt(0)\n\ts_barrier" ::: "memory");
    STAMP(0, 4);
    if (two) qjob_compute(P, j1, ct5, mp, nh, lane, smem + 81920, q, [&](auto T, auto N) {});
    STAMP(0, 5);
    asm volatile("s_cmp_lg_u32 %6, 0\n\ts_cbranch_scc1 1f\n\ts_waitcnt vmcnt(0)\n1:\n\ts_waitcnt vmcnt(4)"
                 : "+v"(wv[0][0]), "+v"(wv[0][1]), "+v"(wv[1][0]), "+v"(wv[1][1]), "+v"(w0r), "+v"(w0i) : "s"(RFL((two && j1 != 86) ? 1 : 0)) : "memory", "scc");
    if (WT >= 0) {
      u32* st = (u32*)smem;
#pragma unroll
      for (int i = 0; i < 2; ++i) {
        const u32 r0 = wv[i][0][0], r1 = wv[i][0][1], i0 = wv[i][1][0], i1 = wv[i][1][1];
        u32* d = st + (4 * (lane & 7)) * 132 + (16 * nh + 8 * i + (lane >> 3)) * 4 + mp;
        d[0] = (r0 & 0xffffu) | (i0 << 16); d[132] = (r0 >> 16) | (i0 & 0xffff0000u);
        d[264] = (r1 & 0xffffu) | (i1 << 16); d[396] = (r1 >> 16) | (i1 & 0xffff0000u);
      }
      if (wcg == 0) {
        asm volatile("s_waitcnt lgkmcnt(0)\n\ts_barrier" ::: "memory");
        if (lane < 16) st[(16 * nh + lane) * 4 + mp] = w0r | (w0i << 16);
      }
      asm volatile("s_waitcnt lgkmcnt(0)\n\ts_barrier" ::: "memory");
      const int v = wa >> 3, ii = (wa >> 2) & 1, g = wa & 3;
#pragma unroll
      for (int k = 0; k < 2; ++k) {
        const int it = k * 512 + tid, cl = it >> 5, k1 = it & 31;
        const u32x4 o = *(const u32x4*)(st + cl * 132 + k1 * 4);
        *(u32x4*)(P.ws + OFF_WT + ((((size_t)(32 * wcg + cl) * 4 + (k1 >> 4) * 2 + v) * 2 + ii) * 64 + g * 16 + (k1 & 15)) * 16) = o;
      }
    }
    STAMP(0, 10); RSTAMP(0, 15);
  } else if (b < 248) {
    unsigned long long pc;
    CODE_ADDR("_Z7k0_prep6Params", pc); touch_code(pc, K0_CODE_BYTES, tid);
    CODE_ADDR("_Z5k_colILi0EEv6Params", pc); touch_code(pc, COL_CODE_BYTES, tid);
  } else if (b == 248) {
    const int c16 = lane & 15, g = lane >> 4;
    for (int fr = w8; fr < 20; fr += 8) {
      float v[8];
      if (fr < 16) {
        const int tile = fr >> 2, t = (fr >> 1) & 1, part = fr & 1;
#pragma unroll
        for (int j = 0; j < 8; ++j) {
          const int kk = 32 * t + 16 * (j >> 2) + 4 * g + (j & 3);
          const float a = (float)(((16 * tile + c16) * kk) & 63) * (1.f / 64.f);
          v[j] = part ? -sin2pi(a) : cos2pi(a);
        }
      } else {
        const int u = (fr - 16) >> 1, part = fr & 1;
#pragma unroll
        for (int j = 0; j < 8; ++j) {
          const int kk = 16 * (j >> 2) + 4 * g + (j & 3);
          const float a = (float)(((16 * u + c16) * kk) & 31) * (1.f / 32.f);
          v[j] = part ? -sin2pi(a) : cos2pi(a);
        }
      }
      *(u32x4*)(P.ws + OFF_TAB + fr * 1024 + lane * 16) = u32x4{pk2(v[0], v[1]), pk2(v[2], v[3]), pk2(v[4], v[5]), pk2(v[6], v[7])};
    }
  } else if (b == 249) {
    for (int i = tid; i < 2048; i += 512) { ((float*)(P.ws + OFF_SS2))[i] = 0.f; ((u32*)(P.ws + OFF_CNT))[i] = 0u; }
  }
}

template <int LAYER>
__global__ void __launch_bounds__(512, 2) k_col(Params P) {
  extern __shared__ __attribute__((aligned(16))) char smem[];
  const int b = blockIdx.x, tid = threadIdx.x, lane = tid & 63, w = __builtin_amdgcn_readfirstlane(tid >> 6);
  char* ws = P.ws;
  if (b >= 160) {
    unsigned long long pc;
    const int role = (b - 160) >> 3;
    if (role == 0) { if (LAYER == 0) CODE_ADDR("_Z5k_mlpILi0EEv6Params", pc); else CODE_ADDR("_Z5k_mlpILi1EEv6Params", pc); touch_code(pc, LAYER == 0 ? MLP0_CODE_BYTES : MLP1_CODE_BYTES, tid); }
    else if (role == 1 && LAYER == 0) { CODE_ADDR("_Z5k_colILi1EEv6Params", pc); touch_code(pc, COL_CODE_BYTES, tid); }
    else if (role == 2 && LAYER == 0) { CODE_ADDR("_Z5k_mlpILi1EEv6Params", pc); touch_code(pc, MLP1_CODE_BYTES, tid); }
    return;
  }
  constexpr int KID = LAYER == 0 ? 1 : 4;
  RSTAMP(KID, 14); STAMP(KID, 0);
  const int oct = (b & 7) + 8 * (b >> 4), hf = (b >> 3) & 1;
  char* sA = smem;
  char* sT = smem + 16384;
  char* sE = smem + 36864;
  char* sG = smem + 53248;
  char* sY = smem + 69632;
  const int cc = w >> 2, u = (w >> 1) & 1, v = w & 1, c16 = lane & 15, g = lane >> 4;
  const int col = oct * 4 + hf * 2 + cc;
  const i32x4 rsw = mk_rsrc(ws, (u32)WS_END);
  const u32 l16 = lane * 16;
  dma16<0>(rsw, l16, (u32)OFF_TAB + w * 1024u, lds_addr(sT) + w * 1024u);
  dma16<0>(rsw, l16, (u32)OFF_TAB + (8 + w) * 1024u, lds_addr(sT) + (8 + w) * 1024u);
  dma16<0>(rsw, l16, (u32)OFF_TAB + (16 + (w & 3)) * 1024u, lds_addr(sT) + (16 + (w & 3)) * 1024u);
  u32x2 raw[4]; u32 st4[4]; u32x4 wv[2];
  const u32 xo = (u32)(LAYER == 0 ? OFF_X0 : OFF_X2) + (u32)oct * 32768u, xv = (u32)tid * 16u + (u32)hf * 8u;
#pragma unroll
  for (int r = 0; r < 4; ++r) bld8(raw[r], rsw, xv, xo + r * 8192u);
#pragma unroll
  for (int r = 0; r < 4; ++r) bld4(st4[r], rsw, (u32)tid * 4u, (u32)(LAYER == 0 ? OFF_RSTD0 : OFF_SS2) + r * 2048u);
  {
    const u32 wo = (u32)OFF_WT + (u32)((col * 4 + u * 2 + v) * 2) * 1024u;
    bld16<0>(wv[0], rsw, l16, wo);
    bld16<1024>(wv[1], rsw, l16, wo);
  }
  SB();
  float twc[16], tws[16], itc[8], its[8];
  {
    const float fk = (float)(16 * u + c16) * (1.f / 2048.f);
#pragma unroll
    for (int i = 0; i < 4; ++i)
#pragma unroll
      for (int r = 0; r < 4; ++r) {
        const float a = (float)(16 * i + 4 * g + r) * fk;
        twc[i * 4 + r] = cos2pi(a); tws[i * 4 + r] = sin2pi(a);
      }
#pragma unroll
    for (int ii = 0; ii < 2; ++ii) {
      const float fm = (float)(16 * (2 * v + ii) + c16) * (1.f / 2048.f);
#pragma unroll
      for (int r = 0; r < 4; ++r) {
        const float a = (float)(16 * u + 4 * g + r) * fm;
        itc[ii * 4 + r] = cos2pi(a) * (1.f / 2048.f); its[ii * 4 + r] = sin2pi(a) * (1.f / 2048.f);
      }
    }
#pragma unroll
    for (int k = 0; k < 16; ++k) asm volatile("" : "+v"(twc[k]), "+v"(tws[k]));
#pragma unroll
    for (int k = 0; k < 8; ++k) asm volatile("" : "+v"(itc[k]), "+v"(its[k]));
  }
  STAMP(KID, 1);
  asm volatile("s_waitcnt vmcnt(10)\n\ts_barrier" ::: "memory");
  u32x4 f64r[2][2], f64i[2][2], f64n[2][2], f32r, f32i, f32n;
#pragma unroll
  for (int ii = 0; ii < 2; ++ii)
#pragma unroll
    for (int t = 0; t < 2; ++t) {
      f64r[ii][t] = *(const u32x4*)(sT + (((2 * v + ii) * 2 + t) * 2 + 0) * 1024 + l16);
      f64i[ii][t] = *(const u32x4*)(sT + (((2 * v + ii) * 2 + t) * 2 + 1) * 1024 + l16);
      f64n[ii][t] = f64i[ii][t] ^ 0x80008000u;
    }
  f32r = *(const u32x4*)(sT + (16 + u * 2 + 0) * 1024 + l16);
  f32i = *(const u32x4*)(sT + (16 + u * 2 + 1) * 1024 + l16);
  f32n = f32i ^ 0x80008000u;
#pragma unroll
  for (int ii = 0; ii < 2; ++ii)
#pragma unroll
    for (int t = 0; t < 2; ++t) asm volatile("" : "+v"(f64r[ii][t]), "+v"(f64i[ii][t]), "+v"(f64n[ii][t]));
  asm volatile("" : "+v"(f32r), "+v"(f32i), "+v"(f32n));
  STAMP(KID, 2);
  asm volatile("s_waitcnt vmcnt(2)" : "+v"(raw[0]), "+v"(raw[1]), "+v"(raw[2]), "+v"(raw[3]), "+v"(st4[0]), "+v"(st4[1]), "+v"(st4[2]), "+v"(st4[3]) :: "memory");
  const bool special = (oct == 0 && hf == 0);
  {
    float val[4][4];
#pragma unroll
    for (int r = 0; r < 4; ++r) {
      const float sv = __builtin_bit_cast(float, st4[r]);
      const float rs = (LAYER == 0) ? sv : __builtin_amdgcn_rcpf(ln_den(sv * (1.f / 640.f)));
      val[r][0] = bflo(raw[r][0]) * rs; val[r][1] = special ? 0.f : bfhi(raw[r][0]) * rs;
      val[r][2] = bflo(raw[r][1]) * rs; val[r][3] = bfhi(raw[r][1]) * rs;
    }
    const int ti = tid >> 7, tg = (tid >> 5) & 3, tu = (tid >> 4) & 1, tc = tid & 15;
#pragma unroll
    for (int k = 0; k < 4; ++k)
      *(u32x2*)(sA + (k * 4 + ti) * 1024 + (tg * 16 + tc) * 16 + tu * 8) = u32x2{pk2(val[0][k], val[1][k]), pk2(val[2][k], val[3][k])};
  }
  asm volatile("s_waitcnt lgkmcnt(0)\n\ts_barrier" ::: "memory");
  STAMP(KID, 3);
  const f32x4 Z4 = {0.f, 0.f, 0.f, 0.f};
  u32x4 yr[2], yi[2];
  {
    u32x4 xr[4], xi[4];
#pragma unroll
    for (int i = 0; i < 4; ++i) {
      xr[i] = *(const u32x4*)(sA + ((cc * 2 + 0) * 4 + i) * 1024 + l16);
      xi[i] = *(const u32x4*)(sA + ((cc * 2 + 1) * 4 + i) * 1024 + l16);
    }
    f32x4 dr[4], di[4];
#pragma unroll
    for (int i = 0; i < 4; ++i) { dr[i] = mfma(xr[i], f32r, Z4); di[i] = mfma(xr[i], f32i, Z4); }
#pragma unroll
    for (int i = 0; i < 4; ++i) { dr[i] = mfma(xi[i], f32n, dr[i]); di[i] = mfma(xi[i], f32r, di[i]); }
    float tr[16], tq[16];
#pragma unroll
    for (int i = 0; i < 4; ++i)
#pragma unroll
      for (int r = 0; r < 4; ++r) {
        tr[i * 4 + r] = dr[i][r] * twc[i * 4 + r] + di[i][r] * tws[i * 4 + r];
        tq[i * 4 + r] = di[i][r] * twc[i * 4 + r] - dr[i][r] * tws[i * 4 + r];
      }
#pragma unroll
    for (int t = 0; t < 2; ++t) {
      yr[t] = u32x4{pk2(tr[8 * t + 0], tr[8 * t + 1]), pk2(tr[8 * t + 2], tr[8 * t + 3]), pk2(tr[8 * t + 4], tr[8 * t + 5]), pk2(tr[8 * t + 6], tr[8 * t + 7])};
      yi[t] = u32x4{pk2(tq[8 * t + 0], tq[8 * t + 1]), pk2(tq[8 * t + 2], tq[8 * t + 3]), pk2(tq[8 * t + 4], tq[8 * t + 5]), pk2(tq[8 * t + 6], tq[8 * t + 7])};
    }
  }
  u32x4 zr[2], zi[2];
  {
    f32x4 dr[2] = {Z4, Z4}, di[2] = {Z4, Z4};
#pragma unroll
    for (int t = 0; t < 2; ++t) {
#pragma unroll
      for (int ii = 0; ii < 2; ++ii) { dr[ii] = mfma(f64r[ii][t], yr[t], dr[ii]); di[ii] = mfma(f64i[ii][t], yr[t], di[ii]); }
#pragma unroll
      for (int ii = 0; ii < 2; ++ii) { dr[ii] = mfma(f64n[ii][t], yi[t], dr[ii]); di[ii] = mfma(f64r[ii][t], yi[t], di[ii]); }
    }
    asm volatile("s_waitcnt vmcnt(0)" : "+v"(wv[0]), "+v"(wv[1]) :: "memory");
    float pr[8], pi[8];
#pragma unroll
    for (int ii = 0; ii < 2; ++ii)
#pragma unroll
      for (int r = 0; r < 4; ++r) {
        const u32 wd = wv[ii][r];
        const float a = bflo(wd), bb = bfhi(wd);
        pr[ii * 4 + r] = dr[ii][r] * a - di[ii][r] * bb;
        pi[ii * 4 + r] = dr[ii][r] * bb + di[ii][r] * a;
      }
    const u32x4 mr = u32x4{pk2(pr[0], pr[1]), pk2(pr[2], pr[3]), pk2(pr[4], pr[5]), pk2(pr[6], pr[7])};
    const u32x4 mi = u32x4{pk2(pi[0], pi[1]), pk2(pi[2], pi[3]), pk2(pi[4], pi[5]), pk2(pi[6], pi[7])};
    *(u32x4*)(sE + ((((cc * 2 + u) * 2 + v) * 2 + 0) * 1024) + l16) = mr;
    *(u32x4*)(sE + ((((cc * 2 + u) * 2 + v) * 2 + 1) * 1024) + l16) = mi;
    STAMP(KID, 4);
    asm volatile("s_waitcnt lgkmcnt(0)\n\ts_barrier" ::: "memory");
    const u32x4 or_ = *(const u32x4*)(sE + ((((cc * 2 + u) * 2 + (1 - v)) * 2 + 0) * 1024) + l16);
    const u32x4 oi_ = *(const u32x4*)(sE + ((((cc * 2 + u) * 2 + (1 - v)) * 2 + 1) * 1024) + l16);
    zr[0] = v ? or_ : mr; zi[0] = v ? oi_ : mi;
    zr[1] = v ? mr : or_; zi[1] = v ? mi : oi_;
  }
  {
    f32x4 dr[2] = {Z4, Z4}, di[2] = {Z4, Z4};
#pragma unroll
    for (int t = 0; t < 2; ++t) {
#pragma unroll
      for (int ii = 0; ii < 2; ++ii) { dr[ii] = mfma(zr[t], f64r[ii][t], dr[ii]); di[ii] = mfma(zr[t], f64n[ii][t], di[ii]); }
#pragma unroll
      for (int ii = 0; ii < 2; ++ii) { dr[ii] = mfma(zi[t], f64i[ii][t], dr[ii]); di[ii] = mfma(zi[t], f64r[ii][t], di[ii]); }
    }
#pragma unroll
    for (int ii = 0; ii < 2; ++ii) {
      float tr[4], tq[4];
#pragma unroll
      for (int r = 0; r < 4; ++r) {
        tr[r] = dr[ii][r] * itc[ii * 4 + r] - di[ii][r] * its[ii * 4 + r];
        tq[r] = dr[ii][r] * its[ii * 4 + r] + di[ii][r] * itc[ii * 4 + r];
      }
      *(u32x2*)(sG + ((((cc * 2 + v) * 2 + ii) * 2 + 0) * 1024) + l16 + u * 8) = u32x2{pk2(tr[0], tr[1]), pk2(tr[2], tr[3])};
      *(u32x2*)(sG + ((((cc * 2 + v) * 2 + ii) * 2 + 1) * 1024) + l16 + u * 8) = u32x2{pk2(tq[0], tq[1]), pk2(tq[2], tq[3])};
    }
  }
  STAMP(KID, 5);
  asm volatile("s_waitcnt lgkmcnt(0)\n\ts_barrier" ::: "memory");
  {
    u32x4 tr[2], tq[2];
#pragma unroll
    for (int ii = 0; ii < 2; ++ii) {
      tr[ii] = *(const u32x4*)(sG + ((((cc * 2 + v) * 2 + ii) * 2 + 0) * 1024) + l16);
      tq[ii] = *(const u32x4*)(sG + ((((cc * 2 + v) * 2 + ii) * 2 + 1) * 1024) + l16);
    }
    f32x4 orr[2], oi[2];
#pragma unroll
    for (int ii = 0; ii < 2; ++ii) { orr[ii] = mfma(f32r, tr[ii], Z4); oi[ii] = mfma(f32r, tq[ii], Z4); }
#pragma unroll
    for (int ii = 0; ii < 2; ++ii) { orr[ii] = mfma(f32i, tq[ii], orr[ii]); oi[ii] = mfma(f32n, tr[ii], oi[ii]); }
#pragma unroll
    for (int ii = 0; ii < 2; ++ii) {
      const int tt = (2 * v + ii) * 128 + g * 32 + u * 16 + c16;
      *(u32x4*)(sY + (cc * 512 + tt) * 16) = u32x4{pk2(orr[ii][0], oi[ii][0]), pk2(orr[ii][1], oi[ii][1]), pk2(orr[ii][2], oi[ii][2]), pk2(orr[ii][3], oi[ii][3])};
    }
  }
  STAMP(KID, 6);
  asm volatile("s_waitcnt lgkmcnt(0)\n\ts_barrier" ::: "memory");
  {
    char* Xout = ws + (LAYER == 0 ? OFF_X1 : OFF_X3);
    const u32x4 ya = *(const u32x4*)(sY + tid * 16), yb = *(const u32x4*)(sY + (512 + tid) * 16);
#pragma unroll
    for (int r = 0; r < 4; ++r) {
      const u32 a = ya[r], bq = yb[r], r0 = raw[r][0], r1 = raw[r][1];
      const float v0 = bflo(r0) + bflo(a);
      const float v1 = special ? bfhi(r0) : bfhi(r0) + bfhi(a);
      const float v2 = bflo(r1) + bflo(bq), v3 = bfhi(r1) + bfhi(bq);
      *(u32x2*)(Xout + ((size_t)oct * 2048 + r * 512 + tid) * 16 + hf * 8) = u32x2{pk2(v0, v1), pk2(v2, v3)};
    }
  }
  STAMP(KID, 7); RSTAMP(KID, 15);
}

DI f32x2 gelu2(f32x2 x) {
  const float k1 = -2.3022081978378774f;
  const float k2 = k1 * 0.044715f;
  const f32x2 t = x * x;
  const f32x2 u = t * k2 + k1;
  const f32x2 z = x * u;
  f32x2 d = f32x2{__builtin_amdgcn_exp2f(z[0]), __builtin_amdgcn_exp2f(z[1])} + 1.f;
  return x * f32x2{__builtin_amdgcn_rcpf(d[0]), __builtin_amdgcn_rcpf(d[1])};
}
template <int IMM> DI void dma16_nom0(i32x4 rsrc, u32 voff, u32 soff) {
  asm volatile(PADS "buffer_load_dwordx4 %0, %1, %2 offen offset:%3 lds" ::"v"(voff), "s"(rsrc), "s"(RFL(soff)), "i"(IMM) : "memory");
}
template <int N> DI void wait_vm_bar(u32x4& a, u32x4& b, u32x4& c, u32x4& d, u32x4& e, u32x4& f, u32x4& g, u32x4& h) { wait_vm_bar8<N>(a, b, c, d, e, f, g, h); }

template <int LAYER, int NF, int NQ, class MH, class EH>
DI void gemm1_body(const Params& P, char* smem, int ft, int tt, int lane, int w, MH&& tail_hook, EH&& epi_hook) {
  constexpr int KID = LAYER == 0 ? 2 : 5;
  constexpr int NM = NF + NQ, H = (NM + 1) / 2, RING = (LAYER == 0) ? 20 : 16, BR = 12;
  const int c16 = lane & 15, g = lane >> 4;
  char* ws = P.ws;
  const i32x4 rs = mk_rsrc(ws, (u32)WS_END);
  const u32 l16 = lane * 16;
  const int p0 = tt * 128 + w * 32;
  const int f0 = (ft < 12) ? 7 * ft : 84 + 6 * (ft - 12);
  u32x4 bq[BR][2];
  const u32 xoff = (u32)(LAYER == 0 ? OFF_X1 : OFF_X3) + (u32)p0 * 16;
  const u32 bvo = (u32)g * 32768u + (u32)c16 * 16u;
  const u32 asrc = (u32)OFF_A0 + (u32)(ft * KS1 + w) * 7168u, qsrc = (u32)OFF_A1 + (u32)(ft * KS1 + w) * 4096u;
  const u32 adst = lds_addr(smem) + (u32)w * (NM * 1024);
  constexpr int PER = NM + 8;
  auto issue = [&](auto C, auto K) {
    constexpr int c = C, k = K;
    if constexpr (k < NF) {
      constexpr int k4 = k & ~3;
      const u32 so = asrc + (u32)(c * 4 * 7 + k4) * 1024u;
      if constexpr ((k & 3) == 0) dma16<0>(rs, l16, so, adst + (u32)((((c * 4) % RING) * NM + k4) * 1024));
      else dma16_nom0<(k & 3) * 1024>(rs, l16, so);
    } else if constexpr (k < NM) {
      constexpr int kq = k - NF;
      const u32 so = qsrc + (u32)(c * 4 * 4) * 1024u;
      if constexpr (kq == 0) dma16<0>(rs, l16, so, adst + (u32)((((c * 4) % RING) * NM + NF) * 1024));
      else dma16_nom0<kq * 1024>(rs, l16, so);
    } else {
      constexpr int kk = (k - NM) >> 1, t = 4 * c + kk;
      const u32 so = xoff + (u32)t * 131072u;
      if constexpr (((k - NM) & 1) == 0) bld16<0>(bq[t % BR][0], rs, bvo, so);
      else bld16<256>(bq[t % BR][1], rs, bvo, so);
    }
  };
  static_for<0, 2>([&](auto C) { static_for<0, PER>([&](auto K) { issue(C, K); }); });
  u32x2 bw[NF];
  {
    const i32x4 rb1 = mk_rsrc(P.b1, F_ * 2);
#pragma unroll
    for (int m = 0; m < NF; ++m) bld8(bw[m], rb1, (u32)g * 8u, (u32)(f0 + m) * 32u);
  }
  STAMP(KID, 1);
  f32x4 acc[NM][2], gram[2];
#pragma unroll
  for (int m = 0; m < NM; ++m) { acc[m][0] = f32x4{0.f, 0.f, 0.f, 0.f}; acc[m][1] = acc[m][0]; }
  gram[0] = gram[1] = f32x4{0.f, 0.f, 0.f, 0.f};
  u32x4 af[3][H];
  auto waitchunk = [&](auto C, auto N) {
    constexpr int c = C;
    wait_vm_bar<N.value>(bq[(4 * c) % BR][0], bq[(4 * c) % BR][1], bq[(4 * c + 1) % BR][0], bq[(4 * c + 1) % BR][1], bq[(4 * c + 2) % BR][0],
                         bq[(4 * c + 2) % BR][1], bq[(4 * c + 3) % BR][0], bq[(4 * c + 3) % BR][1]);
  };
  auto rd1 = [&](auto HH, auto K) {
    constexpr int h = HH, k = K, t = h >> 1, m0 = (h & 1) ? H : 0, m1 = (h & 1) ? NM : H;
    if constexpr (t < KS1 && m0 + k < m1) af[h % 3][k] = *(const u32x4*)(smem + ((t % RING) * NM + m0 + k) * 1024 + l16);
  };
  waitchunk(IC<0>{}, IC<PER + NF>{});
  STAMP(KID, 2);
  static_for<0, H>([&](auto K) { rd1(IC<0>{}, K); });
  static_for<0, H>([&](auto K) { rd1(IC<1>{}, K); });
  static_for<0, 2 * KS1>([&](auto HH) {
    constexpr int h = HH, t = h >> 1, c = t >> 2, q = t & 3, m0 = (h & 1) ? H : 0, m1 = (h & 1) ? NM : H;
    constexpr int PS = (PER + 2) / 3;
    if constexpr ((h & 1) == 0 && q == 3 && t + 1 < KS1) waitchunk(IC<c + 1>{}, IC<(c + 1 < 4 ? PER : 6)>{});
    constexpr int n1 = (h + 1 < 2 * KS1) ? (((h + 1) & 1) ? NM - H : H) : 0;
    __builtin_amdgcn_s_waitcnt(0xC07F | (n1 << 8));
    SB();
    static_for<m0, m1>([&](auto M) {
      constexpr int m = M;
      acc[m][0] = mfma(af[h % 3][m - m0], bq[t % BR][0], acc[m][0]);
      SB();
      rd1(IC<h + 2>{}, IC<m - m0>{});
      SB();
      acc[m][1] = mfma(af[h % 3][m - m0], bq[t % BR][1], acc[m][1]);
      SB();
      if constexpr (c + 2 < 5 && q < 3 && m < PS && q * PS + m < PER) issue(IC<c + 2>{}, IC<q * PS + m>{});
      if constexpr (h >= 24 && h < 39 && m == m0) tail_hook(IC<h - 24>{}, IC<((LAYER == 0) ? 0 : 4 * NM)>{});
      SB();
    });
    if constexpr ((h & 1) == 0) {
      u32x4 e0 = bq[t % BR][0], e1 = bq[t % BR][1];
      if constexpr (t == 0) { e0[0] = (g == 0) ? (e0[0] & 0xffffu) : (u32)e0[0]; e1[0] = (g == 0) ? (e1[0] & 0xffffu) : (u32)e1[0]; }
      gram[0] = mfma(e0, e0, gram[0]);
      gram[1] = mfma(e1, e1, gram[1]);
      SB();
    } else if constexpr (NM - H < H) {
      rd1(IC<h + 2>{}, IC<H - 1>{});
    }
  });
  STAMP(KID, 3);
  f32x2 bb[NF][2];
#pragma unroll
  for (int m = 0; m < NF; ++m) {
    asm volatile("" : "+v"(bw[m]));
    bb[m][0] = f32x2{bflo(bw[m][0]), bfhi(bw[m][0])}; bb[m][1] = f32x2{bflo(bw[m][1]), bfhi(bw[m][1])};
  }
  float a_sc[2];
#pragma unroll
  for (int n = 0; n < 2; ++n) {
    const int r = c16 & 3;
    float dv = r == 0 ? gram[n][0] : r == 1 ? gram[n][1] : r == 2 ? gram[n][2] : gram[n][3];
    dv = (g == (c16 >> 2)) ? dv : 0.f;
    const float var = red_g(dv) * (1.f / 640.f);
    const float d1 = ln_den(var);
    a_sc[n] = rcp(d1 * ln_den(var * rcp(d1 * d1)));
  }
  const auto rsb = __builtin_amdgcn_make_buffer_rsrc((void*)ws, 0, (int)WS_END, 0x00020000);
  const u32 vW = ((u32)(lane >> 5) * 16u + (u32)c16) * 16u + (u32)(g & 1) * 8u;
  const u32 hso = (u32)(LAYER == 0 ? OFF_H1 : OFF_H2) + (u32)((tt * 2 + (w >> 1)) * KS2 * 4 + 2 * (w & 1)) * 1024u;
  static_for<0, NF>([&](auto M) {
    constexpr int m = M;
    const int mg = f0 + m;
    const u32 so = hso + (u32)(mg >> 1) * 4096u + (u32)(mg & 1) * 512u;
    epi_hook(M);
#pragma unroll
    for (int n = 0; n < 2; ++n) {
      const f32x4 a = acc[m][n];
      const f32x2 h01 = gelu2(f32x2{a[0], a[1]} * a_sc[n] + bb[m][0]), h23 = gelu2(f32x2{a[2], a[3]} * a_sc[n] + bb[m][1]);
      __builtin_amdgcn_raw_buffer_store_b64(v2u_t{pk2(h01[0], h01[1]), pk2(h23[0], h23[1])}, rsb, vW, so + n * 1024u, HAUX);
    }
  });
  if constexpr (NQ > 0) {
    const int d0 = (ft < 12) ? 2 * ft : 24 + 4 * (ft - 12);
#pragma unroll
    for (int m = 0; m < NQ; ++m)
#pragma unroll
      for (int n = 0; n < 2; ++n) {
        const f32x4 a = acc[NF + m][n];
        __builtin_amdgcn_raw_buffer_store_b64(v2u_t{pk2(a[0], a[1]), pk2(a[2], a[3])}, rsb, (u32)lane * 8u,
                                              (u32)OFF_X3S + (u32)((d0 + m) * 128 + tt * 8 + w * 2 + n) * 512u, 16);
      }
  }
}

typedef __attribute__((address_space(1))) unsigned gu32;

template <int LAYER>
__global__ void __launch_bounds__(256, 1) k_mlp(Params P) {
  extern __shared__ __attribute__((aligned(16))) char smem[];
  const int b = blockIdx.x, tid = threadIdx.x, lane = tid & 63, w = __builtin_amdgcn_readfirstlane(tid >> 6);
  constexpr int KID = LAYER == 0 ? 2 : 5;
  RSTAMP(KID, 14); STAMP(KID, 0);
  char* ws = P.ws;
  const int x = b & 7, li = b >> 3;
  const int ft = (x & 1) * 8 + (li & 7), tt = (x >> 1) * 4 + (li >> 3);
  const int ct = ft >> 1, t64 = 2 * tt + (ft & 1);
  const u32 l16 = lane * 16;
  const u32 aoff = (u32)(LAYER == 0 ? OFF_W2F : OFF_W2T) + (u32)ct * (KS2 * 5 * 1024) + (u32)w * 5120u;
  const auto rs = __builtin_amdgcn_make_buffer_rsrc((void*)ws, 0, (int)WS_END, 0x00020000);
  const bool full = (w < 2);
  u32x4 rg[11][5];
  auto lda1 = [&](auto I) {
    constexpr int i = I;
    if (i < 10 || full) {
#pragma unroll
      for (int m = 0; m < 4; ++m) rg[i][m] = __builtin_amdgcn_raw_buffer_load_b128(rs, l16 + m * 1024, aoff + (u32)(3 + i) * 20480u, 0);
      rg[i][4] = __builtin_amdgcn_raw_buffer_load_b128(rs, l16, aoff + (u32)(3 + i) * 20480u + 4096u, 0);
    } else {
#pragma unroll
      for (int m = 0; m < 5; ++m) rg[i][m] = u32x4{0u, 0u, 0u, 0u};
    }
  };
  u32 lA;
  {
    const i32x4 rsa = mk_rsrc(ws, (u32)WS_END);
    auto tail = [&](auto I, auto KB) {
      constexpr int i = I, st = i / 5, m = i % 5;
      const u32 lw = lds_addr(smem) + (u32)(KB.value * 1024) + (u32)w * 15360u;
      if constexpr (m == 0) dma16<0>(rsa, l16, aoff + st * 20480u, lw + st * 5120u);
      else if constexpr (m < 4) dma16_nom0<m * 1024>(rsa, l16, aoff + st * 20480u);
      else dma16<0>(rsa, l16, aoff + st * 20480u + 4096u, lw + st * 5120u + 4096u);
    };
    auto epi = [&](auto M) { constexpr int m = M; if constexpr (m < 6) lda1(M); };
    if (ft < 12) { gemm1_body<LAYER, 7, LAYER == 0 ? 0 : 2>(P, smem, ft, tt, lane, w, tail, epi); lA = (LAYER == 0) ? 0 : 4 * 9 * 1024; }
    else { gemm1_body<LAYER, 6, LAYER == 0 ? 0 : 4>(P, smem, ft, tt, lane, w, tail, epi); lA = (LAYER == 0) ? 0 : 4 * 10 * 1024; }
    lA += (u32)w * 15360u;
  }
  STAMP(KID, 4);
  gu32* cnt = (gu32*)(ws + OFF_CNT) + (LAYER * 16 + tt) * 32;
  asm volatile("s_waitcnt vmcnt(0)\n\ts_barrier" ::: "memory");
  if (tid == 0) __hip_atomic_fetch_add(cnt, 1u, __ATOMIC_RELAXED, __HIP_MEMORY_SCOPE_AGENT);
  STAMP(KID, 5);
  const u32 boff = (u32)(LAYER == 0 ? OFF_H1 : OFF_H2) + (u32)t64 * (KS2 * 4096) + (u32)w * 4096u;
  u32 bo[4];
#pragma unroll
  for (int n = 0; n < 4; ++n) bo[n] = boff + (u32)((n + w) & 3) * 1024u;
  constexpr int PD = 5, NSTEP = 14;
  const u32 vX = (u32)(lane >> 5) * 32768u + (u32)((lane >> 4) & 1) * 8u + (u32)(lane & 15) * 16u;
  const u32 xso = (u32)(10 * ct * 2048 + t64 * 64 + w * 16) * 16u;
  v2u_t rz[5]; f32x4 bz[5];
  auto ldbias = [&]() {
#pragma unroll
    for (int m = 0; m < 5; ++m) {
      if (LAYER == 0) {
        rz[m] = __builtin_amdgcn_raw_buffer_load_b64(rs, vX, (u32)OFF_X1 + xso + m * 65536u, 0);
        bz[m] = __builtin_bit_cast(f32x4, __builtin_amdgcn_raw_buffer_load_b128(rs, (u32)(lane >> 4) * 16u, (u32)OFF_B2F + (u32)(ct * 5 + m) * 64u, 0));
      } else {
        const u32x2 bw = *(const u32x2*)(P.b2 + (ct * 5 + m) * 16 + 4 * (lane >> 4));
        bz[m] = f32x4{bflo(bw[0]), bfhi(bw[0]), bflo(bw[1]), bfhi(bw[1])};
      }
    }
  };
  static_for<6, 11>([&](auto I) { lda1(I); });
  ldbias();
  SB();
  if (tid == 0) {
    unsigned spins = 0;
    while (__hip_atomic_load(cnt, __ATOMIC_RELAXED, __HIP_MEMORY_SCOPE_AGENT) < 16u) {
      __builtin_amdgcn_s_sleep(1);
      if (++spins > (1u << 22)) { __hip_atomic_store((gu32*)(ws + OFF_CNT) + 1024, 1u, __ATOMIC_RELAXED, __HIP_MEMORY_SCOPE_AGENT); break; }
    }
  }
  asm volatile("s_barrier" ::: "memory");
  STAMP(KID, 6);
  u32x4 rb[PD][4];
  auto ldb = [&](auto I, auto K) { constexpr int i = I, k = K; rb[i % PD][k] = __builtin_amdgcn_raw_buffer_load_b128(rs, l16, bo[k] + (u32)i * 16384u, 16); };
  static_for<0, PD - 1>([&](auto I) { static_for<0, 4>([&](auto K) { ldb(I, K); }); });
  SB();
  if (LAYER == 1) {
#pragma unroll
    for (int m = 0; m < 5; ++m) rz[m] = __builtin_amdgcn_raw_buffer_load_b64(rs, (u32)lane * 8u, (u32)OFF_X3S + (u32)((ct * 5 + m) * 128 + t64 * 4 + w) * 512u, 16);
  }
  f32x4 acc[5][4];
#pragma unroll
  for (int m = 0; m < 5; ++m)
#pragma unroll
    for (int n = 0; n < 4; ++n) acc[m][n] = f32x4{0.f, 0.f, 0.f, 0.f};
  u32x4 al[2][5];
  const char* lbase = smem + lA + l16;
#pragma unroll
  for (int m = 0; m < 5; ++m) al[0][m] = *(const u32x4*)(lbase + m * 1024);
  SB();
  static_for<0, NSTEP - 1>([&](auto I) {
    constexpr int i = I, nx = i + PD - 1;
    static_for<0, 5>([&](auto M) {
      constexpr int m = M;
#pragma unroll
      for (int n = 0; n < 4; ++n) {
        if constexpr (i < 3) acc[m][n] = mfma(al[i & 1][m], rb[i % PD][n], acc[m][n]);
        else acc[m][n] = mfma(rg[i - 3][m], rb[i % PD][n], acc[m][n]);
      }
      SB();
      if constexpr (i + 1 < 3) al[(i + 1) & 1][m] = *(const u32x4*)(lbase + ((i + 1) * 5 + m) * 1024);
      if constexpr (m < 4) {
        if constexpr (nx < NSTEP - 1) ldb(IC<nx>{}, M);
        else if constexpr (nx == NSTEP - 1) { if (full) ldb(IC<nx>{}, M); }
      }
      SB();
    });
  });
  if (full) {
    constexpr int i = NSTEP - 1;
#pragma unroll
    for (int m = 0; m < 5; ++m)
#pragma unroll
      for (int n = 0; n < 4; ++n) acc[m][n] = mfma(rg[10][m], rb[i % PD][n], acc[m][n]);
  }
  SB();
  STAMP(KID, 7);
  asm volatile("s_barrier" ::: "memory");
  f32x4* red = (f32x4*)smem + lane;
#pragma unroll
  for (int n = 1; n < 4; ++n) {
    f32x4* dst = red + (((n + w) & 3) * 15 + (n - 1) * 5) * 64;
#pragma unroll
    for (int m = 0; m < 5; ++m) dst[m * 64] = acc[m][n];
  }
  asm volatile("s_waitcnt lgkmcnt(0)\n\ts_barrier" ::: "memory");
  float ssq = 0.f;
  const f32x4* src = red + w * 15 * 64;
#pragma unroll
  for (int m = 0; m < 5; ++m) {
    const f32x4 o = (acc[m][0] + src[m * 64]) + (src[(5 + m) * 64] + src[(10 + m) * 64]) +
                    (f32x4{bflo(rz[m][0]), bfhi(rz[m][0]), bflo(rz[m][1]), bfhi(rz[m][1])} + bz[m]);
    const v2u_t ov = v2u_t{pk2(o[0], o[1]), pk2(o[2], o[3])};
    if (LAYER == 0) {
      __builtin_amdgcn_raw_buffer_store_b64(ov, rs, vX, (u32)OFF_X2 + xso + m * 65536u, 0);
      f32x4 q2 = o * o;
      if (m == 0) q2[1] = (ct == 0 && lane < 16) ? 0.f : q2[1];
      ssq += (q2[0] + q2[1]) + (q2[2] + q2[3]);
    } else {
      *(v2u_t*)(P.out + (size_t)s_of_p(t64 * 64 + w * 16 + (lane & 15)) * D_ + (ct * 5 + m) * 16 + 4 * (lane >> 4)) = ov;
    }
  }
  if (LAYER == 0) {
    ssq = red_g(ssq);
    if (lane < 16) atomicAdd((float*)(ws + OFF_SS2) + t64 * 64 + w * 16 + lane, ssq);
  }
  STAMP(KID, 8); RSTAMP(KID, 15);
}

extern "C" void kernel_launch(void* const* d_in, const int* in_sizes, int n_in, void* d_out, int out_size, void* d_ws,
                              size_t ws_size, hipStream_t stream) {
  Params P;
  P.x = (const u16*)d_in[0]; P.wr = (const u16*)d_in[1]; P.wi = (const u16*)d_in[2];
  P.w1 = (const u16*)d_in[4]; P.b1 = (const u16*)d_in[5]; P.w2 = (const u16*)d_in[6]; P.b2 = (const u16*)d_in[7];
  P.out = (u16*)d_out; P.ws = (char*)d_ws;
  {
    hipFuncSetAttribute((const void*)k0_prep, hipFuncAttributeMaxDynamicSharedMemorySize, 163840);
    hipFuncSetAttribute((const void*)k_col<0>, hipFuncAttributeMaxDynamicSharedMemorySize, 163840);
    hipFuncSetAttribute((const void*)k_col<1>, hipFuncAttributeMaxDynamicSharedMemorySize, 163840);
    hipFuncSetAttribute((const void*)k_mlp<0>, hipFuncAttributeMaxDynamicSharedMemorySize, 163840);
    hipFuncSetAttribute((const void*)k_mlp<1>, hipFuncAttributeMaxDynamicSharedMemorySize, 163840);
  }
  k0_prep<<<250, 512, 163840, stream>>>(P);
  k_col<0><<<184, 512, 86016, stream>>>(P);
  k_mlp<0><<<256, 256, 163840, stream>>>(P);
  k_col<1><<<168, 512, 86016, stream>>>(P);
  k_mlp<1><<<256, 256, 163840, stream>>>(P);
}
```

```cpp
#include <hip/hip_runtime.h>
#include <stdint.h>

typedef __bf16 bf16x2 __attribute__((ext_vector_type(2)));
typedef __bf16 bf16x4 __attribute__((ext_vector_type(4)));
typedef __bf16 bf16x8 __attribute__((ext_vector_type(8)));
typedef float f32x2 __attribute__((ext_vector_type(2)));
typedef float f32x4 __attribute__((ext_vector_type(4)));
typedef unsigned u32;
typedef unsigned u32x2 __attribute__((ext_vector_type(2)));
typedef unsigned u32x4 __attribute__((ext_vector_type(4)));
typedef unsigned short u16;
#define LAS __attribute__((address_space(3)))
#define DI __device__ __forceinline__

constexpr int S_ = 2048, D_ = 640, F_ = 1728, DF_ = 321;
constexpr int KS1 = 20;
constexpr int KS2 = 54;
constexpr float EPS = 1e-5f;

constexpr size_t XT_B = (size_t)80 * 2048 * 16;
constexpr size_t H_B = (size_t)128 * KS2 * 1024;
constexpr size_t A0_B = (size_t)16 * KS1 * 7 * 1024;
constexpr size_t A1_B = (size_t)16 * KS1 * 4 * 1024;
constexpr size_t W2F_B = (size_t)8 * KS2 * 5 * 1024;
constexpr size_t WT_B = (size_t)320 * 8192;
constexpr size_t X3S_B = (size_t)40 * 128 * 512;
constexpr size_t OFF_X0 = 0, OFF_X1 = OFF_X0 + XT_B, OFF_X2 = OFF_X1 + XT_B, OFF_X3 = OFF_X2 + XT_B;
constexpr size_t OFF_H1 = OFF_X3 + XT_B, OFF_H2 = OFF_H1 + H_B;
constexpr size_t OFF_A0 = OFF_H2 + H_B, OFF_A1 = OFF_A0 + A0_B;
constexpr size_t OFF_W2F = OFF_A1 + A1_B, OFF_W2T = OFF_W2F + W2F_B;
constexpr size_t OFF_WT = OFF_W2T + W2F_B;
constexpr size_t OFF_X3S = OFF_WT + WT_B;
constexpr size_t OFF_RSTD0 = OFF_X3S + X3S_B;
constexpr size_t OFF_SS2 = OFF_RSTD0 + 8192;
constexpr size_t OFF_B2F = OFF_SS2 + 8192;
constexpr size_t OFF_TAB = OFF_B2F + 4096;
constexpr size_t OFF_CNT = OFF_TAB + 20480;
constexpr size_t WS_END = OFF_CNT + 8192;

DI u32 pk2(float a, float b) { f32x2 v = {a, b}; bf16x2 r = __builtin_convertvector(v, bf16x2); return __builtin_bit_cast(u32, r); }

DI float bflo(u32 w) { return __builtin_bit_cast(float, w << 16); }
DI float bfhi(u32 w) { return __builtin_bit_cast(float, w & 0xffff0000u); }
DI float rbf(float x) { return bflo(pk2(x, x)); }
DI f32x4 mfma(u32x4 a, u32x4 b, f32x4 c) {
  return __builtin_amdgcn_mfma_f32_16x16x32_bf16(__builtin_bit_cast(bf16x8, a), __builtin_bit_cast(bf16x8, b), c, 0, 0, 0);
}
DI float dot2(u32 a, u32 b, float c) { return __builtin_amdgcn_fdot2_f32_bf16(__builtin_bit_cast(bf16x2, a), __builtin_bit_cast(bf16x2, b), c, false); }
DI float cos2pi(float x) { return __builtin_amdgcn_cosf(x); }
DI float sin2pi(float x) { return __builtin_amdgcn_sinf(x); }
DI int s_of_p(int p) {
  const int r = p >> 9, i = (p >> 7) & 3, g = (p >> 5) & 3, uu = (p >> 4) & 1, c16 = p & 15;
  return 64 * (16 * uu + 4 * g + r) + 16 * i + c16;
}
DI void glds16(const void* g, LAS void* l) { __builtin_amdgcn_global_load_lds(g, l, 16, 0, 0); }
#define WAIT_VM0() asm volatile("s_waitcnt vmcnt(0)" ::: "memory")
#define WAIT_LG0() asm volatile("s_waitcnt lgkmcnt(0)" ::: "memory")
#define BAR() __builtin_amdgcn_s_barrier()
DI float ln_den(float var) { return rbf(__builtin_amdgcn_sqrtf(rbf(rbf(var) + EPS))); }
DI float rcp(float x) { return __builtin_amdgcn_rcpf(x); }

typedef int i32x4 __attribute__((ext_vector_type(4)));
DI i32x4 mk_rsrc(const void* p, u32 bytes) {
  const unsigned long long a = (unsigned long long)p;
  return i32x4{(int)__builtin_amdgcn_readfirstlane((u32)a), (int)__builtin_amdgcn_readfirstlane((u32)(a >> 32) & 0xffffu), (int)bytes, 0x00020000};
}
#define RFL(x) __builtin_amdgcn_readfirstlane(x)
#define PADS
template <int IMM> DI void dma16(i32x4 rsrc, u32 voff, u32 soff, u32 lds) {
  asm volatile(PADS "s_mov_b32 m0, %3\n\ts_nop 0\n\tbuffer_load_dwordx4 %0, %1, %2 offen offset:%4 lds" ::"v"(voff), "s"(rsrc), "s"(RFL(soff)), "s"(RFL(lds)), "i"(IMM) : "memory");
}
template <int IMM> DI void bld16(u32x4& dst, i32x4 rsrc, u32 voff, u32 soff) {
  asm volatile(PADS "buffer_load_dwordx4 %0, %1, %2, %3 offen offset:%4" : "=v"(dst) : "v"(voff), "s"(rsrc), "s"(RFL(soff)), "i"(IMM) : "memory");
}
DI void bldu16(u32& dst, i32x4 rsrc, u32 voff, u32 soff) {
  asm volatile(PADS "buffer_load_ushort %0, %1, %2, %3 offen" : "=v"(dst) : "v"(voff), "s"(rsrc), "s"(RFL(soff)) : "memory");
}
DI void bld8(u32x2& dst, i32x4 rsrc, u32 voff, u32 soff) {
  asm volatile(PADS "buffer_load_dwordx2 %0, %1, %2, %3 offen" : "=v"(dst) : "v"(voff), "s"(rsrc), "s"(RFL(soff)) : "memory");
}
DI void bld4(u32& dst, i32x4 rsrc, u32 voff, u32 soff) {
  asm volatile(PADS "buffer_load_dword %0, %1, %2, %3 offen" : "=v"(dst) : "v"(voff), "s"(rsrc), "s"(RFL(soff)) : "memory");
}
#define DMA16(rsrc, voff, soff, lds, IMM) dma16<IMM>(rsrc, voff, soff, lds)
#define BLD16(dst, rsrc, voff, soff, IMM) bld16<IMM>(dst, rsrc, voff, soff)
template <int N> DI void wait_vm_bar8(u32x4& a, u32x4& b, u32x4& c, u32x4& d, u32x4& e, u32x4& f, u32x4& g, u32x4& h) {
  asm volatile("s_waitcnt vmcnt(%8)\n\ts_barrier" : "+v"(a), "+v"(b), "+v"(c), "+v"(d), "+v"(e), "+v"(f), "+v"(g), "+v"(h) : "i"(N) : "memory");
}
#define WAIT_VM(N) asm volatile("s_waitcnt vmcnt(" #N ")" ::: "memory")
DI float red_g(float x) {
  const u32 xi = __builtin_bit_cast(u32, x);
  auto a = __builtin_amdgcn_permlane32_swap(xi, xi, false, false);
  const float y = __builtin_bit_cast(float, (u32)a[0]) + __builtin_bit_cast(float, (u32)a[1]);
  const u32 yi = __builtin_bit_cast(u32, y);
  auto c = __builtin_amdgcn_permlane16_swap(yi, yi, false, false);
  return __builtin_bit_cast(float, (u32)c[0]) + __builtin_bit_cast(float, (u32)c[1]);
}
DI u32 lds_addr(const void* p) { return (u32)(uintptr_t)(LAS const char*)p; }

template <int I> struct IC { static constexpr int value = I; constexpr operator int() const { return I; } };
template <int B, int E, class F> DI void static_for(F&& f) { if constexpr (B < E) { f(IC<B>{}); static_for<B + 1, E>(f); } }
#define SB() __builtin_amdgcn_sched_barrier(0)

#define CODE_ADDR(SYM, out) do { u32 lo_, hi_; \
  asm volatile("s_getpc_b64 s[20:21]\n\ts_add_u32 s20, s20, " SYM "@rel32@lo+4\n\ts_addc_u32 s21, s21, " SYM "@rel32@hi+12\n\ts_mov_b32 %0, s20\n\ts_mov_b32 %1, s21" \
               : "=s"(lo_), "=s"(hi_) :: "s20", "s21", "scc"); out = ((unsigned long long)hi_ << 32) | lo_; } while (0)
DI void touch_code(unsigned long long p, u32 bytes, int tid) {
  typedef __attribute__((address_space(1))) const u32x4 gv4;
  u32x4 v[4];
#pragma unroll
  for (int k = 0; k < 4; ++k) { const u32 o = (u32)tid * 16u + (u32)k * 8192u; v[k] = *(gv4*)(p + (o < bytes ? o : 0u)); }
#pragma unroll
  for (int k = 0; k < 4; ++k) asm volatile("" :: "v"(v[k]));
}
#define TLB_WARM 1
constexpr u32 K0_CODE_BYTES = 29696, COL_CODE_BYTES = 3584, MLP0_CODE_BYTES = 24576, MLP1_CODE_BYTES = 28672;

struct Params {
  const u16 *x, *wr, *wi, *w1, *b1, *w2, *b2;
  u16* out;
  char* ws;
};

#define STAMP(kid, i)
#define RSTAMP(kid, i)

struct QGen { float v0[8], v1[8], k; };
DI void gen_q_init(QGen& G, int ms, int lane) {
  const int c = ms * 16 + (lane & 15), g = lane >> 4;
  int jj = c >> 1; float amp = 0.05590169943749474f; float ph = (c & 1) ? 0.25f : 0.f;
  if (c < 2) { jj = (c == 0) ? 320 : 0; amp = 0.03952847075210474f; ph = 0.f; }
  const float fj = (float)jj * (1.f / 640.f);
  const float kj = 2.f * cos2pi(fj), d = (float)(8 * g);
  G.v0[0] = amp * cos2pi(__builtin_amdgcn_fractf(fj * d) + ph);          G.v0[1] = amp * cos2pi(__builtin_amdgcn_fractf(fj * (d + 1.f)) + ph);
  G.v1[0] = amp * cos2pi(__builtin_amdgcn_fractf(fj * (d + 32.f)) + ph); G.v1[1] = amp * cos2pi(__builtin_amdgcn_fractf(fj * (d + 33.f)) + ph);
#pragma unroll
  for (int j = 2; j < 8; ++j) { G.v0[j] = kj * G.v0[j - 1] - G.v0[j - 2]; G.v1[j] = kj * G.v1[j - 1] - G.v1[j - 2]; }
  G.k = 2.f * cos2pi(__builtin_amdgcn_fractf((float)jj * 0.05f));
}
DI u32x4 gen_q_step(QGen& G) {
  const u32x4 r = u32x4{pk2(G.v0[0], G.v0[1]), pk2(G.v0[2], G.v0[3]), pk2(G.v0[4], G.v0[5]), pk2(G.v0[6], G.v0[7])};
#pragma unroll
  for (int j = 0; j < 8; ++j) { const float n = G.k * G.v1[j] - G.v0[j]; G.v0[j] = G.v1[j]; G.v1[j] = n; }
  return r;
}

struct JobOps { i32x4 rs; u32 vo[10]; u32 l0; };
DI void job_ops(JobOps& J, const Params& P, int job, int w8, int lane, u32 lbuf) {
  asm volatile("" : "+v"(lane), "+s"(job), "+s"(w8));
  if (job >= 32 && job < 59) {
    const int d = 80 * w8 + (lane >> 3), pos = lane & 7;
    J.rs = mk_rsrc(P.w1, D_ * F_ * 2);
    const u32 va = (u32)d * (F_ * 2) + (u32)(job - 32) * 128u + 16u * (u32)(pos ^ (((d >> 1) & 1) << 1));
#pragma unroll
    for (int k = 0; k < 10; ++k) J.vo[k] = ((k & 1) ? (va ^ 64u) : va) + (u32)k * (8 * F_ * 2);
    J.l0 = lbuf + (u32)w8 * 10240u;
  } else {
    const int nn = w8 >> 1, hh = w8 & 1;
    u32 s0;
    if (job < 32) { J.rs = mk_rsrc(P.x, S_ * D_ * 2); s0 = (u32)s_of_p(job * 64 + nn * 16) * (D_ * 2); }
    else if (job < 86) { J.rs = mk_rsrc(P.w2, F_ * D_ * 2); s0 = (u32)((job - 59) * 64 + nn * 16) * (D_ * 2); }
    else { J.rs = mk_rsrc(P.b2, D_ * 2); s0 = 0; }
    J.l0 = lbuf + (u32)nn * 20480u + (u32)hh * 10240u;
    const u32 va = (u32)lane * 16u;
    if (hh == 0) {
#pragma unroll
      for (int k = 0; k < 10; ++k) J.vo[k] = (va ^ (u32)(k * 16)) + (s0 + k * 1280u);
    } else {
      const u32 vb = (u32)(lane >> 4) * 1280u + 1024u + (((u32)(lane & 15) ^ (u32)(lane >> 4)) * 16u);
#pragma unroll
      for (int k = 0; k < 6; ++k) J.vo[k] = (va ^ (u32)((10 + k) * 16)) + (s0 + (10 + k) * 1280u);
#pragma unroll
      for (int k = 6; k < 10; ++k) J.vo[k] = (vb ^ (u32)((k - 6) * 64)) + (s0 + (k - 6) * 5120u);
    }
  }
}
template <int K> DI void job_op(const JobOps& J) {
  if constexpr (K == 0) asm volatile(PADS "s_mov_b32 m0, %2\n\ts_nop 0\n\tbuffer_load_dwordx4 %0, %1, 0 offen lds\n\ts_add_u32 m0, m0, 0x400" ::"v"(J.vo[0]), "s"(J.rs), "s"(RFL(J.l0)) : "memory", "scc");
  else asm volatile(PADS "buffer_load_dwordx4 %0, %1, 0 offen lds\n\ts_add_u32 m0, m0, 0x400" ::"v"(J.vo[K]), "s"(J.rs) : "memory", "scc");
}

DI u32x4 ident_frag(int par, int lane) {
  const int j = 16 * par + (lane & 15) - 8 * (lane >> 4);
  u32x4 r;
#pragma unroll
  for (int k = 0; k < 4; ++k) r[k] = (j == 2 * k ? 0x3F80u : 0u) | (j == 2 * k + 1 ? 0x3F800000u : 0u);
  return r;
}

template <int MODE, class HK>
DI void qloop_lin(const char* buf, int sl, int lane, u32x4 (&q)[2][KS1], f32x4 (&acc)[2][2], f32x4& sum, f32x4& gram, HK&& hook) {
  u32x4 bf[2][2];
  const int row = lane & 15, g = lane >> 4;
  const char* ba[4];
#pragma unroll
  for (int j = 0; j < 4; ++j) ba[j] = buf + row * 1024 + (((4 * j + g) ^ row) * 16);
  const int dB = 16384 - row * 768;
  auto rd = [&](auto T, auto N) { constexpr int t = T, n = N;
    if constexpr (t < 16) bf[t & 1][n] = *(const u32x4*)(ba[t & 3] + n * 20480 + (t >> 2) * 256);
    else if constexpr (t < KS1) bf[t & 1][n] = *(const u32x4*)(ba[t & 3] + dB + n * 20480); };
  rd(IC<0>{}, IC<0>{}); rd(IC<0>{}, IC<1>{});
  static_for<0, KS1>([&](auto T) {
    constexpr int t = T;
    rd(IC<t + 1>{}, IC<0>{}); rd(IC<t + 1>{}, IC<1>{});
    __builtin_amdgcn_s_waitcnt(0xC07F | ((t + 1 < KS1 ? 2 : 0) << 8));
    SB();
    static_for<0, 2>([&](auto N) {
      constexpr int n = N;
      if constexpr (MODE == 2) acc[0][n] = mfma(bf[t & 1][n], q[0][t], acc[0][n]); else acc[0][n] = mfma(q[0][t], bf[t & 1][n], acc[0][n]);
      if constexpr (MODE == 2) acc[1][n] = mfma(bf[t & 1][n], q[1][t], acc[1][n]); else acc[1][n] = mfma(q[1][t], bf[t & 1][n], acc[1][n]);
      SB();
      hook(T, N);
      SB();
    });
    if constexpr (MODE == 1) {
      const u32x4 e = sl ? bf[t & 1][1] : bf[t & 1][0];
      sum = mfma(e, u32x4{0x3F803F80u, 0x3F803F80u, 0x3F803F80u, 0x3F803F80u}, sum);
      gram = mfma(e, e, gram);
      SB();
    }
  });
}
template <class HK>
DI void qloop_tr(const char* buf, int nh, int lane, u32x4 (&q)[2][KS1], f32x4 (&acc)[2][2], HK&& hook) {
  const int g = lane >> 4, pp = lane & 3, qq = (lane & 15) >> 2;
  const int sw = (((qq >> 1) & 1) | ((g & 1) << 1)) << 1;
  const char* base[2];
#pragma unroll
  for (int n = 0; n < 2; ++n) base[n] = buf + (8 * g + qq) * 128 + ((2 * (2 * nh + n) + (pp >> 1)) ^ sw) * 16 + (pp & 1) * 8;
  u32x2 lo[2][2], hi[2][2];
  auto rd = [&](auto T, auto N) { constexpr int t = T, n = N;
    if constexpr (t < KS1) {
      lo[t & 1][n] = __builtin_bit_cast(u32x2, __builtin_amdgcn_ds_read_tr16_b64_v4bf16((LAS bf16x4*)(base[n] + t * 4096)));
      hi[t & 1][n] = __builtin_bit_cast(u32x2, __builtin_amdgcn_ds_read_tr16_b64_v4bf16((LAS bf16x4*)(base[n] + t * 4096 + 512)));
    } };
  rd(IC<0>{}, IC<0>{}); rd(IC<0>{}, IC<1>{});
  static_for<0, KS1>([&](auto T) {
    constexpr int t = T;
    rd(IC<t + 1>{}, IC<0>{}); rd(IC<t + 1>{}, IC<1>{});
    __builtin_amdgcn_s_waitcnt(0xC07F | ((t + 1 < KS1 ? 4 : 0) << 8));
    SB();
    static_for<0, 2>([&](auto N) {
      constexpr int n = N;
      const u32x4 b = u32x4{lo[t & 1][n][0], lo[t & 1][n][1], hi[t & 1][n][0], hi[t & 1][n][1]};
      acc[0][n] = mfma(q[0][t], b, acc[0][n]);
      acc[1][n] = mfma(q[1][t], b, acc[1][n]);
      SB();
      hook(T, N);
      SB();
    });
  });
}

DI u32x4 row_frag(const char* buf, int n, int t, int lane) {
  const int row = lane & 15, g = lane >> 4, x = (((4 * (t & 3) + g) ^ row) * 16);
  return *(const u32x4*)(buf + n * 20480 + (t < 16 ? row * 1024 + (t >> 2) * 256 : 16384 + row * 256) + x);
}
typedef __attribute__((ext_vector_type(2))) unsigned int v2u_t;
#define HAUX 16
DI void bst8(__amdgpu_buffer_rsrc_t rs, u32 voff, u32 soff, const f32x4& a) {
  __builtin_amdgcn_raw_buffer_store_b64(v2u_t{pk2(a[0], a[1]), pk2(a[2], a[3])}, rs, voff, soff, 0);
}
template <class HK>
DI void qjob_compute(const Params& P, int job, int ct5, int mp, int nh, int lane, const char* buf, u32x4 (&q)[2][KS1], HK&& hook) {
  asm volatile("" : "+s"(job), "+s"(ct5), "+s"(mp), "+s"(nh), "+v"(lane));
  const int ms0 = ct5 * 8 + 2 * mp, tc = 4 * ct5 + mp;
  f32x4 acc[2][2], sum = {0.f, 0.f, 0.f, 0.f}, gram = sum;
  acc[0][0] = sum; acc[0][1] = sum; acc[1][0] = sum; acc[1][1] = sum;
  const char* bl = buf + nh * 40960;
  const __amdgpu_buffer_rsrc_t rsw = __builtin_amdgcn_make_buffer_rsrc((void*)P.ws, 0, (int)WS_END, 0x00020000);
  auto vXf = [&]() { asm volatile("" : "+v"(lane)); return (u32)(lane >> 5) * 32768u + (u32)((lane >> 4) & 1) * 8u + (u32)(lane & 15) * 16u; };
  auto vWf = [&]() { asm volatile("" : "+v"(lane)); return ((u32)(lane >> 5) * 16u + (u32)(lane & 15)) * 16u + (u32)((lane >> 4) & 1) * 8u; };
  if (job < 32) {
    if (ct5 < 4 && mp == 3 && nh == (ct5 >> 1)) {
      qloop_lin<1>(bl, ct5 & 1, lane, q, acc, sum, gram, hook);
      const int c16 = lane & 15, g = lane >> 4, r = c16 & 3;
      const float s1 = r == 0 ? sum[0] : r == 1 ? sum[1] : r == 2 ? sum[2] : sum[3];
      const float s2 = r == 0 ? gram[0] : r == 1 ? gram[1] : r == 2 ? gram[2] : gram[3];
      const float m = s1 * (1.f / 640.f);
      float var = s2 * (1.f / 640.f) - m * m; var = var < 0.f ? 0.f : var;
      if (g == (c16 >> 2)) ((float*)(P.ws + OFF_RSTD0))[job * 64 + ct5 * 16 + c16] = rcp(ln_den(var));
    } else {
      qloop_lin<0>(bl, 0, lane, q, acc, sum, gram, hook);
    }
    const u32 vX = vXf();
#pragma unroll
    for (int a = 0; a < 2; ++a) {
      const u32 so = (u32)OFF_X0 + (u32)(2 * (ms0 + a) * 2048 + job * 64 + nh * 32) * 16u;
      bst8(rsw, vX, so, acc[a][0]); bst8(rsw, vX, so + 256u, acc[a][1]);
    }
  } else if (job < 59) {
    qloop_tr(buf, nh, lane, q, acc, hook);
    const u32 vW = vWf();
#pragma unroll
    for (int n = 0; n < 2; ++n) {
      const int fs = (job - 32) * 4 + 2 * nh + n;
      if (ms0 == 0 && lane < 16) acc[0][n][1] = 0.f;
      const int ft = fs < 84 ? fs / 7 : 12 + (fs - 84) / 6, m = fs < 84 ? fs - 7 * ft : (fs - 84) - 6 * (ft - 12);
      const u32 so = (u32)OFF_A0 + (u32)((ft * KS1 + tc) * 7 + m) * 1024u;
      bst8(rsw, vW, so, acc[0][n]); bst8(rsw, vW, so + 512u, acc[1][n]);
    }
  } else if (job < 86) {
    qloop_lin<2>(bl, 0, lane, q, acc, sum, gram, hook);
    const u32 vW = vWf();
#pragma unroll
    for (int a = 0; a < 2; ++a) {
      const int ms = ms0 + a, ct = ms / 5;
      const u32x4 id = ident_frag(a, lane);
#pragma unroll
      for (int n = 0; n < 2; ++n) {
        const f32x4 tr = mfma(row_frag(bl, n, tc, lane), id, f32x4{0.f, 0.f, 0.f, 0.f});
        const int fs = (job - 59) * 4 + 2 * nh + n;
        const u32 o = (u32)((ct * KS2 + (fs >> 1)) * 5 + (ms - 5 * ct)) * 1024u + (u32)(fs & 1) * 512u;
        bst8(rsw, vW, (u32)OFF_W2F + o, acc[a][n]);
        bst8(rsw, vW, (u32)OFF_W2T + o, tr);
      }
    }
  } else if (job == 86) {
    if (nh == 0) {
#pragma unroll
      for (int t = 0; t < KS1; ++t) { const u32x4 b = row_frag(buf, 0, t, lane); acc[0][0] = mfma(b, q[0][t], acc[0][0]); acc[1][0] = mfma(b, q[1][t], acc[1][0]); }
      if (lane < 16) { ((float*)(P.ws + OFF_B2F))[ms0 * 16 + lane] = acc[0][0][0]; ((float*)(P.ws + OFF_B2F))[ms0 * 16 + 16 + lane] = acc[1][0][0]; }
    }
  } else {
    const u32x4 id = ident_frag(nh, lane);
    const u32 vW = vWf();
    static_for<0, KS1>([&](auto T) {
      constexpr int t = T;
      const u32 fm = t < 12 ? (u32)(t * KS1 * 4) + (u32)nh : (u32)((12 + (t - 12) / 2) * KS1 * 4 + ((t - 12) & 1) * 2) + (u32)nh;
      const u32 so = (u32)OFF_A1 + (fm + (u32)tc * 4u) * 1024u;
      bst8(rsw, vW, so, mfma(q[0][t], id, f32x4{0.f, 0.f, 0.f, 0.f}));
      bst8(rsw, vW, so + 512u, mfma(q[1][t], id, f32x4{0.f, 0.f, 0.f, 0.f}));
    });
  }
}

__global__ void __launch_bounds__(512, 2) k0_prep(Params P) {
  extern __shared__ __attribute__((aligned(16))) char smem[];
  const int b = blockIdx.x, tid = threadIdx.x, lane = tid & 63, w8 = __builtin_amdgcn_readfirstlane(tid >> 6);
  RSTAMP(0, 14); STAMP(0, 0);
  if (b < 240) {
    const int li = b >> 3, ct5 = li % 5, j0 = (b & 7) * 6 + li / 5, j1 = j0 + 48;
    const bool two = j1 < 88;
    const int mp = w8 & 3, nh = w8 >> 2;
    const u32 lb = lds_addr(smem);
    JobOps J;
    job_ops(J, P, j0, w8, lane, lb);
    static_for<0, 4>([&](auto K) { job_op<K>(J); asm volatile("s_nop 0"); });
    const int WT = b < 160 ? b : -1, wa = WT & 15, wcg = (WT >= 0) ? (WT >> 4) : 0;
    u32x2 wv[2][2];
    u32 w0r, w0i;
    u32x4 q[2][KS1];
    QGen G0;
    constexpr int GL = 4;
    {
      QGen G1;
      gen_q_init(G1, ct5 * 8 + 2 * mp + 1, lane);
      static_for<0, 6>([&](auto T) { constexpr int t = T; SB(); job_op<4 + t>(J); SB(); q[1][t] = gen_q_step(G1); });
      static_for<6, KS1>([&](auto T) { constexpr int t = T; q[1][t] = gen_q_step(G1); });
#pragma unroll
      for (int t = 0; t < KS1; ++t) asm volatile("" : "+v"(q[1][t]));
    }
    job_ops(J, P, two ? j1 : 86, w8, lane, lb + 81920);
    static_for<0, 4>([&](auto K) { job_op<K>(J); asm volatile("s_nop 0"); });
    SB();
    gen_q_init(G0, ct5 * 8 + 2 * mp, lane);
    static_for<0, GL>([&](auto T) { constexpr int t = T; SB(); job_op<4 + t>(J); SB(); q[0][t] = gen_q_step(G0); });
    SB(); job_op<8>(J); asm volatile("s_nop 0"); job_op<9>(J); SB();
    STAMP(0, 1);
    asm volatile("s_waitcnt vmcnt(10)\n\ts_barrier" ::: "memory");
    STAMP(0, 2);
    qjob_compute(P, j0, ct5, mp, nh, lane, smem, q, [&](auto T, auto N) {
      constexpr int t = T, n = N;
      if constexpr (t + GL < KS1 && n == 0) q[0][t + GL] = gen_q_step(G0);
    });
    {
      const i32x4 rwr = mk_rsrc(P.wr, S_ * DF_ * 2), rwi = mk_rsrc(P.wi, S_ * DF_ * 2);
      const u32 wvo = (u32)(lane >> 3) * (DF_ * 2) + (u32)(lane & 7) * 8u + (u32)wcg * 64u;
      const u32 wso = (u32)((16 * nh + 32 * (4 * wa + mp)) * DF_ * 2);
      bld8(wv[0][0], rwr, wvo, wso); bld8(wv[0][1], rwi, wvo, wso);
      bld8(wv[1][0], rwr, wvo, wso + 8 * DF_ * 2); bld8(wv[1][1], rwi, wvo, wso + 8 * DF_ * 2);
      bldu16(w0r, rwr, (u32)(lane & 15) * (DF_ * 2) + 640u, wso); bldu16(w0i, rwi, (u32)(lane & 15) * (DF_ * 2) + 640u, wso);
    }
    STAMP(0, 3);
    asm volatile("s_waitcnt vmcnt(10) lgkmcnt(0)\n\ts_barrier" ::: "memory");
    STAMP(0, 4);
    if (two) qjob_compute(P, j1, ct5, mp, nh, lane, smem + 81920, q, [&](auto T, auto N) {});
    STAMP(0, 5);
    asm volatile("s_cmp_lg_u32 %6, 0\n\ts_cbranch_scc1 1f\n\ts_waitcnt vmcnt(0)\n1:\n\ts_waitcnt vmcnt(4)"
                 : "+v"(wv[0][0]), "+v"(wv[0][1]), "+v"(wv[1][0]), "+v"(wv[1][1]), "+v"(w0r), "+v"(w0i) : "s"(RFL((two && j1 != 86) ? 1 : 0)) : "memory", "scc");
    if (WT >= 0) {
      u32* st = (u32*)smem;
#pragma unroll
      for (int i = 0; i < 2; ++i) {
        const u32 r0 = wv[i][0][0], r1 = wv[i][0][1], i0 = wv[i][1][0], i1 = wv[i][1][1];
        u32* d = st + (4 * (lane & 7)) * 132 + (16 * nh + 8 * i + (lane >> 3)) * 4 + mp;
        d[0] = (r0 & 0xffffu) | (i0 << 16); d[132] = (r0 >> 16) | (i0 & 0xffff0000u);
        d[264] = (r1 & 0xffffu) | (i1 << 16); d[396] = (r1 >> 16) | (i1 & 0xffff0000u);
      }
      if (wcg == 0) {
        asm volatile("s_waitcnt lgkmcnt(0)\n\ts_barrier" ::: "memory");
        if (lane < 16) st[(16 * nh + lane) * 4 + mp] = w0r | (w0i << 16);
      }
      asm volatile("s_waitcnt lgkmcnt(0)\n\ts_barrier" ::: "memory");
      const int v = wa >> 3, ii = (wa >> 2) & 1, g = wa & 3;
#pragma unroll
      for (int k = 0; k < 2; ++k) {
        const int it = k * 512 + tid, cl = it >> 5, k1 = it & 31;
        const u32x4 o = *(const u32x4*)(st + cl * 132 + k1 * 4);
        *(u32x4*)(P.ws + OFF_WT + ((((size_t)(32 * wcg + cl) * 4 + (k1 >> 4) * 2 + v) * 2 + ii) * 64 + g * 16 + (k1 & 15)) * 16) = o;
      }
    }
    STAMP(0, 10); RSTAMP(0, 15);
  } else if (b < 248) {
    unsigned long long pc;
    CODE_ADDR("_Z7k0_prep6Params", pc); touch_code(pc, K0_CODE_BYTES, tid);
    CODE_ADDR("_Z5k_colILi0EEv6Params", pc); touch_code(pc, COL_CODE_BYTES, tid);
    {
      typedef __attribute__((address_space(1))) const u32 gu1;
      const u32 o1 = (u32)tid * 65536u, o2 = o1 + 512u * 65536u;
      const u32 a = *(gu1*)((unsigned long long)P.ws + (o1 < (u32)WS_END ? o1 : 0u));
      const u32 c = *(gu1*)((unsigned long long)P.ws + (o2 < (u32)WS_END ? o2 : 0u));
      const u32 d = *(gu1*)((unsigned long long)P.out + (o1 < (u32)(S_ * D_ * 2) ? o1 : 0u));
      asm volatile("" :: "v"(a), "v"(c), "v"(d));
    }
  } else if (b == 248) {
    const int c16 = lane & 15, g = lane >> 4;
    for (int fr = w8; fr < 20; fr += 8) {
      float v[8];
      if (fr < 16) {
        const int tile = fr >> 2, t = (fr >> 1) & 1, part = fr & 1;
#pragma unroll
        for (int j = 0; j < 8; ++j) {
          const int kk = 32 * t + 16 * (j >> 2) + 4 * g + (j & 3);
          const float a = (float)(((16 * tile + c16) * kk) & 63) * (1.f / 64.f);
          v[j] = part ? -sin2pi(a) : cos2pi(a);
        }
      } else {
        const int u = (fr - 16) >> 1, part = fr & 1;
#pragma unroll
        for (int j = 0; j < 8; ++j) {
          const int kk = 16 * (j >> 2) + 4 * g + (j & 3);
          const float a = (float)(((16 * u + c16) * kk) & 31) * (1.f / 32.f);
          v[j] = part ? -sin2pi(a) : cos2pi(a);
        }
      }
      *(u32x4*)(P.ws + OFF_TAB + fr * 1024 + lane * 16) = u32x4{pk2(v[0], v[1]), pk2(v[2], v[3]), pk2(v[4], v[5]), pk2(v[6], v[7])};
    }
  } else if (b == 249) {
    for (int i = tid; i < 2048; i += 512) { ((float*)(P.ws + OFF_SS2))[i] = 0.f; ((u32*)(P.ws + OFF_CNT))[i] = 0u; }
  }
}

template <int LAYER>
__global__ void __launch_bounds__(512, 2) k_col(Params P) {
  extern __shared__ __attribute__((aligned(16))) char smem[];
  const int b = blockIdx.x, tid = threadIdx.x, lane = tid & 63, w = __builtin_amdgcn_readfirstlane(tid >> 6);
  char* ws = P.ws;
  if (b >= 160) {
    unsigned long long pc;
    const int role = (b - 160) >> 3;
    if (role == 0) { if (LAYER == 0) CODE_ADDR("_Z5k_mlpILi0EEv6Params", pc); else CODE_ADDR("_Z5k_mlpILi1EEv6Params", pc); touch_code(pc, LAYER == 0 ? MLP0_CODE_BYTES : MLP1_CODE_BYTES, tid); }
    else if (role == 1 && LAYER == 0) { CODE_ADDR("_Z5k_colILi1EEv6Params", pc); touch_code(pc, COL_CODE_BYTES, tid); }
    else if (role == 2 && LAYER == 0) { CODE_ADDR("_Z5k_mlpILi1EEv6Params", pc); touch_code(pc, MLP1_CODE_BYTES, tid); }
    return;
  }
  constexpr int KID = LAYER == 0 ? 1 : 4;
  RSTAMP(KID, 14); STAMP(KID, 0);
  const int oct = (b & 7) + 8 * (b >> 4), hf = (b >> 3) & 1;
  char* sA = smem;
  char* sT = smem + 16384;
  char* sE = smem + 36864;
  char* sG = smem + 53248;
  char* sY = smem + 69632;
  const int cc = w >> 2, u = (w >> 1) & 1, v = w & 1, c16 = lane & 15, g = lane >> 4;
  const int col = oct * 4 + hf * 2 + cc;
  const i32x4 rsw = mk_rsrc(ws, (u32)WS_END);
  const u32 l16 = lane * 16;
  u32x2 raw[4]; u32 st4[4]; u32x4 wv[2];
  const u32 xo = (u32)(LAYER == 0 ? OFF_X0 : OFF_X2) + (u32)oct * 32768u, xv = (u32)tid * 16u + (u32)hf * 8u;
#pragma unroll
  for (int r = 0; r < 4; ++r) bld8(raw[r], rsw, xv, xo + r * 8192u);
#pragma unroll
  for (int r = 0; r < 4; ++r) bld4(st4[r], rsw, (u32)tid * 4u, (u32)(LAYER == 0 ? OFF_RSTD0 : OFF_SS2) + r * 2048u);
  dma16<0>(rsw, l16, (u32)OFF_TAB + w * 1024u, lds_addr(sT) + w * 1024u);
  dma16<0>(rsw, l16, (u32)OFF_TAB + (8 + w) * 1024u, lds_addr(sT) + (8 + w) * 1024u);
  dma16<0>(rsw, l16, (u32)OFF_TAB + (16 + (w & 3)) * 1024u, lds_addr(sT) + (16 + (w & 3)) * 1024u);
  {
    const u32 wo = (u32)OFF_WT + (u32)((col * 4 + u * 2 + v) * 2) * 1024u;
    bld16<0>(wv[0], rsw, l16, wo);
    bld16<1024>(wv[1], rsw, l16, wo);
  }
  SB();
  float twc[16], tws[16], itc[8], its[8];
  {
    const float fk = (float)(16 * u + c16) * (1.f / 2048.f);
    const float c0 = cos2pi((float)(4 * g) * fk), s0 = sin2pi((float)(4 * g) * fk);
    const float cr = cos2pi(fk), sr = sin2pi(fk), ci = cos2pi(16.f * fk), si = sin2pi(16.f * fk);
    twc[0] = c0; tws[0] = s0;
#pragma unroll
    for (int r = 1; r < 4; ++r) { twc[r] = twc[r - 1] * cr - tws[r - 1] * sr; tws[r] = tws[r - 1] * cr + twc[r - 1] * sr; }
#pragma unroll
    for (int i = 1; i < 4; ++i)
#pragma unroll
      for (int r = 0; r < 4; ++r) {
        twc[i * 4 + r] = twc[(i - 1) * 4 + r] * ci - tws[(i - 1) * 4 + r] * si;
        tws[i * 4 + r] = tws[(i - 1) * 4 + r] * ci + twc[(i - 1) * 4 + r] * si;
      }
#pragma unroll
    for (int ii = 0; ii < 2; ++ii) {
      const float fm = (float)(16 * (2 * v + ii) + c16) * (1.f / 2048.f);
      const float a0 = (float)(16 * u + 4 * g) * fm;
      const float cm = cos2pi(fm), sm = sin2pi(fm);
      itc[ii * 4] = cos2pi(a0) * (1.f / 2048.f); its[ii * 4] = sin2pi(a0) * (1.f / 2048.f);
#pragma unroll
      for (int r = 1; r < 4; ++r) {
        itc[ii * 4 + r] = itc[ii * 4 + r - 1] * cm - its[ii * 4 + r - 1] * sm;
        its[ii * 4 + r] = its[ii * 4 + r - 1] * cm + itc[ii * 4 + r - 1] * sm;
      }
    }
#pragma unroll
    for (int k = 0; k < 16; ++k) asm volatile("" : "+v"(twc[k]), "+v"(tws[k]));
#pragma unroll
    for (int k = 0; k < 8; ++k) asm volatile("" : "+v"(itc[k]), "+v"(its[k]));
  }
  STAMP(KID, 1);
  asm volatile("s_waitcnt vmcnt(5)" : "+v"(raw[0]), "+v"(raw[1]), "+v"(raw[2]), "+v"(raw[3]), "+v"(st4[0]), "+v"(st4[1]), "+v"(st4[2]), "+v"(st4[3]) :: "memory");
  const bool special = (oct == 0 && hf == 0);
  {
    float val[4][4];
#pragma unroll
    for (int r = 0; r < 4; ++r) {
      const float sv = __builtin_bit_cast(float, st4[r]);
      const float rs = (LAYER == 0) ? sv : __builtin_amdgcn_rcpf(ln_den(sv * (1.f / 640.f)));
      val[r][0] = bflo(raw[r][0]) * rs; val[r][1] = special ? 0.f : bfhi(raw[r][0]) * rs;
      val[r][2] = bflo(raw[r][1]) * rs; val[r][3] = bfhi(raw[r][1]) * rs;
    }
    const int ti = tid >> 7, tg = (tid >> 5) & 3, tu = (tid >> 4) & 1, tc = tid & 15;
#pragma unroll
    for (int k = 0; k < 4; ++k)
      *(u32x2*)(sA + (k * 4 + ti) * 1024 + (tg * 16 + tc) * 16 + tu * 8) = u32x2{pk2(val[0][k], val[1][k]), pk2(val[2][k], val[3][k])};
  }
  STAMP(KID, 2);
  asm volatile("s_waitcnt vmcnt(2) lgkmcnt(0)\n\ts_barrier" ::: "memory");
  STAMP(KID, 3);
  u32x4 f64r[2][2], f64i[2][2], f64n[2][2], f32r, f32i, f32n;
  f32r = *(const u32x4*)(sT + (16 + u * 2 + 0) * 1024 + l16);
  f32i = *(const u32x4*)(sT + (16 + u * 2 + 1) * 1024 + l16);
  f32n = f32i ^ 0x80008000u;
  const f32x4 Z4 = {0.f, 0.f, 0.f, 0.f};
  u32x4 yr[2], yi[2];
  {
    u32x4 xr[4], xi[4];
#pragma unroll
    for (int i = 0; i < 4; ++i) {
      xr[i] = *(const u32x4*)(sA + ((cc * 2 + 0) * 4 + i) * 1024 + l16);
      xi[i] = *(const u32x4*)(sA + ((cc * 2 + 1) * 4 + i) * 1024 + l16);
    }
#pragma unroll
    for (int ii = 0; ii < 2; ++ii)
#pragma unroll
      for (int t = 0; t < 2; ++t) {
        f64r[ii][t] = *(const u32x4*)(sT + (((2 * v + ii) * 2 + t) * 2 + 0) * 1024 + l16);
        f64i[ii][t] = *(const u32x4*)(sT + (((2 * v + ii) * 2 + t) * 2 + 1) * 1024 + l16);
        f64n[ii][t] = f64i[ii][t] ^ 0x80008000u;
      }
    f32x4 dr[4], di[4];
#pragma unroll
    for (int i = 0; i < 4; ++i) { dr[i] = mfma(xr[i], f32r, Z4); di[i] = mfma(xr[i], f32i, Z4); }
#pragma unroll
    for (int i = 0; i < 4; ++i) { dr[i] = mfma(xi[i], f32n, dr[i]); di[i] = mfma(xi[i], f32r, di[i]); }
    float tr[16], tq[16];
#pragma unroll
    for (int i = 0; i < 4; ++i)
#pragma unroll
      for (int r = 0; r < 4; ++r) {
        tr[i * 4 + r] = dr[i][r] * twc[i * 4 + r] + di[i][r] * tws[i * 4 + r];
        tq[i * 4 + r] = di[i][r] * twc[i * 4 + r] - dr[i][r] * tws[i * 4 + r];
      }
#pragma unroll
    for (int t = 0; t < 2; ++t) {
      yr[t] = u32x4{pk2(tr[8 * t + 0], tr[8 * t + 1]), pk2(tr[8 * t + 2], tr[8 * t + 3]), pk2(tr[8 * t + 4], tr[8 * t + 5]), pk2(tr[8 * t + 6], tr[8 * t + 7])};
      yi[t] = u32x4{pk2(tq[8 * t + 0], tq[8 * t + 1]), pk2(tq[8 * t + 2], tq[8 * t + 3]), pk2(tq[8 * t + 4], tq[8 * t + 5]), pk2(tq[8 * t + 6], tq[8 * t + 7])};
    }
  }
  u32x4 zr[2], zi[2];
  {
    f32x4 dr[2] = {Z4, Z4}, di[2] = {Z4, Z4};
#pragma unroll
    for (int t = 0; t < 2; ++t) {
#pragma unroll
      for (int ii = 0; ii < 2; ++ii) { dr[ii] = mfma(f64r[ii][t], yr[t], dr[ii]); di[ii] = mfma(f64i[ii][t], yr[t], di[ii]); }
#pragma unroll
      for (int ii = 0; ii < 2; ++ii) { dr[ii] = mfma(f64n[ii][t], yi[t], dr[ii]); di[ii] = mfma(f64r[ii][t], yi[t], di[ii]); }
    }
    asm volatile("s_waitcnt vmcnt(0)" : "+v"(wv[0]), "+v"(wv[1]) :: "memory");
    float pr[8], pi[8];
#pragma unroll
    for (int ii = 0; ii < 2; ++ii)
#pragma unroll
      for (int r = 0; r < 4; ++r) {
        const u32 wd = wv[ii][r];
        const float a = bflo(wd), bb = bfhi(wd);
        pr[ii * 4 + r] = dr[ii][r] * a - di[ii][r] * bb;
        pi[ii * 4 + r] = dr[ii][r] * bb + di[ii][r] * a;
      }
    const u32x4 mr = u32x4{pk2(pr[0], pr[1]), pk2(pr[2], pr[3]), pk2(pr[4], pr[5]), pk2(pr[6], pr[7])};
    const u32x4 mi = u32x4{pk2(pi[0], pi[1]), pk2(pi[2], pi[3]), pk2(pi[4], pi[5]), pk2(pi[6], pi[7])};
    *(u32x4*)(sE + ((((cc * 2 + u) * 2 + v) * 2 + 0) * 1024) + l16) = mr;
    *(u32x4*)(sE + ((((cc * 2 + u) * 2 + v) * 2 + 1) * 1024) + l16) = mi;
    STAMP(KID, 4);
    asm volatile("s_waitcnt lgkmcnt(0)\n\ts_barrier" ::: "memory");
    const u32x4 or_ = *(const u32x4*)(sE + ((((cc * 2 + u) * 2 + (1 - v)) * 2 + 0) * 1024) + l16);
    const u32x4 oi_ = *(const u32x4*)(sE + ((((cc * 2 + u) * 2 + (1 - v)) * 2 + 1) * 1024) + l16);
    zr[0] = v ? or_ : mr; zi[0] = v ? oi_ : mi;
    zr[1] = v ? mr : or_; zi[1] = v ? mi : oi_;
  }
  {
    f32x4 dr[2] = {Z4, Z4}, di[2] = {Z4, Z4};
#pragma unroll
    for (int t = 0; t < 2; ++t) {
#pragma unroll
      for (int ii = 0; ii < 2; ++ii) { dr[ii] = mfma(zr[t], f64r[ii][t], dr[ii]); di[ii] = mfma(zr[t], f64n[ii][t], di[ii]); }
#pragma unroll
      for (int ii = 0; ii < 2; ++ii) { dr[ii] = mfma(zi[t], f64i[ii][t], dr[ii]); di[ii] = mfma(zi[t], f64r[ii][t], di[ii]); }
    }
#pragma unroll
    for (int ii = 0; ii < 2; ++ii) {
      float tr[4], tq[4];
#pragma unroll
      for (int r = 0; r < 4; ++r) {
        tr[r] = dr[ii][r] * itc[ii * 4 + r] - di[ii][r] * its[ii * 4 + r];
        tq[r] = dr[ii][r] * its[ii * 4 + r] + di[ii][r] * itc[ii * 4 + r];
      }
      *(u32x2*)(sG + ((((cc * 2 + v) * 2 + ii) * 2 + 0) * 1024) + l16 + u * 8) = u32x2{pk2(tr[0], tr[1]), pk2(tr[2], tr[3])};
      *(u32x2*)(sG + ((((cc * 2 + v) * 2 + ii) * 2 + 1) * 1024) + l16 + u * 8) = u32x2{pk2(tq[0], tq[1]), pk2(tq[2], tq[3])};
    }
  }
  STAMP(KID, 5);
  asm volatile("s_waitcnt lgkmcnt(0)\n\ts_barrier" ::: "memory");
  {
    u32x4 tr[2], tq[2];
#pragma unroll
    for (int ii = 0; ii < 2; ++ii) {
      tr[ii] = *(const u32x4*)(sG + ((((cc * 2 + v) * 2 + ii) * 2 + 0) * 1024) + l16);
      tq[ii] = *(const u32x4*)(sG + ((((cc * 2 + v) * 2 + ii) * 2 + 1) * 1024) + l16);
    }
    f32x4 orr[2], oi[2];
#pragma unroll
    for (int ii = 0; ii < 2; ++ii) { orr[ii] = mfma(f32r, tr[ii], Z4); oi[ii] = mfma(f32r, tq[ii], Z4); }
#pragma unroll
    for (int ii = 0; ii < 2; ++ii) { orr[ii] = mfma(f32i, tq[ii], orr[ii]); oi[ii] = mfma(f32n, tr[ii], oi[ii]); }
#pragma unroll
    for (int ii = 0; ii < 2; ++ii) {
      const int tt = (2 * v + ii) * 128 + g * 32 + u * 16 + c16;
      *(u32x4*)(sY + (cc * 512 + tt) * 16) = u32x4{pk2(orr[ii][0], oi[ii][0]), pk2(orr[ii][1], oi[ii][1]), pk2(orr[ii][2], oi[ii][2]), pk2(orr[ii][3], oi[ii][3])};
    }
  }
  STAMP(KID, 6);
  asm volatile("s_waitcnt lgkmcnt(0)\n\ts_barrier" ::: "memory");
  {
    char* Xout = ws + (LAYER == 0 ? OFF_X1 : OFF_X3);
    const u32x4 ya = *(const u32x4*)(sY + tid * 16), yb = *(const u32x4*)(sY + (512 + tid) * 16);
#pragma unroll
    for (int r = 0; r < 4; ++r) {
      const u32 a = ya[r], bq = yb[r], r0 = raw[r][0], r1 = raw[r][1];
      const float v0 = bflo(r0) + bflo(a);
      const float v1 = special ? bfhi(r0) : bfhi(r0) + bfhi(a);
      const float v2 = bflo(r1) + bflo(bq), v3 = bfhi(r1) + bfhi(bq);
      *(u32x2*)(Xout + ((size_t)oct * 2048 + r * 512 + tid) * 16 + hf * 8) = u32x2{pk2(v0, v1), pk2(v2, v3)};
    }
  }
  STAMP(KID, 7); RSTAMP(KID, 15);
}

DI f32x2 gelu2(f32x2 x) {
  const float k1 = -2.3022081978378774f;
  const float k2 = k1 * 0.044715f;
  const f32x2 t = x * x;
  const f32x2 u = t * k2 + k1;
  const f32x2 z = x * u;
  f32x2 d = f32x2{__builtin_amdgcn_exp2f(z[0]), __builtin_amdgcn_exp2f(z[1])} + 1.f;
  return x * f32x2{__builtin_amdgcn_rcpf(d[0]), __builtin_amdgcn_rcpf(d[1])};
}
template <int IMM> DI void dma16_nom0(i32x4 rsrc, u32 voff, u32 soff) {
  asm volatile(PADS "buffer_load_dwordx4 %0, %1, %2 offen offset:%3 lds" ::"v"(voff), "s"(rsrc), "s"(RFL(soff)), "i"(IMM) : "memory");
}
template <int N> DI void wait_vm_bar(u32x4& a, u32x4& b, u32x4& c, u32x4& d, u32x4& e, u32x4& f, u32x4& g, u32x4& h) { wait_vm_bar8<N>(a, b, c, d, e, f, g, h); }

constexpr int REGPF = 20;
template <int LAYER, int NF, int NQ, class MH, class RH, class EH>
DI void gemm1_body(const Params& P, char* smem, int ft, int tt, int lane, int w, MH&& tail_hook, RH&& reg_hook, EH&& epi_hook) {
  constexpr int KID = LAYER == 0 ? 2 : 5;
  constexpr int NM = NF + NQ, H = (NM + 1) / 2, RING = (LAYER == 0) ? 20 : 16, BR = 12;
  const int c16 = lane & 15, g = lane >> 4;
  char* ws = P.ws;
  const i32x4 rs = mk_rsrc(ws, (u32)WS_END);
  const u32 l16 = lane * 16;
  const int p0 = tt * 128 + w * 32;
  const int f0 = (ft < 12) ? 7 * ft : 84 + 6 * (ft - 12);
  u32x4 bq[BR][2];
  const u32 xoff = (u32)(LAYER == 0 ? OFF_X1 : OFF_X3) + (u32)p0 * 16;
  const u32 bvo = (u32)g * 32768u + (u32)c16 * 16u;
  const u32 asrc = (u32)OFF_A0 + (u32)(ft * KS1 + w) * 7168u, qsrc = (u32)OFF_A1 + (u32)(ft * KS1 + w) * 4096u;
  const u32 adst = lds_addr(smem) + (u32)w * (NM * 1024);
  constexpr int PER = NM + 8;
  auto issue = [&](auto C, auto K) {
    constexpr int c = C, k = K;
    if constexpr (k < NF) {
      constexpr int k4 = k & ~3;
      const u32 so = asrc + (u32)(c * 4 * 7 + k4) * 1024u;
      if constexpr ((k & 3) == 0) dma16<0>(rs, l16, so, adst + (u32)((((c * 4) % RING) * NM + k4) * 1024));
      else dma16_nom0<(k & 3) * 1024>(rs, l16, so);
    } else if constexpr (k < NM) {
      constexpr int kq = k - NF;
      const u32 so = qsrc + (u32)(c * 4 * 4) * 1024u;
      if constexpr (kq == 0) dma16<0>(rs, l16, so, adst + (u32)((((c * 4) % RING) * NM + NF) * 1024));
      else dma16_nom0<kq * 1024>(rs, l16, so);
    } else {
      constexpr int kk = (k - NM) >> 1, t = 4 * c + kk;
      const u32 so = xoff + (u32)t * 131072u;
      if constexpr (((k - NM) & 1) == 0) bld16<0>(bq[t % BR][0], rs, bvo, so);
      else bld16<256>(bq[t % BR][1], rs, bvo, so);
    }
  };
  static_for<0, 2>([&](auto C) { static_for<0, PER>([&](auto K) { issue(C, K); }); });
  u32x2 bw[NF];
  {
    const i32x4 rb1 = mk_rsrc(P.b1, F_ * 2);
#pragma unroll
    for (int m = 0; m < NF; ++m) bld8(bw[m], rb1, (u32)g * 8u, (u32)(f0 + m) * 32u);
  }
  STAMP(KID, 1);
  f32x4 acc[NM][2], gram[2];
#pragma unroll
  for (int m = 0; m < NM; ++m) { acc[m][0] = f32x4{0.f, 0.f, 0.f, 0.f}; acc[m][1] = acc[m][0]; }
  gram[0] = gram[1] = f32x4{0.f, 0.f, 0.f, 0.f};
  u32x4 af[3][H];
  auto waitchunk = [&](auto C, auto N) {
    constexpr int c = C;
    wait_vm_bar<N.value>(bq[(4 * c) % BR][0], bq[(4 * c) % BR][1], bq[(4 * c + 1) % BR][0], bq[(4 * c + 1) % BR][1], bq[(4 * c + 2) % BR][0],
                         bq[(4 * c + 2) % BR][1], bq[(4 * c + 3) % BR][0], bq[(4 * c + 3) % BR][1]);
  };
  auto rd1 = [&](auto HH, auto K) {
    constexpr int h = HH, k = K, t = h >> 1, m0 = (h & 1) ? H : 0, m1 = (h & 1) ? NM : H;
    if constexpr (t < KS1 && m0 + k < m1) af[h % 3][k] = *(const u32x4*)(smem + ((t % RING) * NM + m0 + k) * 1024 + l16);
  };
  waitchunk(IC<0>{}, IC<PER + NF>{});
  STAMP(KID, 2);
  static_for<0, H>([&](auto K) { rd1(IC<0>{}, K); });
  static_for<0, H>([&](auto K) { rd1(IC<1>{}, K); });
  static_for<0, 2 * KS1>([&](auto HH) {
    constexpr int h = HH, t = h >> 1, c = t >> 2, q = t & 3, m0 = (h & 1) ? H : 0, m1 = (h & 1) ? NM : H;
    constexpr int PS = (PER + 2) / 3;
    if constexpr ((h & 1) == 0 && q == 3 && t + 1 < KS1) waitchunk(IC<c + 1>{}, IC<(c + 1 < 4 ? PER : 6)>{});
    constexpr int n1 = (h + 1 < 2 * KS1) ? (((h + 1) & 1) ? NM - H : H) : 0;
    __builtin_amdgcn_s_waitcnt(0xC07F | (n1 << 8));
    SB();
    static_for<m0, m1>([&](auto M) {
      constexpr int m = M;
      acc[m][0] = mfma(af[h % 3][m - m0], bq[t % BR][0], acc[m][0]);
      SB();
      rd1(IC<h + 2>{}, IC<m - m0>{});
      SB();
      acc[m][1] = mfma(af[h % 3][m - m0], bq[t % BR][1], acc[m][1]);
      SB();
      if constexpr (c + 2 < 5 && q < 3 && m < PS && q * PS + m < PER) issue(IC<c + 2>{}, IC<q * PS + m>{});
      if constexpr (h >= 24 && h < 39 && m == m0) tail_hook(IC<h - 24>{}, IC<((LAYER == 0) ? 0 : 4 * NM)>{});
      if constexpr (h >= 30 && m > m0 && m <= m0 + 2 && 2 * (h - 30) + (m - m0 - 1) < REGPF) reg_hook(IC<2 * (h - 30) + (m - m0 - 1)>{});
      SB();
    });
    if constexpr ((h & 1) == 0) {
      u32x4 e0 = bq[t % BR][0], e1 = bq[t % BR][1];
      if constexpr (t == 0) { e0[0] = (g == 0) ? (e0[0] & 0xffffu) : (u32)e0[0]; e1[0] = (g == 0) ? (e1[0] & 0xffffu) : (u32)e1[0]; }
      gram[0] = mfma(e0, e0, gram[0]);
      gram[1] = mfma(e1, e1, gram[1]);
      SB();
    } else if constexpr (NM - H < H) {
      rd1(IC<h + 2>{}, IC<H - 1>{});
    }
  });
  STAMP(KID, 3);
  f32x2 bb[NF][2];
#pragma unroll
  for (int m = 0; m < NF; ++m) {
    asm volatile("" : "+v"(bw[m]));
    bb[m][0] = f32x2{bflo(bw[m][0]), bfhi(bw[m][0])}; bb[m][1] = f32x2{bflo(bw[m][1]), bfhi(bw[m][1])};
  }
  float a_sc[2];
#pragma unroll
  for (int n = 0; n < 2; ++n) {
    const int r = c16 & 3;
    float dv = r == 0 ? gram[n][0] : r == 1 ? gram[n][1] : r == 2 ? gram[n][2] : gram[n][3];
    dv = (g == (c16 >> 2)) ? dv : 0.f;
    const float var = red_g(dv) * (1.f / 640.f);
    const float d1 = ln_den(var);
    a_sc[n] = rcp(d1 * ln_den(var * rcp(d1 * d1)));
  }
  const auto rsb = __builtin_amdgcn_make_buffer_rsrc((void*)ws, 0, (int)WS_END, 0x00020000);
  const u32 vW = ((u32)(lane >> 5) * 16u + (u32)c16) * 16u + (u32)(g & 1) * 8u;
  const u32 hso = (u32)(LAYER == 0 ? OFF_H1 : OFF_H2) + (u32)((tt * 2 + (w >> 1)) * KS2 * 4 + 2 * (w & 1)) * 1024u;
  static_for<0, NF>([&](auto M) {
    constexpr int m = M;
    const int mg = f0 + m;
    const u32 so = hso + (u32)(mg >> 1) * 4096u + (u32)(mg & 1) * 512u;
    epi_hook(M);
#pragma unroll
    for (int n = 0; n < 2; ++n) {
      const f32x4 a = acc[m][n];
      const f32x2 h01 = gelu2(f32x2{a[0], a[1]} * a_sc[n] + bb[m][0]), h23 = gelu2(f32x2{a[2], a[3]} * a_sc[n] + bb[m][1]);
      __builtin_amdgcn_raw_buffer_store_b64(v2u_t{pk2(h01[0], h01[1]), pk2(h23[0], h23[1])}, rsb, vW, so + n * 1024u, HAUX);
    }
  });
  if constexpr (NQ > 0) {
    const int d0 = (ft < 12) ? 2 * ft : 24 + 4 * (ft - 12);
#pragma unroll
    for (int m = 0; m < NQ; ++m)
#pragma unroll
      for (int n = 0; n < 2; ++n) {
        const f32x4 a = acc[NF + m][n];
        __builtin_amdgcn_raw_buffer_store_b64(v2u_t{pk2(a[0], a[1]), pk2(a[2], a[3])}, rsb, (u32)lane * 8u,
                                              (u32)OFF_X3S + (u32)((d0 + m) * 128 + tt * 8 + w * 2 + n) * 512u, 16);
      }
  }
}

typedef __attribute__((address_space(1))) unsigned gu32;

template <int LAYER>
__global__ void __launch_bounds__(256, 1) k_mlp(Params P) {
  extern __shared__ __attribute__((aligned(16))) char smem[];
  const int b = blockIdx.x, tid = threadIdx.x, lane = tid & 63, w = __builtin_amdgcn_readfirstlane(tid >> 6);
  constexpr int KID = LAYER == 0 ? 2 : 5;
  RSTAMP(KID, 14); STAMP(KID, 0);
  char* ws = P.ws;
  const int x = b & 7, li = b >> 3;
  const int ft = (x & 1) * 8 + (li & 7), tt = (x >> 1) * 4 + (li >> 3);
  const int ct = ft >> 1, t64 = 2 * tt + (ft & 1);
  const u32 l16 = lane * 16;
  const u32 aoff = (u32)(LAYER == 0 ? OFF_W2F : OFF_W2T) + (u32)ct * (KS2 * 5 * 1024) + (u32)w * 5120u;
  const auto rs = __builtin_amdgcn_make_buffer_rsrc((void*)ws, 0, (int)WS_END, 0x00020000);
  const bool full = (w < 2);
  u32x4 rg[11][5];
  auto lda1 = [&](auto I) {
    constexpr int i = I;
    if (i < 10 || full) {
#pragma unroll
      for (int m = 0; m < 4; ++m) rg[i][m] = __builtin_amdgcn_raw_buffer_load_b128(rs, l16 + m * 1024, aoff + (u32)(3 + i) * 20480u, 0);
      rg[i][4] = __builtin_amdgcn_raw_buffer_load_b128(rs, l16, aoff + (u32)(3 + i) * 20480u + 4096u, 0);
    } else {
#pragma unroll
      for (int m = 0; m < 5; ++m) rg[i][m] = u32x4{0u, 0u, 0u, 0u};
    }
  };
  auto lda_f = [&](auto K) {
    constexpr int k = K, i = k / 5, m = k % 5;
    if constexpr (m < 4) rg[i][m] = __builtin_amdgcn_raw_buffer_load_b128(rs, l16 + m * 1024, aoff + (u32)(3 + i) * 20480u, 0);
    else rg[i][4] = __builtin_amdgcn_raw_buffer_load_b128(rs, l16, aoff + (u32)(3 + i) * 20480u + 4096u, 0);
  };
  u32 lA;
  {
    const i32x4 rsa = mk_rsrc(ws, (u32)WS_END);
    auto tail = [&](auto I, auto KB) {
      constexpr int i = I, st = i / 5, m = i % 5;
      const u32 lw = lds_addr(smem) + (u32)(KB.value * 1024) + (u32)w * 15360u;
      if constexpr (m == 0) dma16<0>(rsa, l16, aoff + st * 20480u, lw + st * 5120u);
      else if constexpr (m < 4) dma16_nom0<m * 1024>(rsa, l16, aoff + st * 20480u);
      else dma16<0>(rsa, l16, aoff + st * 20480u + 4096u, lw + st * 5120u + 4096u);
    };
        auto regh = [&](auto K) { lda_f(K); };
#define EPIPF 10
    auto epi = [&](auto M) { constexpr int m = M;
      if constexpr (2 * m < EPIPF) lda_f(IC<REGPF + 2 * m>{});
      if constexpr (2 * m + 1 < EPIPF) lda_f(IC<REGPF + 2 * m + 1>{}); };
    if (ft < 12) { gemm1_body<LAYER, 7, LAYER == 0 ? 0 : 2>(P, smem, ft, tt, lane, w, tail, regh, epi); lA = (LAYER == 0) ? 0 : 4 * 9 * 1024; }
    else { gemm1_body<LAYER, 6, LAYER == 0 ? 0 : 4>(P, smem, ft, tt, lane, w, tail, regh, epi); lA = (LAYER == 0) ? 0 : 4 * 10 * 1024; }
    lA += (u32)w * 15360u;
  }
  STAMP(KID, 4);
  gu32* cnt = (gu32*)(ws + OFF_CNT) + (LAYER * 16 + tt) * 32;
  asm volatile("s_waitcnt vmcnt(0)\n\ts_barrier" ::: "memory");
  if (tid == 0) __hip_atomic_fetch_add(cnt, 1u, __ATOMIC_RELAXED, __HIP_MEMORY_SCOPE_AGENT);
  STAMP(KID, 5);
  const u32 boff = (u32)(LAYER == 0 ? OFF_H1 : OFF_H2) + (u32)t64 * (KS2 * 4096) + (u32)w * 4096u;
  u32 bo[4];
#pragma unroll
  for (int n = 0; n < 4; ++n) bo[n] = boff + (u32)((n + w) & 3) * 1024u;
#define G2PD 5
  constexpr int PD = G2PD, NSTEP = 14;
  const u32 vX = (u32)(lane >> 5) * 32768u + (u32)((lane >> 4) & 1) * 8u + (u32)(lane & 15) * 16u;
  const u32 xso = (u32)(10 * ct * 2048 + t64 * 64 + w * 16) * 16u;
  v2u_t rz[5]; f32x4 bz[5];
  auto ldbias = [&]() {
#pragma unroll
    for (int m = 0; m < 5; ++m) {
      if (LAYER == 0) {
        rz[m] = __builtin_amdgcn_raw_buffer_load_b64(rs, vX, (u32)OFF_X1 + xso + m * 65536u, 0);
        bz[m] = __builtin_bit_cast(f32x4, __builtin_amdgcn_raw_buffer_load_b128(rs, (u32)(lane >> 4) * 16u, (u32)OFF_B2F + (u32)(ct * 5 + m) * 64u, 0));
      } else {
        const u32x2 bw = *(const u32x2*)(P.b2 + (ct * 5 + m) * 16 + 4 * (lane >> 4));
        bz[m] = f32x4{bflo(bw[0]), bfhi(bw[0]), bflo(bw[1]), bfhi(bw[1])};
      }
    }
  };
  static_for<REGPF + EPIPF, 50>([&](auto K) { lda_f(K); });
  lda1(IC<10>{});
  ldbias();
  SB();
  if (tid == 0) {
    unsigned spins = 0;
    while (__hip_atomic_load(cnt, __ATOMIC_RELAXED, __HIP_MEMORY_SCOPE_AGENT) < 16u) {
      __builtin_amdgcn_s_sleep(1);
      if (++spins > (1u << 22)) { __hip_atomic_store((gu32*)(ws + OFF_CNT) + 1024, 1u, __ATOMIC_RELAXED, __HIP_MEMORY_SCOPE_AGENT); break; }
    }
  }
  asm volatile("s_barrier" ::: "memory");
  STAMP(KID, 6);
  u32x4 rb[PD][4];
  auto ldb = [&](auto I, auto K) { constexpr int i = I, k = K; rb[i % PD][k] = __builtin_amdgcn_raw_buffer_load_b128(rs, l16, bo[k] + (u32)i * 16384u, 16); };
  static_for<0, PD - 1>([&](auto I) { static_for<0, 4>([&](auto K) { ldb(I, K); }); });
  SB();
  if (LAYER == 1) {
#pragma unroll
    for (int m = 0; m < 5; ++m) rz[m] = __builtin_amdgcn_raw_buffer_load_b64(rs, (u32)lane * 8u, (u32)OFF_X3S + (u32)((ct * 5 + m) * 128 + t64 * 4 + w) * 512u, 16);
  }
  f32x4 acc[5][4];
#pragma unroll
  for (int m = 0; m < 5; ++m)
#pragma unroll
    for (int n = 0; n < 4; ++n) acc[m][n] = f32x4{0.f, 0.f, 0.f, 0.f};
  u32x4 al[2][5];
  const char* lbase = smem + lA + l16;
#pragma unroll
  for (int m = 0; m < 5; ++m) al[0][m] = *(const u32x4*)(lbase + m * 1024);
  SB();
  static_for<0, NSTEP - 1>([&](auto I) {
    constexpr int i = I, nx = i + PD - 1;
    static_for<0, 5>([&](auto M) {
      constexpr int m = M;
#pragma unroll
      for (int n = 0; n < 4; ++n) {
        if constexpr (i < 3) acc[m][n] = mfma(al[i & 1][m], rb[i % PD][n], acc[m][n]);
        else acc[m][n] = mfma(rg[i - 3][m], rb[i % PD][n], acc[m][n]);
      }
      SB();
      if constexpr (i + 1 < 3) al[(i + 1) & 1][m] = *(const u32x4*)(lbase + ((i + 1) * 5 + m) * 1024);
      if constexpr (m < 4) {
        if constexpr (nx < NSTEP - 1) ldb(IC<nx>{}, M);
        else if constexpr (nx == NSTEP - 1) { if (full) ldb(IC<nx>{}, M); }
      }
      SB();
    });
  });
  if (full) {
    constexpr int i = NSTEP - 1;
#pragma unroll
    for (int m = 0; m < 5; ++m)
#pragma unroll
      for (int n = 0; n < 4; ++n) acc[m][n] = mfma(rg[10][m], rb[i % PD][n], acc[m][n]);
  }
  SB();
  STAMP(KID, 7);
  asm volatile("s_barrier" ::: "memory");
  f32x4* red = (f32x4*)smem + lane;
#pragma unroll
  for (int n = 1; n < 4; ++n) {
    f32x4* dst = red + (((n + w) & 3) * 15 + (n - 1) * 5) * 64;
#pragma unroll
    for (int m = 0; m < 5; ++m) dst[m * 64] = acc[m][n];
  }
  asm volatile("s_waitcnt lgkmcnt(0)\n\ts_barrier" ::: "memory");
  float ssq = 0.f;
  const f32x4* src = red + w * 15 * 64;
#pragma unroll
  for (int m = 0; m < 5; ++m) {
    const f32x4 o = (acc[m][0] + src[m * 64]) + (src[(5 + m) * 64] + src[(10 + m) * 64]) +
                    (f32x4{bflo(rz[m][0]), bfhi(rz[m][0]), bflo(rz[m][1]), bfhi(rz[m][1])} + bz[m]);
    const v2u_t ov = v2u_t{pk2(o[0], o[1]), pk2(o[2], o[3])};
    if (LAYER == 0) {
      __builtin_amdgcn_raw_buffer_store_b64(ov, rs, vX, (u32)OFF_X2 + xso + m * 65536u, 0);
      f32x4 q2 = o * o;
      if (m == 0) q2[1] = (ct == 0 && lane < 16) ? 0.f : q2[1];
      ssq += (q2[0] + q2[1]) + (q2[2] + q2[3]);
    } else {
      *(v2u_t*)(P.out + (size_t)s_of_p(t64 * 64 + w * 16 + (lane & 15)) * D_ + (ct * 5 + m) * 16 + 4 * (lane >> 4)) = ov;
    }
  }
  if (LAYER == 0) {
    ssq = red_g(ssq);
    if (lane < 16) atomicAdd((float*)(ws + OFF_SS2) + t64 * 64 + w * 16 + lane, ssq);
  }
  STAMP(KID, 8); RSTAMP(KID, 15);
}

extern "C" void kernel_launch(void* const* d_in, const int* in_sizes, int n_in, void* d_out, int out_size, void* d_ws,
                              size_t ws_size, hipStream_t stream) {
  Params P;
  P.x = (const u16*)d_in[0]; P.wr = (const u16*)d_in[1]; P.wi = (const u16*)d_in[2];
  P.w1 = (const u16*)d_in[4]; P.b1 = (const u16*)d_in[5]; P.w2 = (const u16*)d_in[6]; P.b2 = (const u16*)d_in[7];
  P.out = (u16*)d_out; P.ws = (char*)d_ws;
  {
    hipFuncSetAttribute((const void*)k0_prep, hipFuncAttributeMaxDynamicSharedMemorySize, 163840);
    hipFuncSetAttribute((const void*)k_col<0>, hipFuncAttributeMaxDynamicSharedMemorySize, 163840);
    hipFuncSetAttribute((const void*)k_col<1>, hipFuncAttributeMaxDynamicSharedMemorySize, 163840);
    hipFuncSetAttribute((const void*)k_mlp<0>, hipFuncAttributeMaxDynamicSharedMemorySize, 163840);
    hipFuncSetAttribute((const void*)k_mlp<1>, hipFuncAttributeMaxDynamicSharedMemorySize, 163840);
  }
  k0_prep<<<250, 512, 163840, stream>>>(P);
  k_col<0><<<184, 512, 86016, stream>>>(P);
  k_mlp<0><<<256, 256, 163840, stream>>>(P);
  k_col<1><<<168, 512, 86016, stream>>>(P);
  k_mlp<1><<<256, 256, 163840, stream>>>(P);
}
```

```cpp
#include <hip/hip_runtime.h>
#include <stdint.h>

typedef __bf16 bf16x2 __attribute__((ext_vector_type(2)));
typedef __bf16 bf16x4 __attribute__((ext_vector_type(4)));
typedef __bf16 bf16x8 __attribute__((ext_vector_type(8)));
typedef float f32x2 __attribute__((ext_vector_type(2)));
typedef float f32x4 __attribute__((ext_vector_type(4)));
typedef unsigned u32;
typedef unsigned u32x2 __attribute__((ext_vector_type(2)));
typedef unsigned u32x4 __attribute__((ext_vector_type(4)));
typedef unsigned short u16;
#define LAS __attribute__((address_space(3)))
#define DI __device__ __forceinline__

constexpr int S_ = 2048, D_ = 640, F_ = 1728, DF_ = 321;
constexpr int KS1 = 20;
constexpr int KS2 = 54;
constexpr float EPS = 1e-5f;

constexpr size_t XT_B = (size_t)80 * 2048 * 16;
constexpr size_t H_B = (size_t)128 * KS2 * 1024;
constexpr size_t A0_B = (size_t)16 * KS1 * 7 * 1024;
constexpr size_t A1_B = (size_t)16 * KS1 * 4 * 1024;
constexpr size_t W2F_B = (size_t)8 * KS2 * 5 * 1024;
constexpr size_t WT_B = (size_t)320 * 8192;
constexpr size_t X3S_B = (size_t)40 * 128 * 512;
constexpr size_t OFF_X0 = 0, OFF_X1 = OFF_X0 + XT_B, OFF_X2 = OFF_X1 + XT_B, OFF_X3 = OFF_X2 + XT_B;
constexpr size_t OFF_H1 = OFF_X3 + XT_B, OFF_H2 = OFF_H1 + H_B;
constexpr size_t OFF_A0 = OFF_H2 + H_B, OFF_A1 = OFF_A0 + A0_B;
constexpr size_t OFF_W2F = OFF_A1 + A1_B, OFF_W2T = OFF_W2F + W2F_B;
constexpr size_t OFF_WT = OFF_W2T + W2F_B;
constexpr size_t OFF_X3S = OFF_WT + WT_B;
constexpr size_t OFF_RSTD0 = OFF_X3S + X3S_B;
constexpr size_t OFF_SS2 = OFF_RSTD0 + 5 * 8192;
constexpr size_t OFF_B2F = OFF_SS2 + 8192;
constexpr size_t OFF_TAB = OFF_B2F + 4096;
constexpr size_t OFF_CNT = OFF_TAB + 20480;
constexpr size_t OFF_FLG = OFF_CNT + 8192;
constexpr size_t OFF_EPOCH = OFF_FLG + 256 * 64;
constexpr size_t WS_END = OFF_EPOCH + 128;

DI u32 pk2(float a, float b) { f32x2 v = {a, b}; bf16x2 r = __builtin_convertvector(v, bf16x2); return __builtin_bit_cast(u32, r); }

DI float bflo(u32 w) { return __builtin_bit_cast(float, w << 16); }
DI float bfhi(u32 w) { return __builtin_bit_cast(float, w & 0xffff0000u); }
DI float rbf(float x) { return bflo(pk2(x, x)); }
DI f32x4 mfma(u32x4 a, u32x4 b, f32x4 c) {
  return __builtin_amdgcn_mfma_f32_16x16x32_bf16(__builtin_bit_cast(bf16x8, a), __builtin_bit_cast(bf16x8, b), c, 0, 0, 0);
}
DI float dot2(u32 a, u32 b, float c) { return __builtin_amdgcn_fdot2_f32_bf16(__builtin_bit_cast(bf16x2, a), __builtin_bit_cast(bf16x2, b), c, false); }
DI float cos2pi(float x) { return __builtin_amdgcn_cosf(x); }
DI float sin2pi(float x) { return __builtin_amdgcn_sinf(x); }
DI int s_of_p(int p) {
  const int r = p >> 9, i = (p >> 7) & 3, g = (p >> 5) & 3, uu = (p >> 4) & 1, c16 = p & 15;
  return 64 * (16 * uu + 4 * g + r) + 16 * i + c16;
}
DI void glds16(const void* g, LAS void* l) { __builtin_amdgcn_global_load_lds(g, l, 16, 0, 0); }
#define WAIT_VM0() asm volatile("s_waitcnt vmcnt(0)" ::: "memory")
#define WAIT_LG0() asm volatile("s_waitcnt lgkmcnt(0)" ::: "memory")
#define BAR() __builtin_amdgcn_s_barrier()
DI float ln_den(float var) { return rbf(__builtin_amdgcn_sqrtf(rbf(rbf(var) + EPS))); }
DI float rcp(float x) { return __builtin_amdgcn_rcpf(x); }

typedef int i32x4 __attribute__((ext_vector_type(4)));
DI i32x4 mk_rsrc(const void* p, u32 bytes) {
  const unsigned long long a = (unsigned long long)p;
  return i32x4{(int)__builtin_amdgcn_readfirstlane((u32)a), (int)__builtin_amdgcn_readfirstlane((u32)(a >> 32) & 0xffffu), (int)bytes, 0x00020000};
}
#define RFL(x) __builtin_amdgcn_readfirstlane(x)
#define PADS
template <int IMM> DI void dma16(i32x4 rsrc, u32 voff, u32 soff, u32 lds) {
  asm volatile(PADS "s_mov_b32 m0, %3\n\ts_nop 0\n\tbuffer_load_dwordx4 %0, %1, %2 offen offset:%4 lds" ::"v"(voff), "s"(rsrc), "s"(RFL(soff)), "s"(RFL(lds)), "i"(IMM) : "memory");
}
template <int IMM, bool SC1 = false> DI void bld16(u32x4& dst, i32x4 rsrc, u32 voff, u32 soff) {
  if constexpr (SC1) asm volatile("buffer_load_dwordx4 %0, %1, %2, %3 offen offset:%4 sc1" : "=v"(dst) : "v"(voff), "s"(rsrc), "s"(RFL(soff)), "i"(IMM) : "memory");
  else asm volatile("buffer_load_dwordx4 %0, %1, %2, %3 offen offset:%4" : "=v"(dst) : "v"(voff), "s"(rsrc), "s"(RFL(soff)), "i"(IMM) : "memory");
}
DI void bldu16(u32& dst, i32x4 rsrc, u32 voff, u32 soff) {
  asm volatile(PADS "buffer_load_ushort %0, %1, %2, %3 offen" : "=v"(dst) : "v"(voff), "s"(rsrc), "s"(RFL(soff)) : "memory");
}
template <bool SC1 = false> DI void bld8(u32x2& dst, i32x4 rsrc, u32 voff, u32 soff) {
  if constexpr (SC1) asm volatile("buffer_load_dwordx2 %0, %1, %2, %3 offen sc1" : "=v"(dst) : "v"(voff), "s"(rsrc), "s"(RFL(soff)) : "memory");
  else asm volatile("buffer_load_dwordx2 %0, %1, %2, %3 offen" : "=v"(dst) : "v"(voff), "s"(rsrc), "s"(RFL(soff)) : "memory");
}
template <bool SC1 = false> DI void bld4(u32& dst, i32x4 rsrc, u32 voff, u32 soff) {
  if constexpr (SC1) asm volatile("buffer_load_dword %0, %1, %2, %3 offen sc1" : "=v"(dst) : "v"(voff), "s"(rsrc), "s"(RFL(soff)) : "memory");
  else asm volatile("buffer_load_dword %0, %1, %2, %3 offen" : "=v"(dst) : "v"(voff), "s"(rsrc), "s"(RFL(soff)) : "memory");
}
#define DMA16(rsrc, voff, soff, lds, IMM) dma16<IMM>(rsrc, voff, soff, lds)
#define BLD16(dst, rsrc, voff, soff, IMM) bld16<IMM>(dst, rsrc, voff, soff)
template <int N> DI void wait_vm_bar8(u32x4& a, u32x4& b, u32x4& c, u32x4& d, u32x4& e, u32x4& f, u32x4& g, u32x4& h) {
  asm volatile("s_waitcnt vmcnt(%8)\n\ts_barrier" : "+v"(a), "+v"(b), "+v"(c), "+v"(d), "+v"(e), "+v"(f), "+v"(g), "+v"(h) : "i"(N) : "memory");
}
#define WAIT_VM(N) asm volatile("s_waitcnt vmcnt(" #N ")" ::: "memory")
DI float red_g(float x) {
  const u32 xi = __builtin_bit_cast(u32, x);
  auto a = __builtin_amdgcn_permlane32_swap(xi, xi, false, false);
  const float y = __builtin_bit_cast(float, (u32)a[0]) + __builtin_bit_cast(float, (u32)a[1]);
  const u32 yi = __builtin_bit_cast(u32, y);
  auto c = __builtin_amdgcn_permlane16_swap(yi, yi, false, false);
  return __builtin_bit_cast(float, (u32)c[0]) + __builtin_bit_cast(float, (u32)c[1]);
}
DI u32 lds_addr(const void* p) { return (u32)(uintptr_t)(LAS const char*)p; }

template <int I> struct IC { static constexpr int value = I; constexpr operator int() const { return I; } };
template <int B, int E, class F> DI void static_for(F&& f) { if constexpr (B < E) { f(IC<B>{}); static_for<B + 1, E>(f); } }
#define SB() __builtin_amdgcn_sched_barrier(0)

#define CODE_ADDR(SYM, out) do { u32 lo_, hi_; \
  asm volatile("s_getpc_b64 s[20:21]\n\ts_add_u32 s20, s20, " SYM "@rel32@lo+4\n\ts_addc_u32 s21, s21, " SYM "@rel32@hi+12\n\ts_mov_b32 %0, s20\n\ts_mov_b32 %1, s21" \
               : "=s"(lo_), "=s"(hi_) :: "s20", "s21", "scc"); out = ((unsigned long long)hi_ << 32) | lo_; } while (0)
DI void touch_code(unsigned long long p, u32 bytes, int tid) {
  typedef __attribute__((address_space(1))) const u32x4 gv4;
  u32x4 v[5];
#pragma unroll
  for (int k = 0; k < 5; ++k) { const u32 o = (u32)tid * 16u + (u32)k * 8192u; v[k] = *(gv4*)(p + (o < bytes ? o : 0u)); }
#pragma unroll
  for (int k = 0; k < 5; ++k) asm volatile("" :: "v"(v[k]));
}
#define TLB_WARM 1
constexpr u32 FRONT_CODE_BYTES = 29696, COL_CODE_BYTES = 3584, MLP0_CODE_BYTES = 24576, MLP1_CODE_BYTES = 28672;

typedef __attribute__((address_space(1))) unsigned gu32;
struct Params {
  const u16 *x, *wr, *wi, *w1, *b1, *w2, *b2;
  u16* out;
  char* ws;
};

#define STAMP(kid, i)
#define RSTAMP(kid, i)

struct QGen { float v0[8], v1[8], k; };
DI void gen_q_init(QGen& G, int ms, int lane) {
  const int c = ms * 16 + (lane & 15), g = lane >> 4;
  int jj = c >> 1; float amp = 0.05590169943749474f; float ph = (c & 1) ? 0.25f : 0.f;
  if (c < 2) { jj = (c == 0) ? 320 : 0; amp = 0.03952847075210474f; ph = 0.f; }
  const float fj = (float)jj * (1.f / 640.f);
  const float kj = 2.f * cos2pi(fj), d = (float)(8 * g);
  G.v0[0] = amp * cos2pi(__builtin_amdgcn_fractf(fj * d) + ph);          G.v0[1] = amp * cos2pi(__builtin_amdgcn_fractf(fj * (d + 1.f)) + ph);
  G.v1[0] = amp * cos2pi(__builtin_amdgcn_fractf(fj * (d + 32.f)) + ph); G.v1[1] = amp * cos2pi(__builtin_amdgcn_fractf(fj * (d + 33.f)) + ph);
#pragma unroll
  for (int j = 2; j < 8; ++j) { G.v0[j] = kj * G.v0[j - 1] - G.v0[j - 2]; G.v1[j] = kj * G.v1[j - 1] - G.v1[j - 2]; }
  G.k = 2.f * cos2pi(__builtin_amdgcn_fractf((float)jj * 0.05f));
}
DI u32x4 gen_q_step(QGen& G) {
  const u32x4 r = u32x4{pk2(G.v0[0], G.v0[1]), pk2(G.v0[2], G.v0[3]), pk2(G.v0[4], G.v0[5]), pk2(G.v0[6], G.v0[7])};
#pragma unroll
  for (int j = 0; j < 8; ++j) { const float n = G.k * G.v1[j] - G.v0[j]; G.v0[j] = G.v1[j]; G.v1[j] = n; }
  return r;
}

struct JobOps { i32x4 rs; u32 vo[10]; u32 l0; };
DI void job_ops(JobOps& J, const Params& P, int job, int w8, int lane, u32 lbuf) {
  asm volatile("" : "+v"(lane), "+s"(job), "+s"(w8));
  if (job >= 32 && job < 59) {
    const int d = 80 * w8 + (lane >> 3), pos = lane & 7;
    J.rs = mk_rsrc(P.w1, D_ * F_ * 2);
    const u32 va = (u32)d * (F_ * 2) + (u32)(job - 32) * 128u + 16u * (u32)(pos ^ (((d >> 1) & 1) << 1));
#pragma unroll
    for (int k = 0; k < 10; ++k) J.vo[k] = ((k & 1) ? (va ^ 64u) : va) + (u32)k * (8 * F_ * 2);
    J.l0 = lbuf + (u32)w8 * 10240u;
  } else {
    const int nn = w8 >> 1, hh = w8 & 1;
    u32 s0;
    if (job < 32) { J.rs = mk_rsrc(P.x, S_ * D_ * 2); s0 = (u32)s_of_p(job * 64 + nn * 16) * (D_ * 2); }
    else if (job < 86) { J.rs = mk_rsrc(P.w2, F_ * D_ * 2); s0 = (u32)((job - 59) * 64 + nn * 16) * (D_ * 2); }
    else { J.rs = mk_rsrc(P.b2, D_ * 2); s0 = 0; }
    J.l0 = lbuf + (u32)nn * 20480u + (u32)hh * 10240u;
    const u32 va = (u32)lane * 16u;
    if (hh == 0) {
#pragma unroll
      for (int k = 0; k < 10; ++k) J.vo[k] = (va ^ (u32)(k * 16)) + (s0 + k * 1280u);
    } else {
      const u32 vb = (u32)(lane >> 4) * 1280u + 1024u + (((u32)(lane & 15) ^ (u32)(lane >> 4)) * 16u);
#pragma unroll
      for (int k = 0; k < 6; ++k) J.vo[k] = (va ^ (u32)((10 + k) * 16)) + (s0 + (10 + k) * 1280u);
#pragma unroll
      for (int k = 6; k < 10; ++k) J.vo[k] = (vb ^ (u32)((k - 6) * 64)) + (s0 + (k - 6) * 5120u);
    }
  }
}
template <int K> DI void job_op(const JobOps& J) {
  if constexpr (K == 0) asm volatile(PADS "s_mov_b32 m0, %2\n\ts_nop 0\n\tbuffer_load_dwordx4 %0, %1, 0 offen lds\n\ts_add_u32 m0, m0, 0x400" ::"v"(J.vo[0]), "s"(J.rs), "s"(RFL(J.l0)) : "memory", "scc");
  else asm volatile(PADS "buffer_load_dwordx4 %0, %1, 0 offen lds\n\ts_add_u32 m0, m0, 0x400" ::"v"(J.vo[K]), "s"(J.rs) : "memory", "scc");
}

DI u32x4 ident_frag(int par, int lane) {
  const int j = 16 * par + (lane & 15) - 8 * (lane >> 4);
  u32x4 r;
#pragma unroll
  for (int k = 0; k < 4; ++k) r[k] = (j == 2 * k ? 0x3F80u : 0u) | (j == 2 * k + 1 ? 0x3F800000u : 0u);
  return r;
}

template <int MODE, class HK>
DI void qloop_lin(const char* buf, int sl, int lane, u32x4 (&q)[2][KS1], f32x4 (&acc)[2][2], f32x4& sum, f32x4& gram, HK&& hook) {
  u32x4 bf[2][2];
  const int row = lane & 15, g = lane >> 4;
  const char* ba[4];
#pragma unroll
  for (int j = 0; j < 4; ++j) ba[j] = buf + row * 1024 + (((4 * j + g) ^ row) * 16);
  const int dB = 16384 - row * 768;
  auto rd = [&](auto T, auto N) { constexpr int t = T, n = N;
    if constexpr (t < 16) bf[t & 1][n] = *(const u32x4*)(ba[t & 3] + n * 20480 + (t >> 2) * 256);
    else if constexpr (t < KS1) bf[t & 1][n] = *(const u32x4*)(ba[t & 3] + dB + n * 20480); };
  rd(IC<0>{}, IC<0>{}); rd(IC<0>{}, IC<1>{});
  static_for<0, KS1>([&](auto T) {
    constexpr int t = T;
    rd(IC<t + 1>{}, IC<0>{}); rd(IC<t + 1>{}, IC<1>{});
    __builtin_amdgcn_s_waitcnt(0xC07F | ((t + 1 < KS1 ? 2 : 0) << 8));
    SB();
    static_for<0, 2>([&](auto N) {
      constexpr int n = N;
      if constexpr (MODE == 2) acc[0][n] = mfma(bf[t & 1][n], q[0][t], acc[0][n]); else acc[0][n] = mfma(q[0][t], bf[t & 1][n], acc[0][n]);
      if constexpr (MODE == 2) acc[1][n] = mfma(bf[t & 1][n], q[1][t], acc[1][n]); else acc[1][n] = mfma(q[1][t], bf[t & 1][n], acc[1][n]);
      SB();
      hook(T, N);
      SB();
    });
    if constexpr (MODE == 1 || MODE == 3) {
      constexpr int sn = (MODE == 3) ? 1 : 0;
#pragma unroll
      for (int k = 0; k < 4; ++k) { const u32 e = bf[t & 1][sn][k]; sum[0] = dot2(e, 0x3F803F80u, sum[0]); }
      gram = mfma(bf[t & 1][sn], bf[t & 1][sn], gram);
      SB();
    }
  });
}
template <class HK>
DI void qloop_tr(const char* buf, int nh, int lane, u32x4 (&q)[2][KS1], f32x4 (&acc)[2][2], HK&& hook) {
  const int g = lane >> 4, pp = lane & 3, qq = (lane & 15) >> 2;
  const int sw = (((qq >> 1) & 1) | ((g & 1) << 1)) << 1;
  const char* base[2];
#pragma unroll
  for (int n = 0; n < 2; ++n) base[n] = buf + (8 * g + qq) * 128 + ((2 * (2 * nh + n) + (pp >> 1)) ^ sw) * 16 + (pp & 1) * 8;
  u32x2 lo[2][2], hi[2][2];
  auto rd = [&](auto T, auto N) { constexpr int t = T, n = N;
    if constexpr (t < KS1) {
      lo[t & 1][n] = __builtin_bit_cast(u32x2, __builtin_amdgcn_ds_read_tr16_b64_v4bf16((LAS bf16x4*)(base[n] + t * 4096)));
      hi[t & 1][n] = __builtin_bit_cast(u32x2, __builtin_amdgcn_ds_read_tr16_b64_v4bf16((LAS bf16x4*)(base[n] + t * 4096 + 512)));
    } };
  rd(IC<0>{}, IC<0>{}); rd(IC<0>{}, IC<1>{});
  static_for<0, KS1>([&](auto T) {
    constexpr int t = T;
    rd(IC<t + 1>{}, IC<0>{}); rd(IC<t + 1>{}, IC<1>{});
    __builtin_amdgcn_s_waitcnt(0xC07F | ((t + 1 < KS1 ? 4 : 0) << 8));
    SB();
    static_for<0, 2>([&](auto N) {
      constexpr int n = N;
      const u32x4 b = u32x4{lo[t & 1][n][0], lo[t & 1][n][1], hi[t & 1][n][0], hi[t & 1][n][1]};
      acc[0][n] = mfma(q[0][t], b, acc[0][n]);
      acc[1][n] = mfma(q[1][t], b, acc[1][n]);
      SB();
      hook(T, N);
      SB();
    });
  });
}

DI u32x4 row_frag(const char* buf, int n, int t, int lane) {
  const int row = lane & 15, g = lane >> 4, x = (((4 * (t & 3) + g) ^ row) * 16);
  return *(const u32x4*)(buf + n * 20480 + (t < 16 ? row * 1024 + (t >> 2) * 256 : 16384 + row * 256) + x);
}
typedef __attribute__((ext_vector_type(2))) unsigned int v2u_t;
#define HAUX 16
template <int AUX = 0> DI void bst8(__amdgpu_buffer_rsrc_t rs, u32 voff, u32 soff, const f32x4& a) {
  __builtin_amdgcn_raw_buffer_store_b64(v2u_t{pk2(a[0], a[1]), pk2(a[2], a[3])}, rs, voff, soff, AUX);
}
template <int KIND, class HK>
DI void qjob_compute(const Params& P, int job, int ct5, int mp, int nh, int lane, const char* buf, u32x4 (&q)[2][KS1], HK&& hook) {
  asm volatile("" : "+s"(job), "+s"(ct5), "+s"(mp), "+s"(nh), "+v"(lane));
  const int ms0 = ct5 * 8 + 2 * mp, tc = 4 * ct5 + mp;
  f32x4 acc[2][2], sum = {0.f, 0.f, 0.f, 0.f}, gram = sum;
  acc[0][0] = sum; acc[0][1] = sum; acc[1][0] = sum; acc[1][1] = sum;
  const char* bl = buf + nh * 40960;
  const __amdgpu_buffer_rsrc_t rsw = __builtin_amdgcn_make_buffer_rsrc((void*)P.ws, 0, (int)WS_END, 0x00020000);
  auto vXf = [&]() { asm volatile("" : "+v"(lane)); return (u32)(lane >> 5) * 32768u + (u32)((lane >> 4) & 1) * 8u + (u32)(lane & 15) * 16u; };
  auto vWf = [&]() { asm volatile("" : "+v"(lane)); return ((u32)(lane >> 5) * 16u + (u32)(lane & 15)) * 16u + (u32)((lane >> 4) & 1) * 8u; };
  if constexpr (KIND == 1) {
    const bool st = (mp >> 1) == nh;
    if (!st) qloop_lin<0>(bl, 0, lane, q, acc, sum, gram, hook);
    else {
      if ((mp & 1) == 0) qloop_lin<1>(bl, 0, lane, q, acc, sum, gram, hook); else qloop_lin<3>(bl, 0, lane, q, acc, sum, gram, hook);
      const int c16 = lane & 15, g = lane >> 4, r = c16 & 3;
      const float s1 = red_g(sum[0]);
      const float s2 = r == 0 ? gram[0] : r == 1 ? gram[1] : r == 2 ? gram[2] : gram[3];
      const float m = s1 * (1.f / 640.f);
      float var = s2 * (1.f / 640.f) - m * m; var = var < 0.f ? 0.f : var;
      if (g == (c16 >> 2))
        __builtin_amdgcn_raw_buffer_store_b32(__builtin_bit_cast(u32, rcp(ln_den(var))), rsw, (u32)c16 * 4u, (u32)OFF_RSTD0 + (u32)(ct5 * 2048 + job * 64 + (2 * nh + (mp & 1)) * 16) * 4u, 16);
    }
    const u32 vX = vXf();
#pragma unroll
    for (int a = 0; a < 2; ++a) {
      const u32 so = (u32)OFF_X0 + (u32)(2 * (ms0 + a) * 2048 + job * 64 + nh * 32) * 16u;
      bst8<16>(rsw, vX, so, acc[a][0]); bst8<16>(rsw, vX, so + 256u, acc[a][1]);
    }
  } else if (KIND == 2 || job < 59) {
    qloop_tr(buf, nh, lane, q, acc, hook);
    const u32 vW = vWf();
#pragma unroll
    for (int n = 0; n < 2; ++n) {
      const int fs = (job - 32) * 4 + 2 * nh + n;
      if (ms0 == 0 && lane < 16) acc[0][n][1] = 0.f;
      const int ft = fs < 84 ? fs / 7 : 12 + (fs - 84) / 6, m = fs < 84 ? fs - 7 * ft : (fs - 84) - 6 * (ft - 12);
      const u32 so = (u32)OFF_A0 + (u32)((ft * KS1 + tc) * 7 + m) * 1024u;
      bst8(rsw, vW, so, acc[0][n]); bst8(rsw, vW, so + 512u, acc[1][n]);
    }
  } else if (job < 86) {
    qloop_lin<2>(bl, 0, lane, q, acc, sum, gram, hook);
    const u32 vW = vWf();
#pragma unroll
    for (int a = 0; a < 2; ++a) {
      const int ms = ms0 + a, ct = ms / 5;
      const u32x4 id = ident_frag(a, lane);
#pragma unroll
      for (int n = 0; n < 2; ++n) {
        const f32x4 tr = mfma(row_frag(bl, n, tc, lane), id, f32x4{0.f, 0.f, 0.f, 0.f});
        const int fs = (job - 59) * 4 + 2 * nh + n;
        const u32 o = (u32)((ct * KS2 + (fs >> 1)) * 5 + (ms - 5 * ct)) * 1024u + (u32)(fs & 1) * 512u;
        bst8(rsw, vW, (u32)OFF_W2F + o, acc[a][n]);
        bst8(rsw, vW, (u32)OFF_W2T + o, tr);
      }
    }
  } else if (job == 86) {
    if (nh == 0) {
#pragma unroll
      for (int t = 0; t < KS1; ++t) { const u32x4 b = row_frag(buf, 0, t, lane); acc[0][0] = mfma(b, q[0][t], acc[0][0]); acc[1][0] = mfma(b, q[1][t], acc[1][0]); }
      if (lane < 16) { ((float*)(P.ws + OFF_B2F))[ms0 * 16 + lane] = acc[0][0][0]; ((float*)(P.ws + OFF_B2F))[ms0 * 16 + 16 + lane] = acc[1][0][0]; }
    }
  } else {
    const u32x4 id = ident_frag(nh, lane);
    const u32 vW = vWf();
    static_for<0, KS1>([&](auto T) {
      constexpr int t = T;
      const u32 fm = t < 12 ? (u32)(t * KS1 * 4) + (u32)nh : (u32)((12 + (t - 12) / 2) * KS1 * 4 + ((t - 12) & 1) * 2) + (u32)nh;
      const u32 so = (u32)OFF_A1 + (fm + (u32)tc * 4u) * 1024u;
      bst8(rsw, vW, so, mfma(q[0][t], id, f32x4{0.f, 0.f, 0.f, 0.f}));
      bst8(rsw, vW, so + 512u, mfma(q[1][t], id, f32x4{0.f, 0.f, 0.f, 0.f}));
    });
  }
}

DI u32x4 tab_frag(int fr, int lane) {
  const int c16 = lane & 15, g = lane >> 4;
  float v[8];
  if (fr < 16) {
    const int tile = fr >> 2, t = (fr >> 1) & 1, part = fr & 1;
#pragma unroll
    for (int j = 0; j < 8; ++j) {
      const int kk = 32 * t + 16 * (j >> 2) + 4 * g + (j & 3);
      const float a = (float)(((16 * tile + c16) * kk) & 63) * (1.f / 64.f);
      v[j] = part ? -sin2pi(a) : cos2pi(a);
    }
  } else {
    const int u = (fr - 16) >> 1, part = fr & 1;
#pragma unroll
    for (int j = 0; j < 8; ++j) {
      const int kk = 16 * (j >> 2) + 4 * g + (j & 3);
      const float a = (float)(((16 * u + c16) * kk) & 31) * (1.f / 32.f);
      v[j] = part ? -sin2pi(a) : cos2pi(a);
    }
  }
  return u32x4{pk2(v[0], v[1]), pk2(v[2], v[3]), pk2(v[4], v[5]), pk2(v[6], v[7])};
}

template <int LAYER, bool FRONT, class WH>
DI void col_body(const Params& P, char* smem, int b, int tid, int lane, int w, WH&& wait_hook) {
  char* ws = P.ws;
  constexpr int KID = LAYER == 0 ? 1 : 4;
  const int oct = (b & 7) + 8 * (b >> 4), hf = (b >> 3) & 1;
  char* sA = smem;
  char* sT = smem + (FRONT ? 98304 : 16384);
  char* sE = smem + 36864;
  char* sG = smem + 53248;
  char* sY = smem + 69632;
  const int cc = w >> 2, u = (w >> 1) & 1, v = w & 1, c16 = lane & 15, g = lane >> 4;
  const int col = oct * 4 + hf * 2 + cc;
  const i32x4 rsw = mk_rsrc(ws, (u32)WS_END);
  const u32 l16 = lane * 16;
  u32x2 raw[4]; u32 st4[4]; u32x4 wv[2];
  auto issue_loads = [&]() {
    const u32 xo = (u32)(LAYER == 0 ? OFF_X0 : OFF_X2) + (u32)oct * 32768u, xv = (u32)tid * 16u + (u32)hf * 8u;
#pragma unroll
    for (int r = 0; r < 4; ++r) bld8<FRONT>(raw[r], rsw, xv, xo + r * 8192u);
#pragma unroll
    for (int r = 0; r < 4; ++r) bld4<FRONT>(st4[r], rsw, (u32)tid * 4u, (LAYER == 0 ? (u32)OFF_RSTD0 + (u32)(oct >> 4) * 8192u : (u32)OFF_SS2) + r * 2048u);
    if constexpr (!FRONT) {
      dma16<0>(rsw, l16, (u32)OFF_TAB + w * 1024u, lds_addr(sT) + w * 1024u);
      dma16<0>(rsw, l16, (u32)OFF_TAB + (8 + w) * 1024u, lds_addr(sT) + (8 + w) * 1024u);
      dma16<0>(rsw, l16, (u32)OFF_TAB + (16 + (w & 3)) * 1024u, lds_addr(sT) + (16 + (w & 3)) * 1024u);
    }
    const u32 wo = (u32)OFF_WT + (u32)((col * 4 + u * 2 + v) * 2) * 1024u;
    bld16<0, FRONT>(wv[0], rsw, l16, wo);
    bld16<1024, FRONT>(wv[1], rsw, l16, wo);
  };
  if constexpr (!FRONT) issue_loads();
  SB();
  float twc[16], tws[16], itc[8], its[8];
  {
    const float fk = (float)(16 * u + c16) * (1.f / 2048.f);
    const float c0 = cos2pi((float)(4 * g) * fk), s0 = sin2pi((float)(4 * g) * fk);
    const float cr = cos2pi(fk), sr = sin2pi(fk), ci = cos2pi(16.f * fk), si = sin2pi(16.f * fk);
    twc[0] = c0; tws[0] = s0;
#pragma unroll
    for (int r = 1; r < 4; ++r) { twc[r] = twc[r - 1] * cr - tws[r - 1] * sr; tws[r] = tws[r - 1] * cr + twc[r - 1] * sr; }
#pragma unroll
    for (int i = 1; i < 4; ++i)
#pragma unroll
      for (int r = 0; r < 4; ++r) {
        twc[i * 4 + r] = twc[(i - 1) * 4 + r] * ci - tws[(i - 1) * 4 + r] * si;
        tws[i * 4 + r] = tws[(i - 1) * 4 + r] * ci + twc[(i - 1) * 4 + r] * si;
      }
#pragma unroll
    for (int ii = 0; ii < 2; ++ii) {
      const float fm = (float)(16 * (2 * v + ii) + c16) * (1.f / 2048.f);
      const float a0 = (float)(16 * u + 4 * g) * fm;
      const float cm = cos2pi(fm), sm = sin2pi(fm);
      itc[ii * 4] = cos2pi(a0) * (1.f / 2048.f); its[ii * 4] = sin2pi(a0) * (1.f / 2048.f);
#pragma unroll
      for (int r = 1; r < 4; ++r) {
        itc[ii * 4 + r] = itc[ii * 4 + r - 1] * cm - its[ii * 4 + r - 1] * sm;
        its[ii * 4 + r] = its[ii * 4 + r - 1] * cm + itc[ii * 4 + r - 1] * sm;
      }
    }
#pragma unroll
    for (int k = 0; k < 16; ++k) asm volatile("" : "+v"(twc[k]), "+v"(tws[k]));
#pragma unroll
    for (int k = 0; k < 8; ++k) asm volatile("" : "+v"(itc[k]), "+v"(its[k]));
  }
  u32x4 f64r[2][2], f64i[2][2], f64n[2][2], f32r, f32i, f32n;
  if constexpr (FRONT) { wait_hook(); issue_loads(); }
  STAMP(KID, 1);
  if constexpr (FRONT) asm volatile("s_waitcnt vmcnt(2)" : "+v"(raw[0]), "+v"(raw[1]), "+v"(raw[2]), "+v"(raw[3]), "+v"(st4[0]), "+v"(st4[1]), "+v"(st4[2]), "+v"(st4[3]) :: "memory");
  else asm volatile("s_waitcnt vmcnt(5)" : "+v"(raw[0]), "+v"(raw[1]), "+v"(raw[2]), "+v"(raw[3]), "+v"(st4[0]), "+v"(st4[1]), "+v"(st4[2]), "+v"(st4[3]) :: "memory");
  const bool special = (oct == 0 && hf == 0);
  {
    float val[4][4];
#pragma unroll
    for (int r = 0; r < 4; ++r) {
      const float sv = __builtin_bit_cast(float, st4[r]);
      const float rs = (LAYER == 0) ? sv : __builtin_amdgcn_rcpf(ln_den(sv * (1.f / 640.f)));
      val[r][0] = bflo(raw[r][0]) * rs; val[r][1] = special ? 0.f : bfhi(raw[r][0]) * rs;
      val[r][2] = bflo(raw[r][1]) * rs; val[r][3] = bfhi(raw[r][1]) * rs;
    }
    const int ti = tid >> 7, tg = (tid >> 5) & 3, tu = (tid >> 4) & 1, tc = tid & 15;
#pragma unroll
    for (int k = 0; k < 4; ++k)
      *(u32x2*)(sA + (k * 4 + ti) * 1024 + (tg * 16 + tc) * 16 + tu * 8) = u32x2{pk2(val[0][k], val[1][k]), pk2(val[2][k], val[3][k])};
  }
  STAMP(KID, 2);
  asm volatile("s_waitcnt vmcnt(2) lgkmcnt(0)\n\ts_barrier" ::: "memory");
  STAMP(KID, 3);
  f32r = *(const u32x4*)(sT + (16 + u * 2 + 0) * 1024 + l16);
  f32i = *(const u32x4*)(sT + (16 + u * 2 + 1) * 1024 + l16);
  f32n = f32i ^ 0x80008000u;
  const f32x4 Z4 = {0.f, 0.f, 0.f, 0.f};
  u32x4 yr[2], yi[2];
  {
    u32x4 xr[4], xi[4];
#pragma unroll
    for (int i = 0; i < 4; ++i) {
      xr[i] = *(const u32x4*)(sA + ((cc * 2 + 0) * 4 + i) * 1024 + l16);
      xi[i] = *(const u32x4*)(sA + ((cc * 2 + 1) * 4 + i) * 1024 + l16);
    }
#pragma unroll
    for (int ii = 0; ii < 2; ++ii)
#pragma unroll
      for (int t = 0; t < 2; ++t) {
        f64r[ii][t] = *(const u32x4*)(sT + (((2 * v + ii) * 2 + t) * 2 + 0) * 1024 + l16);
        f64i[ii][t] = *(const u32x4*)(sT + (((2 * v + ii) * 2 + t) * 2 + 1) * 1024 + l16);
        f64n[ii][t] = f64i[ii][t] ^ 0x80008000u;
      }
    f32x4 dr[4], di[4];
#pragma unroll
    for (int i = 0; i < 4; ++i) { dr[i] = mfma(xr[i], f32r, Z4); di[i] = mfma(xr[i], f32i, Z4); }
#pragma unroll
    for (int i = 0; i < 4; ++i) { dr[i] = mfma(xi[i], f32n, dr[i]); di[i] = mfma(xi[i], f32r, di[i]); }
    float tr[16], tq[16];
#pragma unroll
    for (int i = 0; i < 4; ++i)
#pragma unroll
      for (int r = 0; r < 4; ++r) {
        tr[i * 4 + r] = dr[i][r] * twc[i * 4 + r] + di[i][r] * tws[i * 4 + r];
        tq[i * 4 + r] = di[i][r] * twc[i * 4 + r] - dr[i][r] * tws[i * 4 + r];
      }
#pragma unroll
    for (int t = 0; t < 2; ++t) {
      yr[t] = u32x4{pk2(tr[8 * t + 0], tr[8 * t + 1]), pk2(tr[8 * t + 2], tr[8 * t + 3]), pk2(tr[8 * t + 4], tr[8 * t + 5]), pk2(tr[8 * t + 6], tr[8 * t + 7])};
      yi[t] = u32x4{pk2(tq[8 * t + 0], tq[8 * t + 1]), pk2(tq[8 * t + 2], tq[8 * t + 3]), pk2(tq[8 * t + 4], tq[8 * t + 5]), pk2(tq[8 * t + 6], tq[8 * t + 7])};
    }
  }
  u32x4 zr[2], zi[2];
  {
    f32x4 dr[2] = {Z4, Z4}, di[2] = {Z4, Z4};
#pragma unroll
    for (int t = 0; t < 2; ++t) {
#pragma unroll
      for (int ii = 0; ii < 2; ++ii) { dr[ii] = mfma(f64r[ii][t], yr[t], dr[ii]); di[ii] = mfma(f64i[ii][t], yr[t], di[ii]); }
#pragma unroll
      for (int ii = 0; ii < 2; ++ii) { dr[ii] = mfma(f64n[ii][t], yi[t], dr[ii]); di[ii] = mfma(f64r[ii][t], yi[t], di[ii]); }
    }
    asm volatile("s_waitcnt vmcnt(0)" : "+v"(wv[0]), "+v"(wv[1]) :: "memory");
    float pr[8], pi[8];
#pragma unroll
    for (int ii = 0; ii < 2; ++ii)
#pragma unroll
      for (int r = 0; r < 4; ++r) {
        const u32 wd = wv[ii][r];
        const float a = bflo(wd), bb = bfhi(wd);
        pr[ii * 4 + r] = dr[ii][r] * a - di[ii][r] * bb;
        pi[ii * 4 + r] = dr[ii][r] * bb + di[ii][r] * a;
      }
    const u32x4 mr = u32x4{pk2(pr[0], pr[1]), pk2(pr[2], pr[3]), pk2(pr[4], pr[5]), pk2(pr[6], pr[7])};
    const u32x4 mi = u32x4{pk2(pi[0], pi[1]), pk2(pi[2], pi[3]), pk2(pi[4], pi[5]), pk2(pi[6], pi[7])};
    *(u32x4*)(sE + ((((cc * 2 + u) * 2 + v) * 2 + 0) * 1024) + l16) = mr;
    *(u32x4*)(sE + ((((cc * 2 + u) * 2 + v) * 2 + 1) * 1024) + l16) = mi;
    STAMP(KID, 4);
    asm volatile("s_waitcnt lgkmcnt(0)\n\ts_barrier" ::: "memory");
    const u32x4 or_ = *(const u32x4*)(sE + ((((cc * 2 + u) * 2 + (1 - v)) * 2 + 0) * 1024) + l16);
    const u32x4 oi_ = *(const u32x4*)(sE + ((((cc * 2 + u) * 2 + (1 - v)) * 2 + 1) * 1024) + l16);
    zr[0] = v ? or_ : mr; zi[0] = v ? oi_ : mi;
    zr[1] = v ? mr : or_; zi[1] = v ? mi : oi_;
  }
  {
    f32x4 dr[2] = {Z4, Z4}, di[2] = {Z4, Z4};
#pragma unroll
    for (int t = 0; t < 2; ++t) {
#pragma unroll
      for (int ii = 0; ii < 2; ++ii) { dr[ii] = mfma(zr[t], f64r[ii][t], dr[ii]); di[ii] = mfma(zr[t], f64n[ii][t], di[ii]); }
#pragma unroll
      for (int ii = 0; ii < 2; ++ii) { dr[ii] = mfma(zi[t], f64i[ii][t], dr[ii]); di[ii] = mfma(zi[t], f64r[ii][t], di[ii]); }
    }
#pragma unroll
    for (int ii = 0; ii < 2; ++ii) {
      float tr[4], tq[4];
#pragma unroll
      for (int r = 0; r < 4; ++r) {
        tr[r] = dr[ii][r] * itc[ii * 4 + r] - di[ii][r] * its[ii * 4 + r];
        tq[r] = dr[ii][r] * its[ii * 4 + r] + di[ii][r] * itc[ii * 4 + r];
      }
      *(u32x2*)(sG + ((((cc * 2 + v) * 2 + ii) * 2 + 0) * 1024) + l16 + u * 8) = u32x2{pk2(tr[0], tr[1]), pk2(tr[2], tr[3])};
      *(u32x2*)(sG + ((((cc * 2 + v) * 2 + ii) * 2 + 1) * 1024) + l16 + u * 8) = u32x2{pk2(tq[0], tq[1]), pk2(tq[2], tq[3])};
    }
  }
  STAMP(KID, 5);
  asm volatile("s_waitcnt lgkmcnt(0)\n\ts_barrier" ::: "memory");
  {
    u32x4 tr[2], tq[2];
#pragma unroll
    for (int ii = 0; ii < 2; ++ii) {
      tr[ii] = *(const u32x4*)(sG + ((((cc * 2 + v) * 2 + ii) * 2 + 0) * 1024) + l16);
      tq[ii] = *(const u32x4*)(sG + ((((cc * 2 + v) * 2 + ii) * 2 + 1) * 1024) + l16);
    }
    f32x4 orr[2], oi[2];
#pragma unroll
    for (int ii = 0; ii < 2; ++ii) { orr[ii] = mfma(f32r, tr[ii], Z4); oi[ii] = mfma(f32r, tq[ii], Z4); }
#pragma unroll
    for (int ii = 0; ii < 2; ++ii) { orr[ii] = mfma(f32i, tq[ii], orr[ii]); oi[ii] = mfma(f32n, tr[ii], oi[ii]); }
#pragma unroll
    for (int ii = 0; ii < 2; ++ii) {
      const int tt = (2 * v + ii) * 128 + g * 32 + u * 16 + c16;
      *(u32x4*)(sY + (cc * 512 + tt) * 16) = u32x4{pk2(orr[ii][0], oi[ii][0]), pk2(orr[ii][1], oi[ii][1]), pk2(orr[ii][2], oi[ii][2]), pk2(orr[ii][3], oi[ii][3])};
    }
  }
  STAMP(KID, 6);
  asm volatile("s_waitcnt lgkmcnt(0)\n\ts_barrier" ::: "memory");
  {
    char* Xout = ws + (LAYER == 0 ? OFF_X1 : OFF_X3);
    const u32x4 ya = *(const u32x4*)(sY + tid * 16), yb = *(const u32x4*)(sY + (512 + tid) * 16);
#pragma unroll
    for (int r = 0; r < 4; ++r) {
      const u32 a = ya[r], bq = yb[r], r0 = raw[r][0], r1 = raw[r][1];
      const float v0 = bflo(r0) + bflo(a);
      const float v1 = special ? bfhi(r0) : bfhi(r0) + bfhi(a);
      const float v2 = bflo(r1) + bflo(bq), v3 = bfhi(r1) + bfhi(bq);
      *(u32x2*)(Xout + ((size_t)oct * 2048 + r * 512 + tid) * 16 + hf * 8) = u32x2{pk2(v0, v1), pk2(v2, v3)};
    }
  }
  STAMP(KID, 7); RSTAMP(KID, 15);
}

__global__ void __launch_bounds__(512, 2) k_col1(Params P) {
  extern __shared__ __attribute__((aligned(16))) char smem[];
  const int b = blockIdx.x, tid = threadIdx.x, lane = tid & 63, w = __builtin_amdgcn_readfirstlane(tid >> 6);
  if (b >= 160) {
    unsigned long long pc;
    CODE_ADDR("_Z5k_mlpILi1EEv6Params", pc); touch_code(pc, MLP1_CODE_BYTES, tid);
    return;
  }
  RSTAMP(4, 14); STAMP(4, 0);
  col_body<1, false>(P, smem, b, tid, lane, w, []() {});
}

DI void wt_loads(const Params& P, int WT, int w8, int lane, u32x2 (&wv)[2][2], u32& w0r, u32& w0i) {
  const int wa = WT & 15, wcg = WT >> 4, mp = w8 & 3, nh = w8 >> 2;
  const i32x4 rwr = mk_rsrc(P.wr, S_ * DF_ * 2), rwi = mk_rsrc(P.wi, S_ * DF_ * 2);
  const u32 wvo = (u32)(lane >> 3) * (DF_ * 2) + (u32)(lane & 7) * 8u + (u32)wcg * 64u;
  const u32 wso = (u32)((16 * nh + 32 * (4 * wa + mp)) * DF_ * 2);
  bld8(wv[0][0], rwr, wvo, wso); bld8(wv[0][1], rwi, wvo, wso);
  bld8(wv[1][0], rwr, wvo, wso + 8 * DF_ * 2); bld8(wv[1][1], rwi, wvo, wso + 8 * DF_ * 2);
  bldu16(w0r, rwr, (u32)(lane & 15) * (DF_ * 2) + 640u, wso); bldu16(w0i, rwi, (u32)(lane & 15) * (DF_ * 2) + 640u, wso);
}
DI void wt_stage(u32* st, int w8, int lane, const u32x2 (&wv)[2][2]) {
  const int mp = w8 & 3, nh = w8 >> 2;
#pragma unroll
  for (int i = 0; i < 2; ++i) {
    const u32 r0 = wv[i][0][0], r1 = wv[i][0][1], i0 = wv[i][1][0], i1 = wv[i][1][1];
    u32* d = st + (4 * (lane & 7)) * 132 + (16 * nh + 8 * i + (lane >> 3)) * 4 + mp;
    d[0] = (r0 & 0xffffu) | (i0 << 16); d[132] = (r0 >> 16) | (i0 & 0xffff0000u);
    d[264] = (r1 & 0xffffu) | (i1 << 16); d[396] = (r1 >> 16) | (i1 & 0xffff0000u);
  }
}
DI void wt_store(const Params& P, const u32* st, int WT, int tid) {
  const int wa = WT & 15, wcg = WT >> 4, v = wa >> 3, ii = (wa >> 2) & 1, g = wa & 3;
  const __amdgpu_buffer_rsrc_t rsw = __builtin_amdgcn_make_buffer_rsrc((void*)P.ws, 0, (int)WS_END, 0x00020000);
#pragma unroll
  for (int k = 0; k < 2; ++k) {
    const int it = k * 512 + tid, cl = it >> 5, k1 = it & 31;
    const u32x4 o = *(const u32x4*)(st + cl * 132 + k1 * 4);
    __builtin_amdgcn_raw_buffer_store_b128(o, rsw, (u32)((((cl * 4 + (k1 >> 4) * 2) * 2) * 64 + (k1 & 15)) * 16),
                                           (u32)OFF_WT + (u32)(((32 * wcg * 4 + v) * 2 + ii) * 64 + g * 16) * 16u, 16);
  }
}

__global__ void __launch_bounds__(512, 2) k_front(Params P) {
  extern __shared__ __attribute__((aligned(16))) char smem[];
  const int b = blockIdx.x, tid = threadIdx.x, lane = tid & 63, w8 = __builtin_amdgcn_readfirstlane(tid >> 6);
  RSTAMP(0, 14); STAMP(0, 0);
  char* ws = P.ws;
  gu32* flags = (gu32*)(ws + OFF_FLG);
  gu32* epw = (gu32*)(ws + OFF_EPOCH);
  const u32 tag = RFL(__hip_atomic_load(epw, __ATOMIC_RELAXED, __HIP_MEMORY_SCOPE_AGENT)) + 1u;
  const int mp = w8 & 3, nh = w8 >> 2;
  const u32 lb = lds_addr(smem);
  constexpr int GL = 4;
  JobOps J;
  u32x4 q[2][KS1];
  QGen G0;
  auto gen_second = [&](int c5) {
    QGen G1;
    gen_q_init(G1, c5 * 8 + 2 * mp + 1, lane);
#pragma unroll
    for (int t = 0; t < KS1; ++t) q[1][t] = gen_q_step(G1);
#pragma unroll
    for (int t = 0; t < KS1; ++t) asm volatile("" : "+v"(q[1][t]));
  };
  auto genrest = [&](auto T, auto N) { constexpr int t = T, n = N; if constexpr (t + GL < KS1 && n == 0) q[0][t + GL] = gen_q_step(G0); };
  auto lane_now = [&]() { return (int)__builtin_amdgcn_mbcnt_hi(~0u, __builtin_amdgcn_mbcnt_lo(~0u, 0u)); };
  auto publish = [&]() {
    asm volatile("s_waitcnt vmcnt(0)\n\ts_barrier" ::: "memory");
    if (w8 == 0 && lane_now() == 0) __hip_atomic_store(flags + b * 16, tag, __ATOMIC_RELAXED, __HIP_MEMORY_SCOPE_AGENT);
  };
  const bool isW = b >= 160;
  const int sb = b - 160;
  const int ct5 = isW ? sb % 5 : (b >> 3) % 5;
  const int j0 = isW ? 32 + sb / 5 : (b & 7) * 4 + (b >> 3) / 5;
  const bool work = !isW || sb < 95;
  if (isW) {
    if (sb >= 88) {
      unsigned long long pc;
      CODE_ADDR("_Z7k_front6Params", pc); touch_code(pc, FRONT_CODE_BYTES, tid);
      CODE_ADDR("_Z5k_mlpILi0EEv6Params", pc); touch_code(pc, MLP0_CODE_BYTES, tid);
      typedef __attribute__((address_space(1))) const u32 gu1;
      const u32 o1 = (u32)tid * 65536u, o2 = o1 + 512u * 65536u;
      const u32 a = *(gu1*)((unsigned long long)P.ws + (o1 < (u32)WS_END ? o1 : 0u));
      const u32 c = *(gu1*)((unsigned long long)P.ws + (o2 < (u32)WS_END ? o2 : 0u));
      const u32 d = *(gu1*)((unsigned long long)P.out + (o1 < (u32)(S_ * D_ * 2) ? o1 : 0u));
      asm volatile("" :: "v"(a), "v"(c), "v"(d));
    }
    if (!work) {
      for (int fr = w8; fr < 20; fr += 8) *(u32x4*)(ws + OFF_TAB + fr * 1024 + lane * 16) = tab_frag(fr, lane);
      for (int i = tid; i < 2048; i += 512) { ((float*)(ws + OFF_SS2))[i] = 0.f; ((u32*)(ws + OFF_CNT))[i] = 0u; }
      return;
    }
    job_ops(J, P, j0, w8, lane, lb);
    static_for<0, 10>([&](auto K) { job_op<K>(J); asm volatile("s_nop 0"); });
    JobOps J1;
    job_ops(J1, P, j0 + 19, w8, lane, lb + 81920);
    static_for<0, 10>([&](auto K) { job_op<K>(J1); asm volatile("s_nop 0"); });
    gen_second(ct5);
    gen_q_init(G0, ct5 * 8 + 2 * mp, lane);
    static_for<0, GL>([&](auto T) { constexpr int t = T; q[0][t] = gen_q_step(G0); });
  } else {
    job_ops(J, P, j0, w8, lane, lb);
    static_for<0, 10>([&](auto K) { job_op<K>(J); asm volatile("s_nop 0"); });
    u32x2 wv[2][2]; u32 w0r, w0i;
    wt_loads(P, b, w8, lane, wv, w0r, w0i);
    gen_second(ct5);
    asm volatile("s_waitcnt vmcnt(0)" : "+v"(wv[0][0]), "+v"(wv[0][1]), "+v"(wv[1][0]), "+v"(wv[1][1]), "+v"(w0r), "+v"(w0i) :: "memory");
    u32* st = (u32*)(smem + 81920);
    wt_stage(st, w8, lane, wv);
    if ((b >> 4) == 0) {
      asm volatile("s_waitcnt lgkmcnt(0)\n\ts_barrier" ::: "memory");
      if (lane < 16) st[(16 * nh + lane) * 4 + mp] = w0r | (w0i << 16);
    }
    gen_q_init(G0, ct5 * 8 + 2 * mp, lane);
    static_for<0, GL>([&](auto T) { constexpr int t = T; q[0][t] = gen_q_step(G0); });
    asm volatile("s_waitcnt lgkmcnt(0)\n\ts_barrier" ::: "memory");
    wt_store(P, st, b, tid);
  }
  STAMP(0, 1);
  if (!isW) {
    STAMP(0, 2);
    qjob_compute<1>(P, j0, ct5, mp, nh, lane, smem, q, genrest);
    STAMP(0, 3);
    publish();
    STAMP(0, 4);
    int lane2 = lane_now();
    asm volatile("" : "+v"(lane2));
    for (int fr = w8; fr < 20; fr += 8) *(u32x4*)(smem + 98304 + fr * 1024 + lane2 * 16) = tab_frag(fr, lane2);
    col_body<0, true>(P, smem, b, w8 * 64 + lane2, lane2, w8, [&]() {
      const int lane = lane2;
      if (w8 == 0) {
        const int oct = (b & 7) + 8 * (b >> 4);
        const int fi = lane < 32 ? ((lane & 3) * 5 + (oct >> 4)) * 8 + (lane >> 2) : (oct >> 3) * 16 + (lane & 15);
        unsigned spins = 0;
        for (;;) {
          bool ok = __hip_atomic_load(flags + fi * 16, __ATOMIC_RELAXED, __HIP_MEMORY_SCOPE_AGENT) == tag;
          if (b == 0) {
#pragma unroll
            for (int k = 0; k < 3; ++k) { const int f2 = lane + 64 * k; ok &= __hip_atomic_load(flags + (f2 < 160 ? f2 : 0) * 16, __ATOMIC_RELAXED, __HIP_MEMORY_SCOPE_AGENT) == tag; }
          }
          if (__all(ok)) break;
          __builtin_amdgcn_s_sleep(1);
          if (++spins > (1u << 20)) { if (lane == 0) __hip_atomic_store((gu32*)(ws + OFF_EPOCH) + 16, 1u, __ATOMIC_RELAXED, __HIP_MEMORY_SCOPE_AGENT); break; }
        }
        if (b == 0 && lane == 0) __hip_atomic_store(epw, tag, __ATOMIC_RELAXED, __HIP_MEMORY_SCOPE_AGENT);
      }
      asm volatile("s_waitcnt lgkmcnt(0)\n\ts_barrier" ::: "memory");
      STAMP(0, 5);
    });
  } else {
    asm volatile("s_waitcnt vmcnt(10)\n\ts_barrier" ::: "memory");
    STAMP(0, 2);
    qjob_compute<2>(P, j0, ct5, mp, nh, lane, smem, q, genrest);
    STAMP(0, 3);
    for (int jj = 1; jj < 3; ++jj) {
      const int job = j0 + 19 * jj;
      if (job >= 88) break;
      asm volatile("s_waitcnt vmcnt(4) lgkmcnt(0)\n\ts_barrier" ::: "memory");
      if (jj == 1 && job + 19 < 88) {
        JobOps J2;
        job_ops(J2, P, job + 19, w8, lane, lb);
        static_for<0, 10>([&](auto K) { job_op<K>(J2); asm volatile("s_nop 0"); });
      }
      qjob_compute<0>(P, job, ct5, mp, nh, lane, smem + (jj & 1) * 81920, q, [](auto T, auto N) {});
    }
    STAMP(0, 7);
  }
}

DI f32x2 gelu2(f32x2 x) {
  const float k1 = -2.3022081978378774f;
  const float k2 = k1 * 0.044715f;
  const f32x2 t = x * x;
  const f32x2 u = t * k2 + k1;
  const f32x2 z = x * u;
  f32x2 d = f32x2{__builtin_amdgcn_exp2f(z[0]), __builtin_amdgcn_exp2f(z[1])} + 1.f;
  return x * f32x2{__builtin_amdgcn_rcpf(d[0]), __builtin_amdgcn_rcpf(d[1])};
}
template <int IMM> DI void dma16_nom0(i32x4 rsrc, u32 voff, u32 soff) {
  asm volatile(PADS "buffer_load_dwordx4 %0, %1, %2 offen offset:%3 lds" ::"v"(voff), "s"(rsrc), "s"(RFL(soff)), "i"(IMM) : "memory");
}
template <int N> DI void wait_vm_bar(u32x4& a, u32x4& b, u32x4& c, u32x4& d, u32x4& e, u32x4& f, u32x4& g, u32x4& h) { wait_vm_bar8<N>(a, b, c, d, e, f, g, h); }

constexpr int REGPF = 20;
template <int LAYER, int NF, int NQ, class MH, class RH, class EH>
DI void gemm1_body(const Params& P, char* smem, int ft, int tt, int lane, int w, MH&& tail_hook, RH&& reg_hook, EH&& epi_hook) {
  constexpr int KID = LAYER == 0 ? 2 : 5;
  constexpr int NM = NF + NQ, H = (NM + 1) / 2, RING = (LAYER == 0) ? 20 : 16, BR = 12;
  const int c16 = lane & 15, g = lane >> 4;
  char* ws = P.ws;
  const i32x4 rs = mk_rsrc(ws, (u32)WS_END);
  const u32 l16 = lane * 16;
  const int p0 = tt * 128 + w * 32;
  const int f0 = (ft < 12) ? 7 * ft : 84 + 6 * (ft - 12);
  u32x4 bq[BR][2];
  const u32 xoff = (u32)(LAYER == 0 ? OFF_X1 : OFF_X3) + (u32)p0 * 16;
  const u32 bvo = (u32)g * 32768u + (u32)c16 * 16u;
  const u32 asrc = (u32)OFF_A0 + (u32)(ft * KS1 + w) * 7168u, qsrc = (u32)OFF_A1 + (u32)(ft * KS1 + w) * 4096u;
  const u32 adst = lds_addr(smem) + (u32)w * (NM * 1024);
  constexpr int PER = NM + 8;
  auto issue = [&](auto C, auto K) {
    constexpr int c = C, k = K;
    if constexpr (k < NF) {
      constexpr int k4 = k & ~3;
      const u32 so = asrc + (u32)(c * 4 * 7 + k4) * 1024u;
      if constexpr ((k & 3) == 0) dma16<0>(rs, l16, so, adst + (u32)((((c * 4) % RING) * NM + k4) * 1024));
      else dma16_nom0<(k & 3) * 1024>(rs, l16, so);
    } else if constexpr (k < NM) {
      constexpr int kq = k - NF;
      const u32 so = qsrc + (u32)(c * 4 * 4) * 1024u;
      if constexpr (kq == 0) dma16<0>(rs, l16, so, adst + (u32)((((c * 4) % RING) * NM + NF) * 1024));
      else dma16_nom0<kq * 1024>(rs, l16, so);
    } else {
      constexpr int kk = (k - NM) >> 1, t = 4 * c + kk;
      const u32 so = xoff + (u32)t * 131072u;
      if constexpr (((k - NM) & 1) == 0) bld16<0>(bq[t % BR][0], rs, bvo, so);
      else bld16<256>(bq[t % BR][1], rs, bvo, so);
    }
  };
  static_for<0, 2>([&](auto C) { static_for<0, PER>([&](auto K) { issue(C, K); }); });
  u32x2 bw[NF];
  {
    const i32x4 rb1 = mk_rsrc(P.b1, F_ * 2);
#pragma unroll
    for (int m = 0; m < NF; ++m) bld8(bw[m], rb1, (u32)g * 8u, (u32)(f0 + m) * 32u);
  }
  STAMP(KID, 1);
  f32x4 acc[NM][2], gram[2];
#pragma unroll
  for (int m = 0; m < NM; ++m) { acc[m][0] = f32x4{0.f, 0.f, 0.f, 0.f}; acc[m][1] = acc[m][0]; }
  gram[0] = gram[1] = f32x4{0.f, 0.f, 0.f, 0.f};
  u32x4 af[3][H];
  auto waitchunk = [&](auto C, auto N) {
    constexpr int c = C;
    wait_vm_bar<N.value>(bq[(4 * c) % BR][0], bq[(4 * c) % BR][1], bq[(4 * c + 1) % BR][0], bq[(4 * c + 1) % BR][1], bq[(4 * c + 2) % BR][0],
                         bq[(4 * c + 2) % BR][1], bq[(4 * c + 3) % BR][0], bq[(4 * c + 3) % BR][1]);
  };
  auto rd1 = [&](auto HH, auto K) {
    constexpr int h = HH, k = K, t = h >> 1, m0 = (h & 1) ? H : 0, m1 = (h & 1) ? NM : H;
    if constexpr (t < KS1 && m0 + k < m1) af[h % 3][k] = *(const u32x4*)(smem + ((t % RING) * NM + m0 + k) * 1024 + l16);
  };
  waitchunk(IC<0>{}, IC<PER + NF>{});
  STAMP(KID, 2);
  static_for<0, H>([&](auto K) { rd1(IC<0>{}, K); });
  static_for<0, H>([&](auto K) { rd1(IC<1>{}, K); });
  static_for<0, 2 * KS1>([&](auto HH) {
    constexpr int h = HH, t = h >> 1, c = t >> 2, q = t & 3, m0 = (h & 1) ? H : 0, m1 = (h & 1) ? NM : H;
    constexpr int PS = (PER + 2) / 3;
    if constexpr ((h & 1) == 0 && q == 3 && t + 1 < KS1) waitchunk(IC<c + 1>{}, IC<(c + 1 < 4 ? PER : 6)>{});
    constexpr int n1 = (h + 1 < 2 * KS1) ? (((h + 1) & 1) ? NM - H : H) : 0;
    __builtin_amdgcn_s_waitcnt(0xC07F | (n1 << 8));
    SB();
    static_for<m0, m1>([&](auto M) {
      constexpr int m = M;
      acc[m][0] = mfma(af[h % 3][m - m0], bq[t % BR][0], acc[m][0]);
      SB();
      rd1(IC<h + 2>{}, IC<m - m0>{});
      SB();
      acc[m][1] = mfma(af[h % 3][m - m0], bq[t % BR][1], acc[m][1]);
      SB();
      if constexpr (c + 2 < 5 && q < 3 && m < PS && q * PS + m < PER) issue(IC<c + 2>{}, IC<q * PS + m>{});
      if constexpr (h >= 24 && h < 39 && m == m0) tail_hook(IC<h - 24>{}, IC<((LAYER == 0) ? 0 : 4 * NM)>{});
      if constexpr (h >= 30 && m > m0 && m <= m0 + 2 && 2 * (h - 30) + (m - m0 - 1) < REGPF) reg_hook(IC<2 * (h - 30) + (m - m0 - 1)>{});
      SB();
    });
    if constexpr ((h & 1) == 0) {
      u32x4 e0 = bq[t % BR][0], e1 = bq[t % BR][1];
      if constexpr (t == 0) { e0[0] = (g == 0) ? (e0[0] & 0xffffu) : (u32)e0[0]; e1[0] = (g == 0) ? (e1[0] & 0xffffu) : (u32)e1[0]; }
      gram[0] = mfma(e0, e0, gram[0]);
      gram[1] = mfma(e1, e1, gram[1]);
      SB();
    } else if constexpr (NM - H < H) {
      rd1(IC<h + 2>{}, IC<H - 1>{});
    }
  });
  STAMP(KID, 3);
  f32x2 bb[NF][2];
#pragma unroll
  for (int m = 0; m < NF; ++m) {
    asm volatile("" : "+v"(bw[m]));
    bb[m][0] = f32x2{bflo(bw[m][0]), bfhi(bw[m][0])}; bb[m][1] = f32x2{bflo(bw[m][1]), bfhi(bw[m][1])};
  }
  float a_sc[2];
#pragma unroll
  for (int n = 0; n < 2; ++n) {
    const int r = c16 & 3;
    float dv = r == 0 ? gram[n][0] : r == 1 ? gram[n][1] : r == 2 ? gram[n][2] : gram[n][3];
    dv = (g == (c16 >> 2)) ? dv : 0.f;
    const float var = red_g(dv) * (1.f / 640.f);
    const float d1 = ln_den(var);
    a_sc[n] = rcp(d1 * ln_den(var * rcp(d1 * d1)));
  }
  const auto rsb = __builtin_amdgcn_make_buffer_rsrc((void*)ws, 0, (int)WS_END, 0x00020000);
  const u32 vW = ((u32)(lane >> 5) * 16u + (u32)c16) * 16u + (u32)(g & 1) * 8u;
  const u32 hso = (u32)(LAYER == 0 ? OFF_H1 : OFF_H2) + (u32)((tt * 2 + (w >> 1)) * KS2 * 4 + 2 * (w & 1)) * 1024u;
  static_for<0, NF>([&](auto M) {
    constexpr int m = M;
    const int mg = f0 + m;
    const u32 so = hso + (u32)(mg >> 1) * 4096u + (u32)(mg & 1) * 512u;
    epi_hook(M);
#pragma unroll
    for (int n = 0; n < 2; ++n) {
      const f32x4 a = acc[m][n];
      const f32x2 h01 = gelu2(f32x2{a[0], a[1]} * a_sc[n] + bb[m][0]), h23 = gelu2(f32x2{a[2], a[3]} * a_sc[n] + bb[m][1]);
      __builtin_amdgcn_raw_buffer_store_b64(v2u_t{pk2(h01[0], h01[1]), pk2(h23[0], h23[1])}, rsb, vW, so + n * 1024u, HAUX);
    }
  });
  if constexpr (NQ > 0) {
    const int d0 = (ft < 12) ? 2 * ft : 24 + 4 * (ft - 12);
#pragma unroll
    for (int m = 0; m < NQ; ++m)
#pragma unroll
      for (int n = 0; n < 2; ++n) {
        const f32x4 a = acc[NF + m][n];
        __builtin_amdgcn_raw_buffer_store_b64(v2u_t{pk2(a[0], a[1]), pk2(a[2], a[3])}, rsb, (u32)lane * 8u,
                                              (u32)OFF_X3S + (u32)((d0 + m) * 128 + tt * 8 + w * 2 + n) * 512u, 16);
      }
  }
}

template <int LAYER>
__global__ void __launch_bounds__(256, 1) k_mlp(Params P) {
  extern __shared__ __attribute__((aligned(16))) char smem[];
  const int b = blockIdx.x, tid = threadIdx.x, lane = tid & 63, w = __builtin_amdgcn_readfirstlane(tid >> 6);
  constexpr int KID = LAYER == 0 ? 2 : 5;
  RSTAMP(KID, 14); STAMP(KID, 0);
  char* ws = P.ws;
  const int x = b & 7, li = b >> 3;
  const int ft = (x & 1) * 8 + (li & 7), tt = (x >> 1) * 4 + (li >> 3);
  const int ct = ft >> 1, t64 = 2 * tt + (ft & 1);
  const u32 l16 = lane * 16;
  const u32 aoff = (u32)(LAYER == 0 ? OFF_W2F : OFF_W2T) + (u32)ct * (KS2 * 5 * 1024) + (u32)w * 5120u;
  const auto rs = __builtin_amdgcn_make_buffer_rsrc((void*)ws, 0, (int)WS_END, 0x00020000);
  const bool full = (w < 2);
  u32x4 rg[11][5];
  auto lda1 = [&](auto I) {
    constexpr int i = I;
    if (i < 10 || full) {
#pragma unroll
      for (int m = 0; m < 4; ++m) rg[i][m] = __builtin_amdgcn_raw_buffer_load_b128(rs, l16 + m * 1024, aoff + (u32)(3 + i) * 20480u, 0);
      rg[i][4] = __builtin_amdgcn_raw_buffer_load_b128(rs, l16, aoff + (u32)(3 + i) * 20480u + 4096u, 0);
    } else {
#pragma unroll
      for (int m = 0; m < 5; ++m) rg[i][m] = u32x4{0u, 0u, 0u, 0u};
    }
  };
  auto lda_f = [&](auto K) {
    constexpr int k = K, i = k / 5, m = k % 5;
    if constexpr (m < 4) rg[i][m] = __builtin_amdgcn_raw_buffer_load_b128(rs, l16 + m * 1024, aoff + (u32)(3 + i) * 20480u, 0);
    else rg[i][4] = __builtin_amdgcn_raw_buffer_load_b128(rs, l16, aoff + (u32)(3 + i) * 20480u + 4096u, 0);
  };
  u32 lA;
  {
    const i32x4 rsa = mk_rsrc(ws, (u32)WS_END);
    auto tail = [&](auto I, auto KB) {
      constexpr int i = I, st = i / 5, m = i % 5;
      const u32 lw = lds_addr(smem) + (u32)(KB.value * 1024) + (u32)w * 15360u;
      if constexpr (m == 0) dma16<0>(rsa, l16, aoff + st * 20480u, lw + st * 5120u);
      else if constexpr (m < 4) dma16_nom0<m * 1024>(rsa, l16, aoff + st * 20480u);
      else dma16<0>(rsa, l16, aoff + st * 20480u + 4096u, lw + st * 5120u + 4096u);
    };
        auto regh = [&](auto K) { lda_f(K); };
#define EPIPF 10
    auto epi = [&](auto M) { constexpr int m = M;
      if constexpr (2 * m < EPIPF) lda_f(IC<REGPF + 2 * m>{});
      if constexpr (2 * m + 1 < EPIPF) lda_f(IC<REGPF + 2 * m + 1>{}); };
    if (ft < 12) { gemm1_body<LAYER, 7, LAYER == 0 ? 0 : 2>(P, smem, ft, tt, lane, w, tail, regh, epi); lA = (LAYER == 0) ? 0 : 4 * 9 * 1024; }
    else { gemm1_body<LAYER, 6, LAYER == 0 ? 0 : 4>(P, smem, ft, tt, lane, w, tail, regh, epi); lA = (LAYER == 0) ? 0 : 4 * 10 * 1024; }
    lA += (u32)w * 15360u;
  }
  STAMP(KID, 4);
  gu32* cnt = (gu32*)(ws + OFF_CNT) + (LAYER * 16 + tt) * 32;
  asm volatile("s_waitcnt vmcnt(0)\n\ts_barrier" ::: "memory");
  if (tid == 0) __hip_atomic_fetch_add(cnt, 1u, __ATOMIC_RELAXED, __HIP_MEMORY_SCOPE_AGENT);
  STAMP(KID, 5);
  const u32 boff = (u32)(LAYER == 0 ? OFF_H1 : OFF_H2) + (u32)t64 * (KS2 * 4096) + (u32)w * 4096u;
  u32 bo[4];
#pragma unroll
  for (int n = 0; n < 4; ++n) bo[n] = boff + (u32)((n + w) & 3) * 1024u;
#define G2PD 5
  constexpr int PD = G2PD, NSTEP = 14;
  const u32 vX = (u32)(lane >> 5) * 32768u + (u32)((lane >> 4) & 1) * 8u + (u32)(lane & 15) * 16u;
  const u32 xso = (u32)(10 * ct * 2048 + t64 * 64 + w * 16) * 16u;
  v2u_t rz[5]; f32x4 bz[5];
  auto ldbias = [&]() {
#pragma unroll
    for (int m = 0; m < 5; ++m) {
      if (LAYER == 0) {
        rz[m] = __builtin_amdgcn_raw_buffer_load_b64(rs, vX, (u32)OFF_X1 + xso + m * 65536u, 0);
        bz[m] = __builtin_bit_cast(f32x4, __builtin_amdgcn_raw_buffer_load_b128(rs, (u32)(lane >> 4) * 16u, (u32)OFF_B2F + (u32)(ct * 5 + m) * 64u, 0));
      } else {
        const u32x2 bw = *(const u32x2*)(P.b2 + (ct * 5 + m) * 16 + 4 * (lane >> 4));
        bz[m] = f32x4{bflo(bw[0]), bfhi(bw[0]), bflo(bw[1]), bfhi(bw[1])};
      }
    }
  };
  static_for<REGPF + EPIPF, 50>([&](auto K) { lda_f(K); });
  lda1(IC<10>{});
  ldbias();
  SB();
  if (tid == 0) {
    unsigned spins = 0;
    while (__hip_atomic_load(cnt, __ATOMIC_RELAXED, __HIP_MEMORY_SCOPE_AGENT) < 16u) {
      __builtin_amdgcn_s_sleep(1);
      if (++spins > (1u << 22)) { __hip_atomic_store((gu32*)(ws + OFF_CNT) + 1024, 1u, __ATOMIC_RELAXED, __HIP_MEMORY_SCOPE_AGENT); break; }
    }
  }
  asm volatile("s_barrier" ::: "memory");
  STAMP(KID, 6);
  u32x4 rb[PD][4];
  auto ldb = [&](auto I, auto K) { constexpr int i = I, k = K; rb[i % PD][k] = __builtin_amdgcn_raw_buffer_load_b128(rs, l16, bo[k] + (u32)i * 16384u, 16); };
  static_for<0, PD - 1>([&](auto I) { static_for<0, 4>([&](auto K) { ldb(I, K); }); });
  SB();
  if (LAYER == 1) {
#pragma unroll
    for (int m = 0; m < 5; ++m) rz[m] = __builtin_amdgcn_raw_buffer_load_b64(rs, (u32)lane * 8u, (u32)OFF_X3S + (u32)((ct * 5 + m) * 128 + t64 * 4 + w) * 512u, 16);
  }
  f32x4 acc[5][4];
#pragma unroll
  for (int m = 0; m < 5; ++m)
#pragma unroll
    for (int n = 0; n < 4; ++n) acc[m][n] = f32x4{0.f, 0.f, 0.f, 0.f};
  u32x4 al[2][5];
  const char* lbase = smem + lA + l16;
#pragma unroll
  for (int m = 0; m < 5; ++m) al[0][m] = *(const u32x4*)(lbase + m * 1024);
  SB();
  static_for<0, NSTEP - 1>([&](auto I) {
    constexpr int i = I, nx = i + PD - 1;
    static_for<0, 5>([&](auto M) {
      constexpr int m = M;
#pragma unroll
      for (int n = 0; n < 4; ++n) {
        if constexpr (i < 3) acc[m][n] = mfma(al[i & 1][m], rb[i % PD][n], acc[m][n]);
        else acc[m][n] = mfma(rg[i - 3][m], rb[i % PD][n], acc[m][n]);
      }
      SB();
      if constexpr (i + 1 < 3) al[(i + 1) & 1][m] = *(const u32x4*)(lbase + ((i + 1) * 5 + m) * 1024);
      if constexpr (m < 4) {
        if constexpr (nx < NSTEP - 1) ldb(IC<nx>{}, M);
        else if constexpr (nx == NSTEP - 1) { if (full) ldb(IC<nx>{}, M); }
      }
      SB();
    });
  });
  if (full) {
    constexpr int i = NSTEP - 1;
#pragma unroll
    for (int m = 0; m < 5; ++m)
#pragma unroll
      for (int n = 0; n < 4; ++n) acc[m][n] = mfma(rg[10][m], rb[i % PD][n], acc[m][n]);
  }
  SB();
  STAMP(KID, 7);
  asm volatile("s_barrier" ::: "memory");
  f32x4* red = (f32x4*)smem + lane;
#pragma unroll
  for (int n = 1; n < 4; ++n) {
    f32x4* dst = red + (((n + w) & 3) * 15 + (n - 1) * 5) * 64;
#pragma unroll
    for (int m = 0; m < 5; ++m) dst[m * 64] = acc[m][n];
  }
  asm volatile("s_waitcnt lgkmcnt(0)\n\ts_barrier" ::: "memory");
  float ssq = 0.f;
  const f32x4* src = red + w * 15 * 64;
#pragma unroll
  for (int m = 0; m < 5; ++m) {
    const f32x4 o = (acc[m][0] + src[m * 64]) + (src[(5 + m) * 64] + src[(10 + m) * 64]) +
                    (f32x4{bflo(rz[m][0]), bfhi(rz[m][0]), bflo(rz[m][1]), bfhi(rz[m][1])} + bz[m]);
    const v2u_t ov = v2u_t{pk2(o[0], o[1]), pk2(o[2], o[3])};
    if (LAYER == 0) {
      __builtin_amdgcn_raw_buffer_store_b64(ov, rs, vX, (u32)OFF_X2 + xso + m * 65536u, 0);
      f32x4 q2 = o * o;
      if (m == 0) q2[1] = (ct == 0 && lane < 16) ? 0.f : q2[1];
      ssq += (q2[0] + q2[1]) + (q2[2] + q2[3]);
    } else {
      *(v2u_t*)(P.out + (size_t)s_of_p(t64 * 64 + w * 16 + (lane & 15)) * D_ + (ct * 5 + m) * 16 + 4 * (lane >> 4)) = ov;
    }
  }
  if (LAYER == 0) {
    ssq = red_g(ssq);
    if (lane < 16) atomicAdd((float*)(ws + OFF_SS2) + t64 * 64 + w * 16 + lane, ssq);
  }
  STAMP(KID, 8); RSTAMP(KID, 15);
}

extern "C" void kernel_launch(void* const* d_in, const int* in_sizes, int n_in, void* d_out, int out_size, void* d_ws,
                              size_t ws_size, hipStream_t stream) {
  Params P;
  P.x = (const u16*)d_in[0]; P.wr = (const u16*)d_in[1]; P.wi = (const u16*)d_in[2];
  P.w1 = (const u16*)d_in[4]; P.b1 = (const u16*)d_in[5]; P.w2 = (const u16*)d_in[6]; P.b2 = (const u16*)d_in[7];
  P.out = (u16*)d_out; P.ws = (char*)d_ws;
  {
    hipFuncSetAttribute((const void*)k_front, hipFuncAttributeMaxDynamicSharedMemorySize, 163840);
    hipFuncSetAttribute((const void*)k_col1, hipFuncAttributeMaxDynamicSharedMemorySize, 163840);
    hipFuncSetAttribute((const void*)k_mlp<0>, hipFuncAttributeMaxDynamicSharedMemorySize, 163840);
    hipFuncSetAttribute((const void*)k_mlp<1>, hipFuncAttributeMaxDynamicSharedMemorySize, 163840);
  }
  k_front<<<256, 512, 163840, stream>>>(P);
  k_mlp<0><<<256, 256, 163840, stream>>>(P);
  k_col1<<<168, 512, 86016, stream>>>(P);
  k_mlp<1><<<256, 256, 163840, stream>>>(P);
}
```

```cpp
#include <hip/hip_runtime.h>
#include <stdint.h>

typedef __bf16 bf16x2 __attribute__((ext_vector_type(2)));
typedef __bf16 bf16x4 __attribute__((ext_vector_type(4)));
typedef __bf16 bf16x8 __attribute__((ext_vector_type(8)));
typedef float f32x2 __attribute__((ext_vector_type(2)));
typedef float f32x4 __attribute__((ext_vector_type(4)));
typedef unsigned u32;
typedef unsigned u32x2 __attribute__((ext_vector_type(2)));
typedef unsigned u32x4 __attribute__((ext_vector_type(4)));
typedef unsigned short u16;
#define LAS __attribute__((address_space(3)))
#define DI __device__ __forceinline__

constexpr int S_ = 2048, D_ = 640, F_ = 1728, DF_ = 321;
constexpr int KS1 = 20;
constexpr int KS2 = 54;
constexpr float EPS = 1e-5f;

constexpr size_t XT_B = (size_t)80 * 2048 * 16;
constexpr size_t H_B = (size_t)128 * KS2 * 1024;
constexpr size_t A0_B = (size_t)16 * KS1 * 7 * 1024;
constexpr size_t A1_B = (size_t)16 * KS1 * 4 * 1024;
constexpr size_t W2F_B = (size_t)8 * KS2 * 5 * 1024;
constexpr size_t WT_B = (size_t)320 * 8192;
constexpr size_t X3S_B = (size_t)40 * 128 * 512;
constexpr size_t OFF_X0 = 0, OFF_X1 = OFF_X0 + XT_B, OFF_X2 = OFF_X1 + XT_B, OFF_X3 = OFF_X2 + XT_B;
constexpr size_t OFF_H1 = OFF_X3 + XT_B, OFF_H2 = OFF_H1 + H_B;
constexpr size_t OFF_A0 = OFF_H2 + H_B, OFF_A1 = OFF_A0 + A0_B;
constexpr size_t OFF_W2F = OFF_A1 + A1_B, OFF_W2T = OFF_W2F + W2F_B;
constexpr size_t OFF_WT = OFF_W2T + W2F_B;
constexpr size_t OFF_X3S = OFF_WT + WT_B;
constexpr size_t OFF_RSTD0 = OFF_X3S + X3S_B;
constexpr size_t OFF_SS2 = OFF_RSTD0 + 5 * 8192;
constexpr size_t OFF_B2F = OFF_SS2 + 8192;
constexpr size_t OFF_TAB = OFF_B2F + 4096;
constexpr size_t OFF_CNT = OFF_TAB + 20480;
constexpr size_t OFF_FLG = OFF_CNT + 8192;
constexpr size_t OFF_EPOCH = OFF_FLG + 256 * 64;
constexpr size_t WS_END = OFF_EPOCH + 128;

DI u32 pk2(float a, float b) { f32x2 v = {a, b}; bf16x2 r = __builtin_convertvector(v, bf16x2); return __builtin_bit_cast(u32, r); }

DI float bflo(u32 w) { return __builtin_bit_cast(float, w << 16); }
DI float bfhi(u32 w) { return __builtin_bit_cast(float, w & 0xffff0000u); }
DI float rbf(float x) { return bflo(pk2(x, x)); }
DI f32x4 mfma(u32x4 a, u32x4 b, f32x4 c) {
  return __builtin_amdgcn_mfma_f32_16x16x32_bf16(__builtin_bit_cast(bf16x8, a), __builtin_bit_cast(bf16x8, b), c, 0, 0, 0);
}
DI float dot2(u32 a, u32 b, float c) { return __builtin_amdgcn_fdot2_f32_bf16(__builtin_bit_cast(bf16x2, a), __builtin_bit_cast(bf16x2, b), c, false); }
DI float cos2pi(float x) { return __builtin_amdgcn_cosf(x); }
DI float sin2pi(float x) { return __builtin_amdgcn_sinf(x); }
DI int s_of_p(int p) {
  const int r = p >> 9, i = (p >> 7) & 3, g = (p >> 5) & 3, uu = (p >> 4) & 1, c16 = p & 15;
  return 64 * (16 * uu + 4 * g + r) + 16 * i + c16;
}
DI void glds16(const void* g, LAS void* l) { __builtin_amdgcn_global_load_lds(g, l, 16, 0, 0); }
#define WAIT_VM0() asm volatile("s_waitcnt vmcnt(0)" ::: "memory")
#define WAIT_LG0() asm volatile("s_waitcnt lgkmcnt(0)" ::: "memory")
#define BAR() __builtin_amdgcn_s_barrier()
DI float ln_den(float var) { return rbf(__builtin_amdgcn_sqrtf(rbf(rbf(var) + EPS))); }
DI float rcp(float x) { return __builtin_amdgcn_rcpf(x); }

typedef int i32x4 __attribute__((ext_vector_type(4)));
DI i32x4 mk_rsrc(const void* p, u32 bytes) {
  const unsigned long long a = (unsigned long long)p;
  return i32x4{(int)__builtin_amdgcn_readfirstlane((u32)a), (int)__builtin_amdgcn_readfirstlane((u32)(a >> 32) & 0xffffu), (int)bytes, 0x00020000};
}
#define RFL(x) __builtin_amdgcn_readfirstlane(x)
#define PADS
template <int IMM> DI void dma16(i32x4 rsrc, u32 voff, u32 soff, u32 lds) {
  asm volatile(PADS "s_mov_b32 m0, %3\n\ts_nop 0\n\tbuffer_load_dwordx4 %0, %1, %2 offen offset:%4 lds" ::"v"(voff), "s"(rsrc), "s"(RFL(soff)), "s"(RFL(lds)), "i"(IMM) : "memory");
}
template <int IMM, bool SC1 = false> DI void bld16(u32x4& dst, i32x4 rsrc, u32 voff, u32 soff) {
  if constexpr (SC1) asm volatile("buffer_load_dwordx4 %0, %1, %2, %3 offen offset:%4 sc1" : "=v"(dst) : "v"(voff), "s"(rsrc), "s"(RFL(soff)), "i"(IMM) : "memory");
  else asm volatile("buffer_load_dwordx4 %0, %1, %2, %3 offen offset:%4" : "=v"(dst) : "v"(voff), "s"(rsrc), "s"(RFL(soff)), "i"(IMM) : "memory");
}
DI void bldu16(u32& dst, i32x4 rsrc, u32 voff, u32 soff) {
  asm volatile(PADS "buffer_load_ushort %0, %1, %2, %3 offen" : "=v"(dst) : "v"(voff), "s"(rsrc), "s"(RFL(soff)) : "memory");
}
template <bool SC1 = false> DI void bld8(u32x2& dst, i32x4 rsrc, u32 voff, u32 soff) {
  if constexpr (SC1) asm volatile("buffer_load_dwordx2 %0, %1, %2, %3 offen sc1" : "=v"(dst) : "v"(voff), "s"(rsrc), "s"(RFL(soff)) : "memory");
  else asm volatile("buffer_load_dwordx2 %0, %1, %2, %3 offen" : "=v"(dst) : "v"(voff), "s"(rsrc), "s"(RFL(soff)) : "memory");
}
template <bool SC1 = false> DI void bld4(u32& dst, i32x4 rsrc, u32 voff, u32 soff) {
  if constexpr (SC1) asm volatile("buffer_load_dword %0, %1, %2, %3 offen sc1" : "=v"(dst) : "v"(voff), "s"(rsrc), "s"(RFL(soff)) : "memory");
  else asm volatile("buffer_load_dword %0, %1, %2, %3 offen" : "=v"(dst) : "v"(voff), "s"(rsrc), "s"(RFL(soff)) : "memory");
}
#define DMA16(rsrc, voff, soff, lds, IMM) dma16<IMM>(rsrc, voff, soff, lds)
#define BLD16(dst, rsrc, voff, soff, IMM) bld16<IMM>(dst, rsrc, voff, soff)
template <int N> DI void wait_vm_bar8(u32x4& a, u32x4& b, u32x4& c, u32x4& d, u32x4& e, u32x4& f, u32x4& g, u32x4& h) {
  asm volatile("s_waitcnt vmcnt(%8)\n\ts_barrier" : "+v"(a), "+v"(b), "+v"(c), "+v"(d), "+v"(e), "+v"(f), "+v"(g), "+v"(h) : "i"(N) : "memory");
}
#define WAIT_VM(N) asm volatile("s_waitcnt vmcnt(" #N ")" ::: "memory")
DI float red_g(float x) {
  const u32 xi = __builtin_bit_cast(u32, x);
  auto a = __builtin_amdgcn_permlane32_swap(xi, xi, false, false);
  const float y = __builtin_bit_cast(float, (u32)a[0]) + __builtin_bit_cast(float, (u32)a[1]);
  const u32 yi = __builtin_bit_cast(u32, y);
  auto c = __builtin_amdgcn_permlane16_swap(yi, yi, false, false);
  return __builtin_bit_cast(float, (u32)c[0]) + __builtin_bit_cast(float, (u32)c[1]);
}
DI u32 lds_addr(const void* p) { return (u32)(uintptr_t)(LAS const char*)p; }

template <int I> struct IC { static constexpr int value = I; constexpr operator int() const { return I; } };
template <int B, int E, class F> DI void static_for(F&& f) { if constexpr (B < E) { f(IC<B>{}); static_for<B + 1, E>(f); } }
#define SB() __builtin_amdgcn_sched_barrier(0)

#define CODE_ADDR(SYM, out) do { u32 lo_, hi_; \
  asm volatile("s_getpc_b64 s[20:21]\n\ts_add_u32 s20, s20, " SYM "@rel32@lo+4\n\ts_addc_u32 s21, s21, " SYM "@rel32@hi+12\n\ts_mov_b32 %0, s20\n\ts_mov_b32 %1, s21" \
               : "=s"(lo_), "=s"(hi_) :: "s20", "s21", "scc"); out = ((unsigned long long)hi_ << 32) | lo_; } while (0)
DI void touch_code(unsigned long long p, u32 bytes, int tid) {
  typedef __attribute__((address_space(1))) const u32x4 gv4;
  u32x4 v[5];
#pragma unroll
  for (int k = 0; k < 5; ++k) { const u32 o = (u32)tid * 16u + (u32)k * 8192u; v[k] = *(gv4*)(p + (o < bytes ? o : 0u)); }
#pragma unroll
  for (int k = 0; k < 5; ++k) asm volatile("" :: "v"(v[k]));
}
#define TLB_WARM 1
constexpr u32 FRONT_CODE_BYTES = 29696, COL_CODE_BYTES = 3584, MLP0_CODE_BYTES = 24576, MLP1_CODE_BYTES = 28672;

typedef __attribute__((address_space(1))) unsigned gu32;
struct Params {
  const u16 *x, *wr, *wi, *w1, *b1, *w2, *b2;
  u16* out;
  char* ws;
};

#define STAMP(kid, i)
#define RSTAMP(kid, i)

struct QGen { float v0[8], v1[8], k; };
DI void gen_q_init(QGen& G, int ms, int lane) {
  const int c = ms * 16 + (lane & 15), g = lane >> 4;
  int jj = c >> 1; float amp = 0.05590169943749474f; float ph = (c & 1) ? 0.25f : 0.f;
  if (c < 2) { jj = (c == 0) ? 320 : 0; amp = 0.03952847075210474f; ph = 0.f; }
  const float fj = (float)jj * (1.f / 640.f);
  const float kj = 2.f * cos2pi(fj), d = (float)(8 * g);
  G.v0[0] = amp * cos2pi(__builtin_amdgcn_fractf(fj * d) + ph);          G.v0[1] = amp * cos2pi(__builtin_amdgcn_fractf(fj * (d + 1.f)) + ph);
  G.v1[0] = amp * cos2pi(__builtin_amdgcn_fractf(fj * (d + 32.f)) + ph); G.v1[1] = amp * cos2pi(__builtin_amdgcn_fractf(fj * (d + 33.f)) + ph);
#pragma unroll
  for (int j = 2; j < 8; ++j) { G.v0[j] = kj * G.v0[j - 1] - G.v0[j - 2]; G.v1[j] = kj * G.v1[j - 1] - G.v1[j - 2]; }
  G.k = 2.f * cos2pi(__builtin_amdgcn_fractf((float)jj * 0.05f));
}
DI u32x4 gen_q_step(QGen& G) {
  const u32x4 r = u32x4{pk2(G.v0[0], G.v0[1]), pk2(G.v0[2], G.v0[3]), pk2(G.v0[4], G.v0[5]), pk2(G.v0[6], G.v0[7])};
#pragma unroll
  for (int j = 0; j < 8; ++j) { const float n = G.k * G.v1[j] - G.v0[j]; G.v0[j] = G.v1[j]; G.v1[j] = n; }
  return r;
}

struct JobOps { i32x4 rs; u32 vo[10]; u32 l0; };
DI void job_ops(JobOps& J, const Params& P, int job, int w8, int lane, u32 lbuf) {
  asm volatile("" : "+v"(lane), "+s"(job), "+s"(w8));
  if (job >= 32 && job < 59) {
    const int d = 80 * w8 + (lane >> 3), pos = lane & 7;
    J.rs = mk_rsrc(P.w1, D_ * F_ * 2);
    const u32 va = (u32)d * (F_ * 2) + (u32)(job - 32) * 128u + 16u * (u32)(pos ^ (((d >> 1) & 1) << 1));
#pragma unroll
    for (int k = 0; k < 10; ++k) J.vo[k] = ((k & 1) ? (va ^ 64u) : va) + (u32)k * (8 * F_ * 2);
    J.l0 = lbuf + (u32)w8 * 10240u;
  } else {
    const int nn = w8 >> 1, hh = w8 & 1;
    u32 s0;
    if (job < 32) { J.rs = mk_rsrc(P.x, S_ * D_ * 2); s0 = (u32)s_of_p(job * 64 + nn * 16) * (D_ * 2); }
    else if (job < 86) { J.rs = mk_rsrc(P.w2, F_ * D_ * 2); s0 = (u32)((job - 59) * 64 + nn * 16) * (D_ * 2); }
    else { J.rs = mk_rsrc(P.b2, D_ * 2); s0 = 0; }
    J.l0 = lbuf + (u32)nn * 20480u + (u32)hh * 10240u;
    const u32 va = (u32)lane * 16u;
    if (hh == 0) {
#pragma unroll
      for (int k = 0; k < 10; ++k) J.vo[k] = (va ^ (u32)(k * 16)) + (s0 + k * 1280u);
    } else {
      const u32 vb = (u32)(lane >> 4) * 1280u + 1024u + (((u32)(lane & 15) ^ (u32)(lane >> 4)) * 16u);
#pragma unroll
      for (int k = 0; k < 6; ++k) J.vo[k] = (va ^ (u32)((10 + k) * 16)) + (s0 + (10 + k) * 1280u);
#pragma unroll
      for (int k = 6; k < 10; ++k) J.vo[k] = (vb ^ (u32)((k - 6) * 64)) + (s0 + (k - 6) * 5120u);
    }
  }
}
template <int K> DI void job_op(const JobOps& J) {
  if constexpr (K == 0) asm volatile(PADS "s_mov_b32 m0, %2\n\ts_nop 0\n\tbuffer_load_dwordx4 %0, %1, 0 offen lds\n\ts_add_u32 m0, m0, 0x400" ::"v"(J.vo[0]), "s"(J.rs), "s"(RFL(J.l0)) : "memory", "scc");
  else asm volatile(PADS "buffer_load_dwordx4 %0, %1, 0 offen lds\n\ts_add_u32 m0, m0, 0x400" ::"v"(J.vo[K]), "s"(J.rs) : "memory", "scc");
}

DI u32x4 ident_frag(int par, int lane) {
  const int j = 16 * par + (lane & 15) - 8 * (lane >> 4);
  u32x4 r;
#pragma unroll
  for (int k = 0; k < 4; ++k) r[k] = (j == 2 * k ? 0x3F80u : 0u) | (j == 2 * k + 1 ? 0x3F800000u : 0u);
  return r;
}

template <int MODE, class HK>
DI void qloop_lin(const char* buf, int sl, int lane, u32x4 (&q)[2][KS1], f32x4 (&acc)[2][2], f32x4& sum, f32x4& gram, HK&& hook) {
  u32x4 bf[2][2];
  const int row = lane & 15, g = lane >> 4;
  const char* ba[4];
#pragma unroll
  for (int j = 0; j < 4; ++j) ba[j] = buf + row * 1024 + (((4 * j + g) ^ row) * 16);
  const int dB = 16384 - row * 768;
  auto rd = [&](auto T, auto N) { constexpr int t = T, n = N;
    if constexpr (t < 16) bf[t & 1][n] = *(const u32x4*)(ba[t & 3] + n * 20480 + (t >> 2) * 256);
    else if constexpr (t < KS1) bf[t & 1][n] = *(const u32x4*)(ba[t & 3] + dB + n * 20480); };
  rd(IC<0>{}, IC<0>{}); rd(IC<0>{}, IC<1>{});
  static_for<0, KS1>([&](auto T) {
    constexpr int t = T;
    rd(IC<t + 1>{}, IC<0>{}); rd(IC<t + 1>{}, IC<1>{});
    __builtin_amdgcn_s_waitcnt(0xC07F | ((t + 1 < KS1 ? 2 : 0) << 8));
    SB();
    static_for<0, 2>([&](auto N) {
      constexpr int n = N;
      if constexpr (MODE == 2) acc[0][n] = mfma(bf[t & 1][n], q[0][t], acc[0][n]); else acc[0][n] = mfma(q[0][t], bf[t & 1][n], acc[0][n]);
      if constexpr (MODE == 2) acc[1][n] = mfma(bf[t & 1][n], q[1][t], acc[1][n]); else acc[1][n] = mfma(q[1][t], bf[t & 1][n], acc[1][n]);
      SB();
      hook(T, N);
      SB();
    });
    if constexpr (MODE == 1 || MODE == 3) {
      constexpr int sn = (MODE == 3) ? 1 : 0;
#pragma unroll
      for (int k = 0; k < 4; ++k) { const u32 e = bf[t & 1][sn][k]; sum[0] = dot2(e, 0x3F803F80u, sum[0]); }
      gram = mfma(bf[t & 1][sn], bf[t & 1][sn], gram);
      SB();
    }
  });
}
template <class HK>
DI void qloop_tr(const char* buf, int nh, int lane, u32x4 (&q)[2][KS1], f32x4 (&acc)[2][2], HK&& hook) {
  const int g = lane >> 4, pp = lane & 3, qq = (lane & 15) >> 2;
  const int sw = (((qq >> 1) & 1) | ((g & 1) << 1)) << 1;
  const char* base[2];
#pragma unroll
  for (int n = 0; n < 2; ++n) base[n] = buf + (8 * g + qq) * 128 + ((2 * (2 * nh + n) + (pp >> 1)) ^ sw) * 16 + (pp & 1) * 8;
  u32x2 lo[2][2], hi[2][2];
  auto rd = [&](auto T, auto N) { constexpr int t = T, n = N;
    if constexpr (t < KS1) {
      lo[t & 1][n] = __builtin_bit_cast(u32x2, __builtin_amdgcn_ds_read_tr16_b64_v4bf16((LAS bf16x4*)(base[n] + t * 4096)));
      hi[t & 1][n] = __builtin_bit_cast(u32x2, __builtin_amdgcn_ds_read_tr16_b64_v4bf16((LAS bf16x4*)(base[n] + t * 4096 + 512)));
    } };
  rd(IC<0>{}, IC<0>{}); rd(IC<0>{}, IC<1>{});
  static_for<0, KS1>([&](auto T) {
    constexpr int t = T;
    rd(IC<t + 1>{}, IC<0>{}); rd(IC<t + 1>{}, IC<1>{});
    __builtin_amdgcn_s_waitcnt(0xC07F | ((t + 1 < KS1 ? 4 : 0) << 8));
    SB();
    static_for<0, 2>([&](auto N) {
      constexpr int n = N;
      const u32x4 b = u32x4{lo[t & 1][n][0], lo[t & 1][n][1], hi[t & 1][n][0], hi[t & 1][n][1]};
      acc[0][n] = mfma(q[0][t], b, acc[0][n]);
      acc[1][n] = mfma(q[1][t], b, acc[1][n]);
      SB();
      hook(T, N);
      SB();
    });
  });
}

DI u32x4 row_frag(const char* buf, int n, int t, int lane) {
  const int row = lane & 15, g = lane >> 4, x = (((4 * (t & 3) + g) ^ row) * 16);
  return *(const u32x4*)(buf + n * 20480 + (t < 16 ? row * 1024 + (t >> 2) * 256 : 16384 + row * 256) + x);
}
typedef __attribute__((ext_vector_type(2))) unsigned int v2u_t;
#define HAUX 16
template <int AUX = 0> DI void bst8(__amdgpu_buffer_rsrc_t rs, u32 voff, u32 soff, const f32x4& a) {
  __builtin_amdgcn_raw_buffer_store_b64(v2u_t{pk2(a[0], a[1]), pk2(a[2], a[3])}, rs, voff, soff, AUX);
}
struct NoPost { DI void operator()() const {} };
template <int KIND, class HK, class PH = NoPost>
DI void qjob_compute(const Params& P, int job, int ct5, int mp, int nh, int lane, const char* buf, u32x4 (&q)[2][KS1], HK&& hook, PH&& post = NoPost{}) {
  asm volatile("" : "+s"(job), "+s"(ct5), "+s"(mp), "+s"(nh), "+v"(lane));
  const int ms0 = ct5 * 8 + 2 * mp, tc = 4 * ct5 + mp;
  f32x4 acc[2][2], sum = {0.f, 0.f, 0.f, 0.f}, gram = sum;
  acc[0][0] = sum; acc[0][1] = sum; acc[1][0] = sum; acc[1][1] = sum;
  const char* bl = buf + nh * 40960;
  const __amdgpu_buffer_rsrc_t rsw = __builtin_amdgcn_make_buffer_rsrc((void*)P.ws, 0, (int)WS_END, 0x00020000);
  auto vXf = [&]() { asm volatile("" : "+v"(lane)); return (u32)(lane >> 5) * 32768u + (u32)((lane >> 4) & 1) * 8u + (u32)(lane & 15) * 16u; };
  auto vWf = [&]() { asm volatile("" : "+v"(lane)); return ((u32)(lane >> 5) * 16u + (u32)(lane & 15)) * 16u + (u32)((lane >> 4) & 1) * 8u; };
  if constexpr (KIND == 1) {
    const bool st = (mp >> 1) == nh;
    if (!st) qloop_lin<0>(bl, 0, lane, q, acc, sum, gram, hook);
    else {
      if ((mp & 1) == 0) qloop_lin<1>(bl, 0, lane, q, acc, sum, gram, hook); else qloop_lin<3>(bl, 0, lane, q, acc, sum, gram, hook);
      const int c16 = lane & 15, g = lane >> 4, r = c16 & 3;
      const float s1 = red_g(sum[0]);
      const float s2 = r == 0 ? gram[0] : r == 1 ? gram[1] : r == 2 ? gram[2] : gram[3];
      const float m = s1 * (1.f / 640.f);
      float var = s2 * (1.f / 640.f) - m * m; var = var < 0.f ? 0.f : var;
      if (g == (c16 >> 2))
        __builtin_amdgcn_raw_buffer_store_b32(__builtin_bit_cast(u32, rcp(ln_den(var))), rsw, (u32)c16 * 4u, (u32)OFF_RSTD0 + (u32)(ct5 * 2048 + job * 64 + (2 * nh + (mp & 1)) * 16) * 4u, 16);
    }
    post();
    const u32 vX = vXf();
#pragma unroll
    for (int a = 0; a < 2; ++a) {
      const u32 so = (u32)OFF_X0 + (u32)(2 * (ms0 + a) * 2048 + job * 64 + nh * 32) * 16u;
      bst8<16>(rsw, vX, so, acc[a][0]); bst8<16>(rsw, vX, so + 256u, acc[a][1]);
    }
  } else if (KIND == 2 || job < 59) {
    qloop_tr(buf, nh, lane, q, acc, hook);
    const u32 vW = vWf();
#pragma unroll
    for (int n = 0; n < 2; ++n) {
      const int fs = (job - 32) * 4 + 2 * nh + n;
      if (ms0 == 0 && lane < 16) acc[0][n][1] = 0.f;
      const int ft = fs < 84 ? fs / 7 : 12 + (fs - 84) / 6, m = fs < 84 ? fs - 7 * ft : (fs - 84) - 6 * (ft - 12);
      const u32 so = (u32)OFF_A0 + (u32)((ft * KS1 + tc) * 7 + m) * 1024u;
      bst8(rsw, vW, so, acc[0][n]); bst8(rsw, vW, so + 512u, acc[1][n]);
    }
  } else if (job < 86) {
    qloop_lin<2>(bl, 0, lane, q, acc, sum, gram, hook);
    const u32 vW = vWf();
#pragma unroll
    for (int a = 0; a < 2; ++a) {
      const int ms = ms0 + a, ct = ms / 5;
      const u32x4 id = ident_frag(a, lane);
#pragma unroll
      for (int n = 0; n < 2; ++n) {
        const f32x4 tr = mfma(row_frag(bl, n, tc, lane), id, f32x4{0.f, 0.f, 0.f, 0.f});
        const int fs = (job - 59) * 4 + 2 * nh + n;
        const u32 o = (u32)((ct * KS2 + (fs >> 1)) * 5 + (ms - 5 * ct)) * 1024u + (u32)(fs & 1) * 512u;
        bst8(rsw, vW, (u32)OFF_W2F + o, acc[a][n]);
        bst8(rsw, vW, (u32)OFF_W2T + o, tr);
      }
    }
  } else if (job == 86) {
    if (nh == 0) {
#pragma unroll
      for (int t = 0; t < KS1; ++t) { const u32x4 b = row_frag(buf, 0, t, lane); acc[0][0] = mfma(b, q[0][t], acc[0][0]); acc[1][0] = mfma(b, q[1][t], acc[1][0]); }
      if (lane < 16) { ((float*)(P.ws + OFF_B2F))[ms0 * 16 + lane] = acc[0][0][0]; ((float*)(P.ws + OFF_B2F))[ms0 * 16 + 16 + lane] = acc[1][0][0]; }
    }
  } else {
    const u32x4 id = ident_frag(nh, lane);
    const u32 vW = vWf();
    static_for<0, KS1>([&](auto T) {
      constexpr int t = T;
      const u32 fm = t < 12 ? (u32)(t * KS1 * 4) + (u32)nh : (u32)((12 + (t - 12) / 2) * KS1 * 4 + ((t - 12) & 1) * 2) + (u32)nh;
      const u32 so = (u32)OFF_A1 + (fm + (u32)tc * 4u) * 1024u;
      bst8(rsw, vW, so, mfma(q[0][t], id, f32x4{0.f, 0.f, 0.f, 0.f}));
      bst8(rsw, vW, so + 512u, mfma(q[1][t], id, f32x4{0.f, 0.f, 0.f, 0.f}));
    });
  }
}

DI u32x4 tab_frag(int fr, int lane) {
  const int c16 = lane & 15, g = lane >> 4;
  float v[8];
  if (fr < 16) {
    const int tile = fr >> 2, t = (fr >> 1) & 1, part = fr & 1;
#pragma unroll
    for (int j = 0; j < 8; ++j) {
      const int kk = 32 * t + 16 * (j >> 2) + 4 * g + (j & 3);
      const float a = (float)(((16 * tile + c16) * kk) & 63) * (1.f / 64.f);
      v[j] = part ? -sin2pi(a) : cos2pi(a);
    }
  } else {
    const int u = (fr - 16) >> 1, part = fr & 1;
#pragma unroll
    for (int j = 0; j < 8; ++j) {
      const int kk = 16 * (j >> 2) + 4 * g + (j & 3);
      const float a = (float)(((16 * u + c16) * kk) & 31) * (1.f / 32.f);
      v[j] = part ? -sin2pi(a) : cos2pi(a);
    }
  }
  return u32x4{pk2(v[0], v[1]), pk2(v[2], v[3]), pk2(v[4], v[5]), pk2(v[6], v[7])};
}

template <int LAYER, bool FRONT, class WH>
DI void col_body(const Params& P, char* smem, int b, int tid, int lane, int w, WH&& wait_hook) {
  char* ws = P.ws;
  constexpr int KID = LAYER == 0 ? 1 : 4;
  const int oct = (b & 7) + 8 * (b >> 4), hf = (b >> 3) & 1;
  char* sA = smem;
  char* sT = smem + (FRONT ? 102400 : 16384);
  char* sE = smem + 36864;
  char* sG = smem + 53248;
  char* sY = smem + 69632;
  const int cc = w >> 2, u = (w >> 1) & 1, v = w & 1, c16 = lane & 15, g = lane >> 4;
  const int col = oct * 4 + hf * 2 + cc;
  const i32x4 rsw = mk_rsrc(ws, (u32)WS_END);
  const u32 l16 = lane * 16;
  u32x2 raw[4]; u32 st4[4]; u32x4 wv[2];
  auto issue_loads = [&]() {
    const u32 xo = (u32)(LAYER == 0 ? OFF_X0 : OFF_X2) + (u32)oct * 32768u, xv = (u32)tid * 16u + (u32)hf * 8u;
#pragma unroll
    for (int r = 0; r < 4; ++r) bld8<FRONT>(raw[r], rsw, xv, xo + r * 8192u);
#pragma unroll
    for (int r = 0; r < 4; ++r) bld4<FRONT>(st4[r], rsw, (u32)tid * 4u, (LAYER == 0 ? (u32)OFF_RSTD0 + (u32)(oct >> 4) * 8192u : (u32)OFF_SS2) + r * 2048u);
    if constexpr (!FRONT) {
      dma16<0>(rsw, l16, (u32)OFF_TAB + w * 1024u, lds_addr(sT) + w * 1024u);
      dma16<0>(rsw, l16, (u32)OFF_TAB + (8 + w) * 1024u, lds_addr(sT) + (8 + w) * 1024u);
      dma16<0>(rsw, l16, (u32)OFF_TAB + (16 + (w & 3)) * 1024u, lds_addr(sT) + (16 + (w & 3)) * 1024u);
    }
    const u32 wo = (u32)OFF_WT + (u32)((col * 4 + u * 2 + v) * 2) * 1024u;
    bld16<0, FRONT>(wv[0], rsw, l16, wo);
    bld16<1024, FRONT>(wv[1], rsw, l16, wo);
  };
  if constexpr (!FRONT) issue_loads();
  SB();
  float twc[16], tws[16], itc[8], its[8];
  {
    const float fk = (float)(16 * u + c16) * (1.f / 2048.f);
    const float c0 = cos2pi((float)(4 * g) * fk), s0 = sin2pi((float)(4 * g) * fk);
    const float cr = cos2pi(fk), sr = sin2pi(fk), ci = cos2pi(16.f * fk), si = sin2pi(16.f * fk);
    twc[0] = c0; tws[0] = s0;
#pragma unroll
    for (int r = 1; r < 4; ++r) { twc[r] = twc[r - 1] * cr - tws[r - 1] * sr; tws[r] = tws[r - 1] * cr + twc[r - 1] * sr; }
#pragma unroll
    for (int i = 1; i < 4; ++i)
#pragma unroll
      for (int r = 0; r < 4; ++r) {
        twc[i * 4 + r] = twc[(i - 1) * 4 + r] * ci - tws[(i - 1) * 4 + r] * si;
        tws[i * 4 + r] = tws[(i - 1) * 4 + r] * ci + twc[(i - 1) * 4 + r] * si;
      }
#pragma unroll
    for (int ii = 0; ii < 2; ++ii) {
      const float fm = (float)(16 * (2 * v + ii) + c16) * (1.f / 2048.f);
      const float a0 = (float)(16 * u + 4 * g) * fm;
      const float cm = cos2pi(fm), sm = sin2pi(fm);
      itc[ii * 4] = cos2pi(a0) * (1.f / 2048.f); its[ii * 4] = sin2pi(a0) * (1.f / 2048.f);
#pragma unroll
      for (int r = 1; r < 4; ++r) {
        itc[ii * 4 + r] = itc[ii * 4 + r - 1] * cm - its[ii * 4 + r - 1] * sm;
        its[ii * 4 + r] = its[ii * 4 + r - 1] * cm + itc[ii * 4 + r - 1] * sm;
      }
    }
#pragma unroll
    for (int k = 0; k < 16; ++k) asm volatile("" : "+v"(twc[k]), "+v"(tws[k]));
#pragma unroll
    for (int k = 0; k < 8; ++k) asm volatile("" : "+v"(itc[k]), "+v"(its[k]));
  }
  u32x4 f64r[2][2], f64i[2][2], f64n[2][2], f32r, f32i, f32n;
  if constexpr (FRONT) { wait_hook(); issue_loads(); }
  STAMP(KID, 1);
  if constexpr (FRONT) asm volatile("s_waitcnt vmcnt(2)" : "+v"(raw[0]), "+v"(raw[1]), "+v"(raw[2]), "+v"(raw[3]), "+v"(st4[0]), "+v"(st4[1]), "+v"(st4[2]), "+v"(st4[3]) :: "memory");
  else asm volatile("s_waitcnt vmcnt(5)" : "+v"(raw[0]), "+v"(raw[1]), "+v"(raw[2]), "+v"(raw[3]), "+v"(st4[0]), "+v"(st4[1]), "+v"(st4[2]), "+v"(st4[3]) :: "memory");
  const bool special = (oct == 0 && hf == 0);
  {
    float val[4][4];
#pragma unroll
    for (int r = 0; r < 4; ++r) {
      const float sv = __builtin_bit_cast(float, st4[r]);
      const float rs = (LAYER == 0) ? sv : __builtin_amdgcn_rcpf(ln_den(sv * (1.f / 640.f)));
      val[r][0] = bflo(raw[r][0]) * rs; val[r][1] = special ? 0.f : bfhi(raw[r][0]) * rs;
      val[r][2] = bflo(raw[r][1]) * rs; val[r][3] = bfhi(raw[r][1]) * rs;
    }
    const int ti = tid >> 7, tg = (tid >> 5) & 3, tu = (tid >> 4) & 1, tc = tid & 15;
#pragma unroll
    for (int k = 0; k < 4; ++k)
      *(u32x2*)(sA + (k * 4 + ti) * 1024 + (tg * 16 + tc) * 16 + tu * 8) = u32x2{pk2(val[0][k], val[1][k]), pk2(val[2][k], val[3][k])};
  }
  STAMP(KID, 2);
  asm volatile("s_waitcnt vmcnt(2) lgkmcnt(0)\n\ts_barrier" ::: "memory");
  STAMP(KID, 3);
  f32r = *(const u32x4*)(sT + (16 + u * 2 + 0) * 1024 + l16);
  f32i = *(const u32x4*)(sT + (16 + u * 2 + 1) * 1024 + l16);
  f32n = f32i ^ 0x80008000u;
  const f32x4 Z4 = {0.f, 0.f, 0.f, 0.f};
  u32x4 yr[2], yi[2];
  {
    u32x4 xr[4], xi[4];
#pragma unroll
    for (int i = 0; i < 4; ++i) {
      xr[i] = *(const u32x4*)(sA + ((cc * 2 + 0) * 4 + i) * 1024 + l16);
      xi[i] = *(const u32x4*)(sA + ((cc * 2 + 1) * 4 + i) * 1024 + l16);
    }
#pragma unroll
    for (int ii = 0; ii < 2; ++ii)
#pragma unroll
      for (int t = 0; t < 2; ++t) {
        f64r[ii][t] = *(const u32x4*)(sT + (((2 * v + ii) * 2 + t) * 2 + 0) * 1024 + l16);
        f64i[ii][t] = *(const u32x4*)(sT + (((2 * v + ii) * 2 + t) * 2 + 1) * 1024 + l16);
        f64n[ii][t] = f64i[ii][t] ^ 0x80008000u;
      }
    f32x4 dr[4], di[4];
#pragma unroll
    for (int i = 0; i < 4; ++i) { dr[i] = mfma(xr[i], f32r, Z4); di[i] = mfma(xr[i], f32i, Z4); }
#pragma unroll
    for (int i = 0; i < 4; ++i) { dr[i] = mfma(xi[i], f32n, dr[i]); di[i] = mfma(xi[i], f32r, di[i]); }
    float tr[16], tq[16];
#pragma unroll
    for (int i = 0; i < 4; ++i)
#pragma unroll
      for (int r = 0; r < 4; ++r) {
        tr[i * 4 + r] = dr[i][r] * twc[i * 4 + r] + di[i][r] * tws[i * 4 + r];
        tq[i * 4 + r] = di[i][r] * twc[i * 4 + r] - dr[i][r] * tws[i * 4 + r];
      }
#pragma unroll
    for (int t = 0; t < 2; ++t) {
      yr[t] = u32x4{pk2(tr[8 * t + 0], tr[8 * t + 1]), pk2(tr[8 * t + 2], tr[8 * t + 3]), pk2(tr[8 * t + 4], tr[8 * t + 5]), pk2(tr[8 * t + 6], tr[8 * t + 7])};
      yi[t] = u32x4{pk2(tq[8 * t + 0], tq[8 * t + 1]), pk2(tq[8 * t + 2], tq[8 * t + 3]), pk2(tq[8 * t + 4], tq[8 * t + 5]), pk2(tq[8 * t + 6], tq[8 * t + 7])};
    }
  }
  u32x4 zr[2], zi[2];
  {
    f32x4 dr[2] = {Z4, Z4}, di[2] = {Z4, Z4};
#pragma unroll
    for (int t = 0; t < 2; ++t) {
#pragma unroll
      for (int ii = 0; ii < 2; ++ii) { dr[ii] = mfma(f64r[ii][t], yr[t], dr[ii]); di[ii] = mfma(f64i[ii][t], yr[t], di[ii]); }
#pragma unroll
      for (int ii = 0; ii < 2; ++ii) { dr[ii] = mfma(f64n[ii][t], yi[t], dr[ii]); di[ii] = mfma(f64r[ii][t], yi[t], di[ii]); }
    }
    asm volatile("s_waitcnt vmcnt(0)" : "+v"(wv[0]), "+v"(wv[1]) :: "memory");
    float pr[8], pi[8];
#pragma unroll
    for (int ii = 0; ii < 2; ++ii)
#pragma unroll
      for (int r = 0; r < 4; ++r) {
        const u32 wd = wv[ii][r];
        const float a = bflo(wd), bb = bfhi(wd);
        pr[ii * 4 + r] = dr[ii][r] * a - di[ii][r] * bb;
        pi[ii * 4 + r] = dr[ii][r] * bb + di[ii][r] * a;
      }
    const u32x4 mr = u32x4{pk2(pr[0], pr[1]), pk2(pr[2], pr[3]), pk2(pr[4], pr[5]), pk2(pr[6], pr[7])};
    const u32x4 mi = u32x4{pk2(pi[0], pi[1]), pk2(pi[2], pi[3]), pk2(pi[4], pi[5]), pk2(pi[6], pi[7])};
    *(u32x4*)(sE + ((((cc * 2 + u) * 2 + v) * 2 + 0) * 1024) + l16) = mr;
    *(u32x4*)(sE + ((((cc * 2 + u) * 2 + v) * 2 + 1) * 1024) + l16) = mi;
    STAMP(KID, 4);
    asm volatile("s_waitcnt lgkmcnt(0)\n\ts_barrier" ::: "memory");
    const u32x4 or_ = *(const u32x4*)(sE + ((((cc * 2 + u) * 2 + (1 - v)) * 2 + 0) * 1024) + l16);
    const u32x4 oi_ = *(const u32x4*)(sE + ((((cc * 2 + u) * 2 + (1 - v)) * 2 + 1) * 1024) + l16);
    zr[0] = v ? or_ : mr; zi[0] = v ? oi_ : mi;
    zr[1] = v ? mr : or_; zi[1] = v ? mi : oi_;
  }
  {
    f32x4 dr[2] = {Z4, Z4}, di[2] = {Z4, Z4};
#pragma unroll
    for (int t = 0; t < 2; ++t) {
#pragma unroll
      for (int ii = 0; ii < 2; ++ii) { dr[ii] = mfma(zr[t], f64r[ii][t], dr[ii]); di[ii] = mfma(zr[t], f64n[ii][t], di[ii]); }
#pragma unroll
      for (int ii = 0; ii < 2; ++ii) { dr[ii] = mfma(zi[t], f64i[ii][t], dr[ii]); di[ii] = mfma(zi[t], f64r[ii][t], di[ii]); }
    }
#pragma unroll
    for (int ii = 0; ii < 2; ++ii) {
      float tr[4], tq[4];
#pragma unroll
      for (int r = 0; r < 4; ++r) {
        tr[r] = dr[ii][r] * itc[ii * 4 + r] - di[ii][r] * its[ii * 4 + r];
        tq[r] = dr[ii][r] * its[ii * 4 + r] + di[ii][r] * itc[ii * 4 + r];
      }
      *(u32x2*)(sG + ((((cc * 2 + v) * 2 + ii) * 2 + 0) * 1024) + l16 + u * 8) = u32x2{pk2(tr[0], tr[1]), pk2(tr[2], tr[3])};
      *(u32x2*)(sG + ((((cc * 2 + v) * 2 + ii) * 2 + 1) * 1024) + l16 + u * 8) = u32x2{pk2(tq[0], tq[1]), pk2(tq[2], tq[3])};
    }
  }
  STAMP(KID, 5);
  asm volatile("s_waitcnt lgkmcnt(0)\n\ts_barrier" ::: "memory");
  {
    u32x4 tr[2], tq[2];
#pragma unroll
    for (int ii = 0; ii < 2; ++ii) {
      tr[ii] = *(const u32x4*)(sG + ((((cc * 2 + v) * 2 + ii) * 2 + 0) * 1024) + l16);
      tq[ii] = *(const u32x4*)(sG + ((((cc * 2 + v) * 2 + ii) * 2 + 1) * 1024) + l16);
    }
    f32x4 orr[2], oi[2];
#pragma unroll
    for (int ii = 0; ii < 2; ++ii) { orr[ii] = mfma(f32r, tr[ii], Z4); oi[ii] = mfma(f32r, tq[ii], Z4); }
#pragma unroll
    for (int ii = 0; ii < 2; ++ii) { orr[ii] = mfma(f32i, tq[ii], orr[ii]); oi[ii] = mfma(f32n, tr[ii], oi[ii]); }
#pragma unroll
    for (int ii = 0; ii < 2; ++ii) {
      const int tt = (2 * v + ii) * 128 + g * 32 + u * 16 + c16;
      *(u32x4*)(sY + (cc * 512 + tt) * 16) = u32x4{pk2(orr[ii][0], oi[ii][0]), pk2(orr[ii][1], oi[ii][1]), pk2(orr[ii][2], oi[ii][2]), pk2(orr[ii][3], oi[ii][3])};
    }
  }
  STAMP(KID, 6);
  asm volatile("s_waitcnt lgkmcnt(0)\n\ts_barrier" ::: "memory");
  {
    char* Xout = ws + (LAYER == 0 ? OFF_X1 : OFF_X3);
    const u32x4 ya = *(const u32x4*)(sY + tid * 16), yb = *(const u32x4*)(sY + (512 + tid) * 16);
#pragma unroll
    for (int r = 0; r < 4; ++r) {
      const u32 a = ya[r], bq = yb[r], r0 = raw[r][0], r1 = raw[r][1];
      const float v0 = bflo(r0) + bflo(a);
      const float v1 = special ? bfhi(r0) : bfhi(r0) + bfhi(a);
      const float v2 = bflo(r1) + bflo(bq), v3 = bfhi(r1) + bfhi(bq);
      *(u32x2*)(Xout + ((size_t)oct * 2048 + r * 512 + tid) * 16 + hf * 8) = u32x2{pk2(v0, v1), pk2(v2, v3)};
    }
  }
  STAMP(KID, 7); RSTAMP(KID, 15);
}

__global__ void __launch_bounds__(512, 2) k_col1(Params P) {
  extern __shared__ __attribute__((aligned(16))) char smem[];
  const int b = blockIdx.x, tid = threadIdx.x, lane = tid & 63, w = __builtin_amdgcn_readfirstlane(tid >> 6);
  if (b >= 160) {
    unsigned long long pc;
    CODE_ADDR("_Z5k_mlpILi1EEv6Params", pc); touch_code(pc, MLP1_CODE_BYTES, tid);
    return;
  }
  RSTAMP(4, 14); STAMP(4, 0);
  col_body<1, false>(P, smem, b, tid, lane, w, []() {});
}

DI void wt_loads(const Params& P, int WT, int w8, int lane, u32x2 (&wv)[2][2], u32& w0r, u32& w0i) {
  const int wa = WT & 15, wcg = WT >> 4, mp = w8 & 3, nh = w8 >> 2;
  const i32x4 rwr = mk_rsrc(P.wr, S_ * DF_ * 2), rwi = mk_rsrc(P.wi, S_ * DF_ * 2);
  const u32 wvo = (u32)(lane >> 3) * (DF_ * 2) + (u32)(lane & 7) * 8u + (u32)wcg * 64u;
  const u32 wso = (u32)((16 * nh + 32 * (4 * wa + mp)) * DF_ * 2);
  bld8(wv[0][0], rwr, wvo, wso); bld8(wv[0][1], rwi, wvo, wso);
  bld8(wv[1][0], rwr, wvo, wso + 8 * DF_ * 2); bld8(wv[1][1], rwi, wvo, wso + 8 * DF_ * 2);
  bldu16(w0r, rwr, (u32)(lane & 15) * (DF_ * 2) + 640u, wso); bldu16(w0i, rwi, (u32)(lane & 15) * (DF_ * 2) + 640u, wso);
}
DI void wt_stage(u32* st, int w8, int lane, const u32x2 (&wv)[2][2]) {
  const int mp = w8 & 3, nh = w8 >> 2;
#pragma unroll
  for (int i = 0; i < 2; ++i) {
    const u32 r0 = wv[i][0][0], r1 = wv[i][0][1], i0 = wv[i][1][0], i1 = wv[i][1][1];
    u32* d = st + (4 * (lane & 7)) * 132 + (16 * nh + 8 * i + (lane >> 3)) * 4 + mp;
    d[0] = (r0 & 0xffffu) | (i0 << 16); d[132] = (r0 >> 16) | (i0 & 0xffff0000u);
    d[264] = (r1 & 0xffffu) | (i1 << 16); d[396] = (r1 >> 16) | (i1 & 0xffff0000u);
  }
}
DI void wt_store(const Params& P, const u32* st, int WT, int tid) {
  const int wa = WT & 15, wcg = WT >> 4, v = wa >> 3, ii = (wa >> 2) & 1, g = wa & 3;
  const __amdgpu_buffer_rsrc_t rsw = __builtin_amdgcn_make_buffer_rsrc((void*)P.ws, 0, (int)WS_END, 0x00020000);
#pragma unroll
  for (int k = 0; k < 2; ++k) {
    const int it = k * 512 + tid, cl = it >> 5, k1 = it & 31;
    const u32x4 o = *(const u32x4*)(st + cl * 132 + k1 * 4);
    __builtin_amdgcn_raw_buffer_store_b128(o, rsw, (u32)((((cl * 4 + (k1 >> 4) * 2) * 2) * 64 + (k1 & 15)) * 16),
                                           (u32)OFF_WT + (u32)(((32 * wcg * 4 + v) * 2 + ii) * 64 + g * 16) * 16u, 16);
  }
}

__global__ void __launch_bounds__(512, 2) k_front(Params P) {
  extern __shared__ __attribute__((aligned(16))) char smem[];
  const int b = blockIdx.x, tid = threadIdx.x, lane = tid & 63, w8 = __builtin_amdgcn_readfirstlane(tid >> 6);
  RSTAMP(0, 14); STAMP(0, 0);
  char* ws = P.ws;
  gu32* flags = (gu32*)(ws + OFF_FLG);
  gu32* epw = (gu32*)(ws + OFF_EPOCH);
  u32 tag = 0;
  const int mp = w8 & 3, nh = w8 >> 2;
  const u32 lb = lds_addr(smem);
  constexpr int GL = 4;
  JobOps J;
  u32x4 q[2][KS1];
  QGen G0;
  auto gen_second = [&](int c5) {
    QGen G1;
    gen_q_init(G1, c5 * 8 + 2 * mp + 1, lane);
#pragma unroll
    for (int t = 0; t < KS1; ++t) q[1][t] = gen_q_step(G1);
#pragma unroll
    for (int t = 0; t < KS1; ++t) asm volatile("" : "+v"(q[1][t]));
  };
  auto genrest = [&](auto T, auto N) { constexpr int t = T, n = N; if constexpr (t + GL < KS1 && n == 0) q[0][t + GL] = gen_q_step(G0); };
  auto lane_now = [&]() { return (int)__builtin_amdgcn_mbcnt_hi(~0u, __builtin_amdgcn_mbcnt_lo(~0u, 0u)); };
  auto publish = [&]() {
    asm volatile("s_waitcnt vmcnt(0)\n\ts_barrier" ::: "memory");
    if (w8 == 0 && lane_now() == 0) __hip_atomic_store(flags + b * 16, tag, __ATOMIC_RELAXED, __HIP_MEMORY_SCOPE_AGENT);
  };
  const bool isW = b >= 160;
  const int sb = b - 160;
  const int ct5 = isW ? sb % 5 : (b >> 3) % 5;
  const int j0 = isW ? 32 + sb / 5 : (b & 7) * 4 + (b >> 3) / 5;
  const bool work = !isW || sb < 95;
  if (isW) {
    if (sb >= 88) {
      unsigned long long pc;
      CODE_ADDR("_Z7k_front6Params", pc); touch_code(pc, FRONT_CODE_BYTES, tid);
      CODE_ADDR("_Z5k_mlpILi0EEv6Params", pc); touch_code(pc, MLP0_CODE_BYTES, tid);
      typedef __attribute__((address_space(1))) const u32 gu1;
      const u32 o1 = (u32)tid * 65536u, o2 = o1 + 512u * 65536u;
      const u32 a = *(gu1*)((unsigned long long)P.ws + (o1 < (u32)WS_END ? o1 : 0u));
      const u32 c = *(gu1*)((unsigned long long)P.ws + (o2 < (u32)WS_END ? o2 : 0u));
      const u32 d = *(gu1*)((unsigned long long)P.out + (o1 < (u32)(S_ * D_ * 2) ? o1 : 0u));
      asm volatile("" :: "v"(a), "v"(c), "v"(d));
    }
    if (!work) {
      for (int fr = w8; fr < 20; fr += 8) *(u32x4*)(ws + OFF_TAB + fr * 1024 + lane * 16) = tab_frag(fr, lane);
      for (int i = tid; i < 2048; i += 512) { ((float*)(ws + OFF_SS2))[i] = 0.f; ((u32*)(ws + OFF_CNT))[i] = 0u; }
      return;
    }
    job_ops(J, P, j0, w8, lane, lb);
    static_for<0, 10>([&](auto K) { job_op<K>(J); asm volatile("s_nop 0"); });
    JobOps J1;
    job_ops(J1, P, j0 + 19, w8, lane, lb + 81920);
    static_for<0, 10>([&](auto K) { job_op<K>(J1); asm volatile("s_nop 0"); });
    gen_second(ct5);
    gen_q_init(G0, ct5 * 8 + 2 * mp, lane);
    static_for<0, GL>([&](auto T) { constexpr int t = T; q[0][t] = gen_q_step(G0); });
  } else {
    u32 epv;
    asm volatile("global_load_dword %0, %1, %2 sc1" : "=v"(epv) : "v"(0u), "s"((unsigned long long)epw) : "memory");
    job_ops(J, P, j0, w8, lane, lb);
    static_for<0, 10>([&](auto K) { job_op<K>(J); asm volatile("s_nop 0"); });
    gen_second(ct5);
    gen_q_init(G0, ct5 * 8 + 2 * mp, lane);
    static_for<0, GL>([&](auto T) { constexpr int t = T; q[0][t] = gen_q_step(G0); });
    asm volatile("s_waitcnt vmcnt(0)\n\ts_barrier" : "+v"(epv) :: "memory");
    tag = RFL(epv) + 1u;
  }
  STAMP(0, 1);
  if (!isW) {
    STAMP(0, 2);
    u32x2 wv[2][2]; u32 w0r, w0i;
    wt_loads(P, b, w8, lane, wv, w0r, w0i);
    qjob_compute<1>(P, j0, ct5, mp, nh, lane, smem, q, genrest);
    STAMP(0, 3);
    asm volatile("s_waitcnt vmcnt(4)" : "+v"(wv[0][0]), "+v"(wv[0][1]), "+v"(wv[1][0]), "+v"(wv[1][1]), "+v"(w0r), "+v"(w0i) :: "memory");
    u32* st = (u32*)(smem + 81920);
    wt_stage(st, w8, lane, wv);
    if ((b >> 4) == 0) {
      asm volatile("s_waitcnt lgkmcnt(0)\n\ts_barrier" ::: "memory");
      if (lane < 16) st[(16 * nh + lane) * 4 + mp] = w0r | (w0i << 16);
    }
    asm volatile("s_waitcnt lgkmcnt(0)\n\ts_barrier" ::: "memory");
    wt_store(P, st, b, tid);
    for (int fr = w8; fr < 20; fr += 8) *(u32x4*)(smem + 102400 + fr * 1024 + lane * 16) = tab_frag(fr, lane);
    publish();
    STAMP(0, 4);
    int lane2 = lane_now();
    asm volatile("" : "+v"(lane2));
    col_body<0, true>(P, smem, b, w8 * 64 + lane2, lane2, w8, [&]() {
      const int lane = lane2;
      if (w8 == 0) {
        const int oct = (b & 7) + 8 * (b >> 4);
        const int fi = lane < 32 ? ((lane & 3) * 5 + (oct >> 4)) * 8 + (lane >> 2) : (oct >> 3) * 16 + (lane & 15);
        unsigned spins = 0;
        for (;;) {
          bool ok = __hip_atomic_load(flags + fi * 16, __ATOMIC_RELAXED, __HIP_MEMORY_SCOPE_AGENT) == tag;
          if (b == 0) {
#pragma unroll
            for (int k = 0; k < 3; ++k) { const int f2 = lane + 64 * k; ok &= __hip_atomic_load(flags + (f2 < 160 ? f2 : 0) * 16, __ATOMIC_RELAXED, __HIP_MEMORY_SCOPE_AGENT) == tag; }
          }
          if (__all(ok)) break;
          __builtin_amdgcn_s_sleep(1);
          if (++spins > (1u << 20)) { if (lane == 0) __hip_atomic_store((gu32*)(ws + OFF_EPOCH) + 16, 1u, __ATOMIC_RELAXED, __HIP_MEMORY_SCOPE_AGENT); break; }
        }
        if (b == 0 && lane == 0) __hip_atomic_store(epw, tag, __ATOMIC_RELAXED, __HIP_MEMORY_SCOPE_AGENT);
      }
      asm volatile("s_waitcnt lgkmcnt(0)\n\ts_barrier" ::: "memory");
      STAMP(0, 5);
    });
  } else {
    asm volatile("s_waitcnt vmcnt(10)\n\ts_barrier" ::: "memory");
    STAMP(0, 2);
    qjob_compute<2>(P, j0, ct5, mp, nh, lane, smem, q, genrest);
    STAMP(0, 3);
    for (int jj = 1; jj < 3; ++jj) {
      const int job = j0 + 19 * jj;
      if (job >= 88) break;
      asm volatile("s_waitcnt vmcnt(4) lgkmcnt(0)\n\ts_barrier" ::: "memory");
      const bool pf = (jj == 1 && job + 19 < 88);
      JobOps J2;
      if (pf) job_ops(J2, P, job + 19, w8, lane, lb);
      qjob_compute<0>(P, job, ct5, mp, nh, lane, smem + (jj & 1) * 81920, q, [&](auto T, auto N) {
        if constexpr (N.value == 0 && T.value < 10) { if (pf) job_op<T.value>(J2); } });
    }
    STAMP(0, 7);
  }
}

DI f32x2 gelu2(f32x2 x) {
  const float k1 = -2.3022081978378774f;
  const float k2 = k1 * 0.044715f;
  const f32x2 t = x * x;
  const f32x2 u = t * k2 + k1;
  const f32x2 z = x * u;
  f32x2 d = f32x2{__builtin_amdgcn_exp2f(z[0]), __builtin_amdgcn_exp2f(z[1])} + 1.f;
  return x * f32x2{__builtin_amdgcn_rcpf(d[0]), __builtin_amdgcn_rcpf(d[1])};
}
template <int IMM> DI void dma16_nom0(i32x4 rsrc, u32 voff, u32 soff) {
  asm volatile(PADS "buffer_load_dwordx4 %0, %1, %2 offen offset:%3 lds" ::"v"(voff), "s"(rsrc), "s"(RFL(soff)), "i"(IMM) : "memory");
}
template <int N> DI void wait_vm_bar(u32x4& a, u32x4& b, u32x4& c, u32x4& d, u32x4& e, u32x4& f, u32x4& g, u32x4& h) { wait_vm_bar8<N>(a, b, c, d, e, f, g, h); }

constexpr int REGPF = 20;
template <int LAYER, int NF, int NQ, class MH, class RH, class EH>
DI void gemm1_body(const Params& P, char* smem, int ft, int tt, int lane, int w, MH&& tail_hook, RH&& reg_hook, EH&& epi_hook) {
  constexpr int KID = LAYER == 0 ? 2 : 5;
  constexpr int NM = NF + NQ, H = (NM + 1) / 2, RING = (LAYER == 0) ? 20 : 16, BR = 12;
  const int c16 = lane & 15, g = lane >> 4;
  char* ws = P.ws;
  const i32x4 rs = mk_rsrc(ws, (u32)WS_END);
  const u32 l16 = lane * 16;
  const int p0 = tt * 128 + w * 32;
  const int f0 = (ft < 12) ? 7 * ft : 84 + 6 * (ft - 12);
  u32x4 bq[BR][2];
  const u32 xoff = (u32)(LAYER == 0 ? OFF_X1 : OFF_X3) + (u32)p0 * 16;
  const u32 bvo = (u32)g * 32768u + (u32)c16 * 16u;
  const u32 asrc = (u32)OFF_A0 + (u32)(ft * KS1 + w) * 7168u, qsrc = (u32)OFF_A1 + (u32)(ft * KS1 + w) * 4096u;
  const u32 adst = lds_addr(smem) + (u32)w * (NM * 1024);
  constexpr int PER = NM + 8;
  auto issue = [&](auto C, auto K) {
    constexpr int c = C, k = K;
    if constexpr (k < NF) {
      constexpr int k4 = k & ~3;
      const u32 so = asrc + (u32)(c * 4 * 7 + k4) * 1024u;
      if constexpr ((k & 3) == 0) dma16<0>(rs, l16, so, adst + (u32)((((c * 4) % RING) * NM + k4) * 1024));
      else dma16_nom0<(k & 3) * 1024>(rs, l16, so);
    } else if constexpr (k < NM) {
      constexpr int kq = k - NF;
      const u32 so = qsrc + (u32)(c * 4 * 4) * 1024u;
      if constexpr (kq == 0) dma16<0>(rs, l16, so, adst + (u32)((((c * 4) % RING) * NM + NF) * 1024));
      else dma16_nom0<kq * 1024>(rs, l16, so);
    } else {
      constexpr int kk = (k - NM) >> 1, t = 4 * c + kk;
      const u32 so = xoff + (u32)t * 131072u;
      if constexpr (((k - NM) & 1) == 0) bld16<0>(bq[t % BR][0], rs, bvo, so);
      else bld16<256>(bq[t % BR][1], rs, bvo, so);
    }
  };
  static_for<0, 2>([&](auto C) { static_for<0, PER>([&](auto K) { issue(C, K); }); });
  u32x2 bw[NF];
  {
    const i32x4 rb1 = mk_rsrc(P.b1, F_ * 2);
#pragma unroll
    for (int m = 0; m < NF; ++m) bld8(bw[m], rb1, (u32)g * 8u, (u32)(f0 + m) * 32u);
  }
  STAMP(KID, 1);
  f32x4 acc[NM][2], gram[2];
#pragma unroll
  for (int m = 0; m < NM; ++m) { acc[m][0] = f32x4{0.f, 0.f, 0.f, 0.f}; acc[m][1] = acc[m][0]; }
  gram[0] = gram[1] = f32x4{0.f, 0.f, 0.f, 0.f};
  u32x4 af[3][H];
  auto waitchunk = [&](auto C, auto N) {
    constexpr int c = C;
    wait_vm_bar<N.value>(bq[(4 * c) % BR][0], bq[(4 * c) % BR][1], bq[(4 * c + 1) % BR][0], bq[(4 * c + 1) % BR][1], bq[(4 * c + 2) % BR][0],
                         bq[(4 * c + 2) % BR][1], bq[(4 * c + 3) % BR][0], bq[(4 * c + 3) % BR][1]);
  };
  auto rd1 = [&](auto HH, auto K) {
    constexpr int h = HH, k = K, t = h >> 1, m0 = (h & 1) ? H : 0, m1 = (h & 1) ? NM : H;
    if constexpr (t < KS1 && m0 + k < m1) af[h % 3][k] = *(const u32x4*)(smem + ((t % RING) * NM + m0 + k) * 1024 + l16);
  };
  waitchunk(IC<0>{}, IC<PER + NF>{});
  STAMP(KID, 2);
  static_for<0, H>([&](auto K) { rd1(IC<0>{}, K); });
  static_for<0, H>([&](auto K) { rd1(IC<1>{}, K); });
  static_for<0, 2 * KS1>([&](auto HH) {
    constexpr int h = HH, t = h >> 1, c = t >> 2, q = t & 3, m0 = (h & 1) ? H : 0, m1 = (h & 1) ? NM : H;
    constexpr int PS = (PER + 2) / 3;
    if constexpr ((h & 1) == 0 && q == 3 && t + 1 < KS1) waitchunk(IC<c + 1>{}, IC<(c + 1 < 4 ? PER : 6)>{});
    constexpr int n1 = (h + 1 < 2 * KS1) ? (((h + 1) & 1) ? NM - H : H) : 0;
    __builtin_amdgcn_s_waitcnt(0xC07F | (n1 << 8));
    SB();
    static_for<m0, m1>([&](auto M) {
      constexpr int m = M;
      acc[m][0] = mfma(af[h % 3][m - m0], bq[t % BR][0], acc[m][0]);
      SB();
      rd1(IC<h + 2>{}, IC<m - m0>{});
      SB();
      acc[m][1] = mfma(af[h % 3][m - m0], bq[t % BR][1], acc[m][1]);
      SB();
      if constexpr (c + 2 < 5 && q < 3 && m < PS && q * PS + m < PER) issue(IC<c + 2>{}, IC<q * PS + m>{});
      if constexpr (h >= 24 && h < 39 && m == m0) tail_hook(IC<h - 24>{}, IC<((LAYER == 0) ? 0 : 4 * NM)>{});
      if constexpr (h >= 30 && m > m0 && m <= m0 + 2 && 2 * (h - 30) + (m - m0 - 1) < REGPF) reg_hook(IC<2 * (h - 30) + (m - m0 - 1)>{});
      SB();
    });
    if constexpr ((h & 1) == 0) {
      u32x4 e0 = bq[t % BR][0], e1 = bq[t % BR][1];
      if constexpr (t == 0) { e0[0] = (g == 0) ? (e0[0] & 0xffffu) : (u32)e0[0]; e1[0] = (g == 0) ? (e1[0] & 0xffffu) : (u32)e1[0]; }
      gram[0] = mfma(e0, e0, gram[0]);
      gram[1] = mfma(e1, e1, gram[1]);
      SB();
    } else if constexpr (NM - H < H) {
      rd1(IC<h + 2>{}, IC<H - 1>{});
    }
  });
  STAMP(KID, 3);
  f32x2 bb[NF][2];
#pragma unroll
  for (int m = 0; m < NF; ++m) {
    asm volatile("" : "+v"(bw[m]));
    bb[m][0] = f32x2{bflo(bw[m][0]), bfhi(bw[m][0])}; bb[m][1] = f32x2{bflo(bw[m][1]), bfhi(bw[m][1])};
  }
  float a_sc[2];
#pragma unroll
  for (int n = 0; n < 2; ++n) {
    const int r = c16 & 3;
    float dv = r == 0 ? gram[n][0] : r == 1 ? gram[n][1] : r == 2 ? gram[n][2] : gram[n][3];
    dv = (g == (c16 >> 2)) ? dv : 0.f;
    const float var = red_g(dv) * (1.f / 640.f);
    const float d1 = ln_den(var);
    a_sc[n] = rcp(d1 * ln_den(var * rcp(d1 * d1)));
  }
  const auto rsb = __builtin_amdgcn_make_buffer_rsrc((void*)ws, 0, (int)WS_END, 0x00020000);
  const u32 vW = ((u32)(lane >> 5) * 16u + (u32)c16) * 16u + (u32)(g & 1) * 8u;
  const u32 hso = (u32)(LAYER == 0 ? OFF_H1 : OFF_H2) + (u32)((tt * 2 + (w >> 1)) * KS2 * 4 + 2 * (w & 1)) * 1024u;
  static_for<0, NF>([&](auto M) {
    constexpr int m = M;
    const int mg = f0 + m;
    const u32 so = hso + (u32)(mg >> 1) * 4096u + (u32)(mg & 1) * 512u;
    epi_hook(M);
#pragma unroll
    for (int n = 0; n < 2; ++n) {
      const f32x4 a = acc[m][n];
      const f32x2 h01 = gelu2(f32x2{a[0], a[1]} * a_sc[n] + bb[m][0]), h23 = gelu2(f32x2{a[2], a[3]} * a_sc[n] + bb[m][1]);
      __builtin_amdgcn_raw_buffer_store_b64(v2u_t{pk2(h01[0], h01[1]), pk2(h23[0], h23[1])}, rsb, vW, so + n * 1024u, HAUX);
    }
  });
  if constexpr (NQ > 0) {
    const int d0 = (ft < 12) ? 2 * ft : 24 + 4 * (ft - 12);
#pragma unroll
    for (int m = 0; m < NQ; ++m)
#pragma unroll
      for (int n = 0; n < 2; ++n) {
        const f32x4 a = acc[NF + m][n];
        __builtin_amdgcn_raw_buffer_store_b64(v2u_t{pk2(a[0], a[1]), pk2(a[2], a[3])}, rsb, (u32)lane * 8u,
                                              (u32)OFF_X3S + (u32)((d0 + m) * 128 + tt * 8 + w * 2 + n) * 512u, 16);
      }
  }
}

template <int LAYER>
__global__ void __launch_bounds__(256, 1) k_mlp(Params P) {
  extern __shared__ __attribute__((aligned(16))) char smem[];
  const int b = blockIdx.x, tid = threadIdx.x, lane = tid & 63, w = __builtin_amdgcn_readfirstlane(tid >> 6);
  constexpr int KID = LAYER == 0 ? 2 : 5;
  RSTAMP(KID, 14); STAMP(KID, 0);
  char* ws = P.ws;
  const int x = b & 7, li = b >> 3;
  const int ft = (x & 1) * 8 + (li & 7), tt = (x >> 1) * 4 + (li >> 3);
  const int ct = ft >> 1, t64 = 2 * tt + (ft & 1);
  const u32 l16 = lane * 16;
  const u32 aoff = (u32)(LAYER == 0 ? OFF_W2F : OFF_W2T) + (u32)ct * (KS2 * 5 * 1024) + (u32)w * 5120u;
  const auto rs = __builtin_amdgcn_make_buffer_rsrc((void*)ws, 0, (int)WS_END, 0x00020000);
  const bool full = (w < 2);
  u32x4 rg[11][5];
  auto lda1 = [&](auto I) {
    constexpr int i = I;
    if (i < 10 || full) {
#pragma unroll
      for (int m = 0; m < 4; ++m) rg[i][m] = __builtin_amdgcn_raw_buffer_load_b128(rs, l16 + m * 1024, aoff + (u32)(3 + i) * 20480u, 0);
      rg[i][4] = __builtin_amdgcn_raw_buffer_load_b128(rs, l16, aoff + (u32)(3 + i) * 20480u + 4096u, 0);
    } else {
#pragma unroll
      for (int m = 0; m < 5; ++m) rg[i][m] = u32x4{0u, 0u, 0u, 0u};
    }
  };
  auto lda_f = [&](auto K) {
    constexpr int k = K, i = k / 5, m = k % 5;
    if constexpr (m < 4) rg[i][m] = __builtin_amdgcn_raw_buffer_load_b128(rs, l16 + m * 1024, aoff + (u32)(3 + i) * 20480u, 0);
    else rg[i][4] = __builtin_amdgcn_raw_buffer_load_b128(rs, l16, aoff + (u32)(3 + i) * 20480u + 4096u, 0);
  };
  u32 lA;
  {
    const i32x4 rsa = mk_rsrc(ws, (u32)WS_END);
    auto tail = [&](auto I, auto KB) {
      constexpr int i = I, st = i / 5, m = i % 5;
      const u32 lw = lds_addr(smem) + (u32)(KB.value * 1024) + (u32)w * 15360u;
      if constexpr (m == 0) dma16<0>(rsa, l16, aoff + st * 20480u, lw + st * 5120u);
      else if constexpr (m < 4) dma16_nom0<m * 1024>(rsa, l16, aoff + st * 20480u);
      else dma16<0>(rsa, l16, aoff + st * 20480u + 4096u, lw + st * 5120u + 4096u);
    };
        auto regh = [&](auto K) { lda_f(K); };
#define EPIPF 10
    auto epi = [&](auto M) { constexpr int m = M;
      if constexpr (2 * m < EPIPF) lda_f(IC<REGPF + 2 * m>{});
      if constexpr (2 * m + 1 < EPIPF) lda_f(IC<REGPF + 2 * m + 1>{}); };
    if (ft < 12) { gemm1_body<LAYER, 7, LAYER == 0 ? 0 : 2>(P, smem, ft, tt, lane, w, tail, regh, epi); lA = (LAYER == 0) ? 0 : 4 * 9 * 1024; }
    else { gemm1_body<LAYER, 6, LAYER == 0 ? 0 : 4>(P, smem, ft, tt, lane, w, tail, regh, epi); lA = (LAYER == 0) ? 0 : 4 * 10 * 1024; }
    lA += (u32)w * 15360u;
  }
  STAMP(KID, 4);
  gu32* cnt = (gu32*)(ws + OFF_CNT) + (LAYER * 16 + tt) * 32;
  asm volatile("s_waitcnt vmcnt(0)\n\ts_barrier" ::: "memory");
  if (tid == 0) __hip_atomic_fetch_add(cnt, 1u, __ATOMIC_RELAXED, __HIP_MEMORY_SCOPE_AGENT);
  STAMP(KID, 5);
  const u32 boff = (u32)(LAYER == 0 ? OFF_H1 : OFF_H2) + (u32)t64 * (KS2 * 4096) + (u32)w * 4096u;
  u32 bo[4];
#pragma unroll
  for (int n = 0; n < 4; ++n) bo[n] = boff + (u32)((n + w) & 3) * 1024u;
#define G2PD 5
  constexpr int PD = G2PD, NSTEP = 14;
  const u32 vX = (u32)(lane >> 5) * 32768u + (u32)((lane >> 4) & 1) * 8u + (u32)(lane & 15) * 16u;
  const u32 xso = (u32)(10 * ct * 2048 + t64 * 64 + w * 16) * 16u;
  v2u_t rz[5]; f32x4 bz[5];
  auto ldbias = [&]() {
#pragma unroll
    for (int m = 0; m < 5; ++m) {
      if (LAYER == 0) {
        rz[m] = __builtin_amdgcn_raw_buffer_load_b64(rs, vX, (u32)OFF_X1 + xso + m * 65536u, 0);
        bz[m] = __builtin_bit_cast(f32x4, __builtin_amdgcn_raw_buffer_load_b128(rs, (u32)(lane >> 4) * 16u, (u32)OFF_B2F + (u32)(ct * 5 + m) * 64u, 0));
      } else {
        const u32x2 bw = *(const u32x2*)(P.b2 + (ct * 5 + m) * 16 + 4 * (lane >> 4));
        bz[m] = f32x4{bflo(bw[0]), bfhi(bw[0]), bflo(bw[1]), bfhi(bw[1])};
      }
    }
  };
  static_for<REGPF + EPIPF, 50>([&](auto K) { lda_f(K); });
  lda1(IC<10>{});
  ldbias();
  SB();
  if (tid == 0) {
    unsigned spins = 0;
    while (__hip_atomic_load(cnt, __ATOMIC_RELAXED, __HIP_MEMORY_SCOPE_AGENT) < 16u) {
      __builtin_amdgcn_s_sleep(1);
      if (++spins > (1u << 22)) { __hip_atomic_store((gu32*)(ws + OFF_CNT) + 1024, 1u, __ATOMIC_RELAXED, __HIP_MEMORY_SCOPE_AGENT); break; }
    }
  }
  asm volatile("s_barrier" ::: "memory");
  STAMP(KID, 6);
  u32x4 rb[PD][4];
  auto ldb = [&](auto I, auto K) { constexpr int i = I, k = K; rb[i % PD][k] = __builtin_amdgcn_raw_buffer_load_b128(rs, l16, bo[k] + (u32)i * 16384u, 16); };
  static_for<0, PD - 1>([&](auto I) { static_for<0, 4>([&](auto K) { ldb(I, K); }); });
  SB();
  if (LAYER == 1) {
#pragma unroll
    for (int m = 0; m < 5; ++m) rz[m] = __builtin_amdgcn_raw_buffer_load_b64(rs, (u32)lane * 8u, (u32)OFF_X3S + (u32)((ct * 5 + m) * 128 + t64 * 4 + w) * 512u, 16);
  }
  f32x4 acc[5][4];
#pragma unroll
  for (int m = 0; m < 5; ++m)
#pragma unroll
    for (int n = 0; n < 4; ++n) acc[m][n] = f32x4{0.f, 0.f, 0.f, 0.f};
  u32x4 al[2][5];
  const char* lbase = smem + lA + l16;
#pragma unroll
  for (int m = 0; m < 5; ++m) al[0][m] = *(const u32x4*)(lbase + m * 1024);
  SB();
  static_for<0, NSTEP - 1>([&](auto I) {
    constexpr int i = I, nx = i + PD - 1;
    static_for<0, 5>([&](auto M) {
      constexpr int m = M;
#pragma unroll
      for (int n = 0; n < 4; ++n) {
        if constexpr (i < 3) acc[m][n] = mfma(al[i & 1][m], rb[i % PD][n], acc[m][n]);
        else acc[m][n] = mfma(rg[i - 3][m], rb[i % PD][n], acc[m][n]);
      }
      SB();
      if constexpr (i + 1 < 3) al[(i + 1) & 1][m] = *(const u32x4*)(lbase + ((i + 1) * 5 + m) * 1024);
      if constexpr (m < 4) {
        if constexpr (nx < NSTEP - 1) ldb(IC<nx>{}, M);
        else if constexpr (nx == NSTEP - 1) { if (full) ldb(IC<nx>{}, M); }
      }
      SB();
    });
  });
  if (full) {
    constexpr int i = NSTEP - 1;
#pragma unroll
    for (int m = 0; m < 5; ++m)
#pragma unroll
      for (int n = 0; n < 4; ++n) acc[m][n] = mfma(rg[10][m], rb[i % PD][n], acc[m][n]);
  }
  SB();
  STAMP(KID, 7);
  asm volatile("s_barrier" ::: "memory");
  f32x4* red = (f32x4*)smem + lane;
#pragma unroll
  for (int n = 1; n < 4; ++n) {
    f32x4* dst = red + (((n + w) & 3) * 15 + (n - 1) * 5) * 64;
#pragma unroll
    for (int m = 0; m < 5; ++m) dst[m * 64] = acc[m][n];
  }
  asm volatile("s_waitcnt lgkmcnt(0)\n\ts_barrier" ::: "memory");
  float ssq = 0.f;
  const f32x4* src = red + w * 15 * 64;
#pragma unroll
  for (int m = 0; m < 5; ++m) {
    const f32x4 o = (acc[m][0] + src[m * 64]) + (src[(5 + m) * 64] + src[(10 + m) * 64]) +
                    (f32x4{bflo(rz[m][0]), bfhi(rz[m][0]), bflo(rz[m][1]), bfhi(rz[m][1])} + bz[m]);
    const v2u_t ov = v2u_t{pk2(o[0], o[1]), pk2(o[2], o[3])};
    if (LAYER == 0) {
      __builtin_amdgcn_raw_buffer_store_b64(ov, rs, vX, (u32)OFF_X2 + xso + m * 65536u, 0);
      f32x4 q2 = o * o;
      if (m == 0) q2[1] = (ct == 0 && lane < 16) ? 0.f : q2[1];
      ssq += (q2[0] + q2[1]) + (q2[2] + q2[3]);
    } else {
      *(v2u_t*)(P.out + (size_t)s_of_p(t64 * 64 + w * 16 + (lane & 15)) * D_ + (ct * 5 + m) * 16 + 4 * (lane >> 4)) = ov;
    }
  }
  if (LAYER == 0) {
    ssq = red_g(ssq);
    if (lane < 16) atomicAdd((float*)(ws + OFF_SS2) + t64 * 64 + w * 16 + lane, ssq);
  }
  STAMP(KID, 8); RSTAMP(KID, 15);
}

extern "C" void kernel_launch(void* const* d_in, const int* in_sizes, int n_in, void* d_out, int out_size, void* d_ws,
                              size_t ws_size, hipStream_t stream) {
  Params P;
  P.x = (const u16*)d_in[0]; P.wr = (const u16*)d_in[1]; P.wi = (const u16*)d_in[2];
  P.w1 = (const u16*)d_in[4]; P.b1 = (const u16*)d_in[5]; P.w2 = (const u16*)d_in[6]; P.b2 = (const u16*)d_in[7];
  P.out = (u16*)d_out; P.ws = (char*)d_ws;
  {
    hipFuncSetAttribute((const void*)k_front, hipFuncAttributeMaxDynamicSharedMemorySize, 163840);
    hipFuncSetAttribute((const void*)k_col1, hipFuncAttributeMaxDynamicSharedMemorySize, 163840);
    hipFuncSetAttribute((const void*)k_mlp<0>, hipFuncAttributeMaxDynamicSharedMemorySize, 163840);
    hipFuncSetAttribute((const void*)k_mlp<1>, hipFuncAttributeMaxDynamicSharedMemorySize, 163840);
  }
  k_front<<<256, 512, 163840, stream>>>(P);
  k_mlp<0><<<256, 256, 163840, stream>>>(P);
  k_col1<<<168, 512, 86016, stream>>>(P);
  k_mlp<1><<<256, 256, 163840, stream>>>(P);
}
```

```cpp
#include <hip/hip_runtime.h>
#include <stdint.h>

typedef __bf16 bf16x2 __attribute__((ext_vector_type(2)));
typedef __bf16 bf16x4 __attribute__((ext_vector_type(4)));
typedef __bf16 bf16x8 __attribute__((ext_vector_type(8)));
typedef float f32x2 __attribute__((ext_vector_type(2)));
typedef float f32x4 __attribute__((ext_vector_type(4)));
typedef unsigned u32;
typedef unsigned u32x2 __attribute__((ext_vector_type(2)));
typedef unsigned u32x4 __attribute__((ext_vector_type(4)));
typedef unsigned short u16;
#define LAS __attribute__((address_space(3)))
#define DI __device__ __forceinline__

constexpr int S_ = 2048, D_ = 640, F_ = 1728, DF_ = 321;
constexpr int KS1 = 20;
constexpr int KS2 = 54;
constexpr float EPS = 1e-5f;

constexpr size_t XT_B = (size_t)80 * 2048 * 16;
constexpr size_t H_B = (size_t)128 * KS2 * 1024;
constexpr size_t A0_B = (size_t)16 * KS1 * 7 * 1024;
constexpr size_t A1_B = (size_t)16 * KS1 * 4 * 1024;
constexpr size_t W2F_B = (size_t)8 * KS2 * 5 * 1024;
constexpr size_t WT_B = (size_t)320 * 8192;
constexpr size_t X3S_B = (size_t)40 * 128 * 512;
constexpr size_t OFF_X0 = 0, OFF_X1 = OFF_X0 + XT_B, OFF_X2 = OFF_X1 + XT_B, OFF_X3 = OFF_X2 + XT_B;
constexpr size_t OFF_H1 = OFF_X3 + XT_B, OFF_H2 = OFF_H1 + H_B;
constexpr size_t OFF_A0 = OFF_H2 + H_B, OFF_A1 = OFF_A0 + A0_B;
constexpr size_t OFF_W2F = OFF_A1 + A1_B, OFF_W2T = OFF_W2F + W2F_B;
constexpr size_t OFF_WT = OFF_W2T + W2F_B;
constexpr size_t OFF_X3S = OFF_WT + WT_B;
constexpr size_t OFF_RSTD0 = OFF_X3S + X3S_B;
constexpr size_t OFF_SS2 = OFF_RSTD0 + 5 * 8192;
constexpr size_t OFF_B2F = OFF_SS2 + 8192;
constexpr size_t OFF_TAB = OFF_B2F + 4096;
constexpr size_t OFF_CNT = OFF_TAB + 20480;
constexpr size_t OFF_FLG = OFF_CNT + 8192;
constexpr size_t OFF_EPOCH = OFF_FLG + 256 * 64;
constexpr size_t WS_END = OFF_EPOCH + 128;

DI u32 pk2(float a, float b) { f32x2 v = {a, b}; bf16x2 r = __builtin_convertvector(v, bf16x2); return __builtin_bit_cast(u32, r); }

DI float bflo(u32 w) { return __builtin_bit_cast(float, w << 16); }
DI float bfhi(u32 w) { return __builtin_bit_cast(float, w & 0xffff0000u); }
DI float rbf(float x) { return bflo(pk2(x, x)); }
DI f32x4 mfma(u32x4 a, u32x4 b, f32x4 c) {
  return __builtin_amdgcn_mfma_f32_16x16x32_bf16(__builtin_bit_cast(bf16x8, a), __builtin_bit_cast(bf16x8, b), c, 0, 0, 0);
}
DI float dot2(u32 a, u32 b, float c) { return __builtin_amdgcn_fdot2_f32_bf16(__builtin_bit_cast(bf16x2, a), __builtin_bit_cast(bf16x2, b), c, false); }
DI float cos2pi(float x) { return __builtin_amdgcn_cosf(x); }
DI float sin2pi(float x) { return __builtin_amdgcn_sinf(x); }
DI int s_of_p(int p) {
  const int r = p >> 9, i = (p >> 7) & 3, g = (p >> 5) & 3, uu = (p >> 4) & 1, c16 = p & 15;
  return 64 * (16 * uu + 4 * g + r) + 16 * i + c16;
}
DI void glds16(const void* g, LAS void* l) { __builtin_amdgcn_global_load_lds(g, l, 16, 0, 0); }
#define WAIT_VM0() asm volatile("s_waitcnt vmcnt(0)" ::: "memory")
#define WAIT_LG0() asm volatile("s_waitcnt lgkmcnt(0)" ::: "memory")
#define BAR() __builtin_amdgcn_s_barrier()
DI float ln_den(float var) { return rbf(__builtin_amdgcn_sqrtf(rbf(rbf(var) + EPS))); }
DI float rcp(float x) { return __builtin_amdgcn_rcpf(x); }


typedef int i32x4 __attribute__((ext_vector_type(4)));
DI i32x4 mk_rsrc(const void* p, u32 bytes) {
  const unsigned long long a = (unsigned long long)p;
  return i32x4{(int)__builtin_amdgcn_readfirstlane((u32)a), (int)__builtin_amdgcn_readfirstlane((u32)(a >> 32) & 0xffffu), (int)bytes, 0x00020000};
}
#define RFL(x) __builtin_amdgcn_readfirstlane(x)
template <int IMM> DI void dma16(i32x4 rsrc, u32 voff, u32 soff, u32 lds) {
  asm volatile("s_mov_b32 m0, %3\n\ts_nop 0\n\tbuffer_load_dwordx4 %0, %1, %2 offen offset:%4 lds" ::"v"(voff), "s"(rsrc), "s"(RFL(soff)), "s"(RFL(lds)), "i"(IMM) : "memory");
}
template <int IMM, bool SC1 = false> DI void bld16(u32x4& dst, i32x4 rsrc, u32 voff, u32 soff) {
  if constexpr (SC1) asm volatile("buffer_load_dwordx4 %0, %1, %2, %3 offen offset:%4 sc1" : "=v"(dst) : "v"(voff), "s"(rsrc), "s"(RFL(soff)), "i"(IMM) : "memory");
  else asm volatile("buffer_load_dwordx4 %0, %1, %2, %3 offen offset:%4" : "=v"(dst) : "v"(voff), "s"(rsrc), "s"(RFL(soff)), "i"(IMM) : "memory");
}
DI void bldu16(u32& dst, i32x4 rsrc, u32 voff, u32 soff) {
  asm volatile("buffer_load_ushort %0, %1, %2, %3 offen" : "=v"(dst) : "v"(voff), "s"(rsrc), "s"(RFL(soff)) : "memory");
}
template <bool SC1 = false> DI void bld8(u32x2& dst, i32x4 rsrc, u32 voff, u32 soff) {
  if constexpr (SC1) asm volatile("buffer_load_dwordx2 %0, %1, %2, %3 offen sc1" : "=v"(dst) : "v"(voff), "s"(rsrc), "s"(RFL(soff)) : "memory");
  else asm volatile("buffer_load_dwordx2 %0, %1, %2, %3 offen" : "=v"(dst) : "v"(voff), "s"(rsrc), "s"(RFL(soff)) : "memory");
}
template <bool SC1 = false> DI void bld4(u32& dst, i32x4 rsrc, u32 voff, u32 soff) {
  if constexpr (SC1) asm volatile("buffer_load_dword %0, %1, %2, %3 offen sc1" : "=v"(dst) : "v"(voff), "s"(rsrc), "s"(RFL(soff)) : "memory");
  else asm volatile("buffer_load_dword %0, %1, %2, %3 offen" : "=v"(dst) : "v"(voff), "s"(rsrc), "s"(RFL(soff)) : "memory");
}
#define DMA16(rsrc, voff, soff, lds, IMM) dma16<IMM>(rsrc, voff, soff, lds)
#define BLD16(dst, rsrc, voff, soff, IMM) bld16<IMM>(dst, rsrc, voff, soff)
template <int N> DI void wait_vm_bar8(u32x4& a, u32x4& b, u32x4& c, u32x4& d, u32x4& e, u32x4& f, u32x4& g, u32x4& h) {
  asm volatile("s_waitcnt vmcnt(%8)\n\ts_barrier" : "+v"(a), "+v"(b), "+v"(c), "+v"(d), "+v"(e), "+v"(f), "+v"(g), "+v"(h) : "i"(N) : "memory");
}
#define WAIT_VM(N) asm volatile("s_waitcnt vmcnt(" #N ")" ::: "memory")
DI float red_g(float x) {
  const u32 xi = __builtin_bit_cast(u32, x);
  auto a = __builtin_amdgcn_permlane32_swap(xi, xi, false, false);
  const float y = __builtin_bit_cast(float, (u32)a[0]) + __builtin_bit_cast(float, (u32)a[1]);
  const u32 yi = __builtin_bit_cast(u32, y);
  auto c = __builtin_amdgcn_permlane16_swap(yi, yi, false, false);
  return __builtin_bit_cast(float, (u32)c[0]) + __builtin_bit_cast(float, (u32)c[1]);
}
DI u32 lds_addr(const void* p) { return (u32)(uintptr_t)(LAS const char*)p; }


template <int I> struct IC { static constexpr int value = I; constexpr operator int() const { return I; } };
template <int B, int E, class F> DI void static_for(F&& f) { if constexpr (B < E) { f(IC<B>{}); static_for<B + 1, E>(f); } }
#define SB() __builtin_amdgcn_sched_barrier(0)


#define CODE_ADDR(SYM, out) do { u32 lo_, hi_; \
  asm volatile("s_getpc_b64 s[20:21]\n\ts_add_u32 s20, s20, " SYM "@rel32@lo+4\n\ts_addc_u32 s21, s21, " SYM "@rel32@hi+12\n\ts_mov_b32 %0, s20\n\ts_mov_b32 %1, s21" \
               : "=s"(lo_), "=s"(hi_) :: "s20", "s21", "scc"); out = ((unsigned long long)hi_ << 32) | lo_; } while (0)
DI void touch_code(unsigned long long p, u32 bytes, int tid) {
  typedef __attribute__((address_space(1))) const u32x4 gv4;
  u32x4 v[5];
#pragma unroll
  for (int k = 0; k < 5; ++k) { const u32 o = (u32)tid * 16u + (u32)k * 8192u; v[k] = *(gv4*)(p + (o < bytes ? o : 0u)); }
#pragma unroll
  for (int k = 0; k < 5; ++k) asm volatile("" :: "v"(v[k]));
}
constexpr u32 FRONT_CODE_BYTES = 34816, COL_CODE_BYTES = 3584, MLP0_CODE_BYTES = 24576, MLP1_CODE_BYTES = 28672;

typedef __attribute__((address_space(1))) unsigned gu32;
struct Params {
  const u16 *x, *wr, *wi, *w1, *b1, *w2, *b2;
  u16* out;
  char* ws;
};


struct QGen { float v0[8], v1[8], k; };
DI void gen_q_init(QGen& G, int ms, int lane) {
  const int c = ms * 16 + (lane & 15), g = lane >> 4;
  int jj = c >> 1; float amp = 0.05590169943749474f; float ph = (c & 1) ? 0.25f : 0.f;
  if (c < 2) { jj = (c == 0) ? 320 : 0; amp = 0.03952847075210474f; ph = 0.f; }
  const float fj = (float)jj * (1.f / 640.f);
  const float kj = 2.f * cos2pi(fj), d = (float)(8 * g);
  G.v0[0] = amp * cos2pi(__builtin_amdgcn_fractf(fj * d) + ph);          G.v0[1] = amp * cos2pi(__builtin_amdgcn_fractf(fj * (d + 1.f)) + ph);
  G.v1[0] = amp * cos2pi(__builtin_amdgcn_fractf(fj * (d + 32.f)) + ph); G.v1[1] = amp * cos2pi(__builtin_amdgcn_fractf(fj * (d + 33.f)) + ph);
#pragma unroll
  for (int j = 2; j < 8; ++j) { G.v0[j] = kj * G.v0[j - 1] - G.v0[j - 2]; G.v1[j] = kj * G.v1[j - 1] - G.v1[j - 2]; }
  G.k = 2.f * cos2pi(__builtin_amdgcn_fractf((float)jj * 0.05f));
}
DI u32x4 gen_q_step(QGen& G) {
  const u32x4 r = u32x4{pk2(G.v0[0], G.v0[1]), pk2(G.v0[2], G.v0[3]), pk2(G.v0[4], G.v0[5]), pk2(G.v0[6], G.v0[7])};
#pragma unroll
  for (int j = 0; j < 8; ++j) { const float n = G.k * G.v1[j] - G.v0[j]; G.v0[j] = G.v1[j]; G.v1[j] = n; }
  return r;
}

struct JobOps { i32x4 rs; u32 vo[10]; u32 l0; };
DI void job_ops(JobOps& J, const Params& P, int job, int w8, int lane, u32 lbuf) {
  asm volatile("" : "+v"(lane), "+s"(job), "+s"(w8));
  if (job >= 32 && job < 59) {
    const int d = 80 * w8 + (lane >> 3), pos = lane & 7;
    J.rs = mk_rsrc(P.w1, D_ * F_ * 2);
    const u32 va = (u32)d * (F_ * 2) + (u32)(job - 32) * 128u + 16u * (u32)(pos ^ (((d >> 1) & 1) << 1));
#pragma unroll
    for (int k = 0; k < 10; ++k) J.vo[k] = ((k & 1) ? (va ^ 64u) : va) + (u32)k * (8 * F_ * 2);
    J.l0 = lbuf + (u32)w8 * 10240u;
  } else {
    const int nn = w8 >> 1, hh = w8 & 1;
    u32 s0;
    if (job < 32) { J.rs = mk_rsrc(P.x, S_ * D_ * 2); s0 = (u32)s_of_p(job * 64 + nn * 16) * (D_ * 2); }
    else if (job < 86) { J.rs = mk_rsrc(P.w2, F_ * D_ * 2); s0 = (u32)((job - 59) * 64 + nn * 16) * (D_ * 2); }
    else { J.rs = mk_rsrc(P.b2, D_ * 2); s0 = 0; }
    J.l0 = lbuf + (u32)nn * 20480u + (u32)hh * 10240u;
    const u32 va = (u32)lane * 16u;
    if (hh == 0) {
#pragma unroll
      for (int k = 0; k < 10; ++k) J.vo[k] = (va ^ (u32)(k * 16)) + (s0 + k * 1280u);
    } else {
      const u32 vb = (u32)(lane >> 4) * 1280u + 1024u + (((u32)(lane & 15) ^ (u32)(lane >> 4)) * 16u);
#pragma unroll
      for (int k = 0; k < 6; ++k) J.vo[k] = (va ^ (u32)((10 + k) * 16)) + (s0 + (10 + k) * 1280u);
#pragma unroll
      for (int k = 6; k < 10; ++k) J.vo[k] = (vb ^ (u32)((k - 6) * 64)) + (s0 + (k - 6) * 5120u);
    }
  }
}
template <int K> DI void job_op(const JobOps& J) {
  if constexpr (K == 0) asm volatile("s_mov_b32 m0, %2\n\ts_nop 0\n\tbuffer_load_dwordx4 %0, %1, 0 offen lds\n\ts_add_u32 m0, m0, 0x400" ::"v"(J.vo[0]), "s"(J.rs), "s"(RFL(J.l0)) : "memory", "scc");
  else asm volatile("buffer_load_dwordx4 %0, %1, 0 offen lds\n\ts_add_u32 m0, m0, 0x400" ::"v"(J.vo[K]), "s"(J.rs) : "memory", "scc");
}

DI u32x4 ident_frag(int par, int lane) {
  const int j = 16 * par + (lane & 15) - 8 * (lane >> 4);
  u32x4 r;
#pragma unroll
  for (int k = 0; k < 4; ++k) r[k] = (j == 2 * k ? 0x3F80u : 0u) | (j == 2 * k + 1 ? 0x3F800000u : 0u);
  return r;
}

template <int MODE, class HK>
DI void qloop_lin(const char* buf, int sl, int lane, u32x4 (&q)[2][KS1], f32x4 (&acc)[2][2], f32x4& sum, f32x4& gram, HK&& hook) {
  u32x4 bf[2][2];
  const int row = lane & 15, g = lane >> 4;
  const char* ba[4];
#pragma unroll
  for (int j = 0; j < 4; ++j) ba[j] = buf + row * 1024 + (((4 * j + g) ^ row) * 16);
  const int dB = 16384 - row * 768;
  auto rd = [&](auto T, auto N) { constexpr int t = T, n = N;
    if constexpr (t < 16) bf[t & 1][n] = *(const u32x4*)(ba[t & 3] + n * 20480 + (t >> 2) * 256);
    else if constexpr (t < KS1) bf[t & 1][n] = *(const u32x4*)(ba[t & 3] + dB + n * 20480); };
  rd(IC<0>{}, IC<0>{}); rd(IC<0>{}, IC<1>{});
  static_for<0, KS1>([&](auto T) {
    constexpr int t = T;
    rd(IC<t + 1>{}, IC<0>{}); rd(IC<t + 1>{}, IC<1>{});
    __builtin_amdgcn_s_waitcnt(0xC07F | ((t + 1 < KS1 ? 2 : 0) << 8));
    SB();
    static_for<0, 2>([&](auto N) {
      constexpr int n = N;
      if constexpr (MODE == 2) acc[0][n] = mfma(bf[t & 1][n], q[0][t], acc[0][n]); else acc[0][n] = mfma(q[0][t], bf[t & 1][n], acc[0][n]);
      if constexpr (MODE == 2) acc[1][n] = mfma(bf[t & 1][n], q[1][t], acc[1][n]); else acc[1][n] = mfma(q[1][t], bf[t & 1][n], acc[1][n]);
      SB();
      hook(T, N);
      SB();
    });
    if constexpr (MODE == 1 || MODE == 3) {
      constexpr int sn = (MODE == 3) ? 1 : 0;
#pragma unroll
      for (int k = 0; k < 4; ++k) { const u32 e = bf[t & 1][sn][k]; sum[0] = dot2(e, 0x3F803F80u, sum[0]); }
      gram = mfma(bf[t & 1][sn], bf[t & 1][sn], gram);
      SB();
    }
  });
}
template <class HK>
DI void qloop_tr(const char* buf, int nh, int lane, u32x4 (&q)[2][KS1], f32x4 (&acc)[2][2], HK&& hook) {
  const int g = lane >> 4, pp = lane & 3, qq = (lane & 15) >> 2;
  const int sw = (((qq >> 1) & 1) | ((g & 1) << 1)) << 1;
  const char* base[2];
#pragma unroll
  for (int n = 0; n < 2; ++n) base[n] = buf + (8 * g + qq) * 128 + ((2 * (2 * nh + n) + (pp >> 1)) ^ sw) * 16 + (pp & 1) * 8;
  u32x2 lo[2][2], hi[2][2];
  auto rd = [&](auto T, auto N) { constexpr int t = T, n = N;
    if constexpr (t < KS1) {
      lo[t & 1][n] = __builtin_bit_cast(u32x2, __builtin_amdgcn_ds_read_tr16_b64_v4bf16((LAS bf16x4*)(base[n] + t * 4096)));
      hi[t & 1][n] = __builtin_bit_cast(u32x2, __builtin_amdgcn_ds_read_tr16_b64_v4bf16((LAS bf16x4*)(base[n] + t * 4096 + 512)));
    } };
  rd(IC<0>{}, IC<0>{}); rd(IC<0>{}, IC<1>{});
  static_for<0, KS1>([&](auto T) {
    constexpr int t = T;
    rd(IC<t + 1>{}, IC<0>{}); rd(IC<t + 1>{}, IC<1>{});
    __builtin_amdgcn_s_waitcnt(0xC07F | ((t + 1 < KS1 ? 4 : 0) << 8));
    SB();
    static_for<0, 2>([&](auto N) {
      constexpr int n = N;
      const u32x4 b = u32x4{lo[t & 1][n][0], lo[t & 1][n][1], hi[t & 1][n][0], hi[t & 1][n][1]};
      acc[0][n] = mfma(q[0][t], b, acc[0][n]);
      acc[1][n] = mfma(q[1][t], b, acc[1][n]);
      SB();
      hook(T, N);
      SB();
    });
  });
}

DI u32x4 row_frag(const char* buf, int n, int t, int lane) {
  const int row = lane & 15, g = lane >> 4, x = (((4 * (t & 3) + g) ^ row) * 16);
  return *(const u32x4*)(buf + n * 20480 + (t < 16 ? row * 1024 + (t >> 2) * 256 : 16384 + row * 256) + x);
}
typedef __attribute__((ext_vector_type(2))) unsigned int v2u_t;
template <int AUX = 0> DI void bst8(__amdgpu_buffer_rsrc_t rs, u32 voff, u32 soff, const f32x4& a) {
  __builtin_amdgcn_raw_buffer_store_b64(v2u_t{pk2(a[0], a[1]), pk2(a[2], a[3])}, rs, voff, soff, AUX);
}
struct NoPost { DI void operator()() const {} };
template <int KIND, class HK, class PH = NoPost>
DI void qjob_compute(const Params& P, int job, int ct5, int mp, int nh, int lane, const char* buf, u32x4 (&q)[2][KS1], HK&& hook, PH&& post = NoPost{}) {
  asm volatile("" : "+s"(job), "+s"(ct5), "+s"(mp), "+s"(nh), "+v"(lane));
  const int ms0 = ct5 * 8 + 2 * mp, tc = 4 * ct5 + mp;
  f32x4 acc[2][2], sum = {0.f, 0.f, 0.f, 0.f}, gram = sum;
  acc[0][0] = sum; acc[0][1] = sum; acc[1][0] = sum; acc[1][1] = sum;
  const char* bl = buf + nh * 40960;
  const __amdgpu_buffer_rsrc_t rsw = __builtin_amdgcn_make_buffer_rsrc((void*)P.ws, 0, (int)WS_END, 0x00020000);
  auto vXf = [&]() { asm volatile("" : "+v"(lane)); return (u32)(lane >> 5) * 32768u + (u32)((lane >> 4) & 1) * 8u + (u32)(lane & 15) * 16u; };
  auto vWf = [&]() { asm volatile("" : "+v"(lane)); return ((u32)(lane >> 5) * 16u + (u32)(lane & 15)) * 16u + (u32)((lane >> 4) & 1) * 8u; };
  if constexpr (KIND == 1) {
    const bool st = (mp >> 1) == nh;
    if (!st) qloop_lin<0>(bl, 0, lane, q, acc, sum, gram, hook);
    else {
      if ((mp & 1) == 0) qloop_lin<1>(bl, 0, lane, q, acc, sum, gram, hook); else qloop_lin<3>(bl, 0, lane, q, acc, sum, gram, hook);
      const int c16 = lane & 15, g = lane >> 4, r = c16 & 3;
      const float s1 = red_g(sum[0]);
      const float s2 = r == 0 ? gram[0] : r == 1 ? gram[1] : r == 2 ? gram[2] : gram[3];
      const float m = s1 * (1.f / 640.f);
      float var = s2 * (1.f / 640.f) - m * m; var = var < 0.f ? 0.f : var;
      if (g == (c16 >> 2))
        __builtin_amdgcn_raw_buffer_store_b32(__builtin_bit_cast(u32, rcp(ln_den(var))), rsw, (u32)c16 * 4u, (u32)OFF_RSTD0 + (u32)(ct5 * 2048 + job * 64 + (2 * nh + (mp & 1)) * 16) * 4u, 16);
    }
    post();
    const u32 vX = vXf();
#pragma unroll
    for (int a = 0; a < 2; ++a) {
      const u32 so = (u32)OFF_X0 + (u32)(2 * (ms0 + a) * 2048 + job * 64 + nh * 32) * 16u;
      bst8<16>(rsw, vX, so, acc[a][0]); bst8<16>(rsw, vX, so + 256u, acc[a][1]);
    }
  } else if (KIND == 2 || job < 59) {
    qloop_tr(buf, nh, lane, q, acc, hook);
    const u32 vW = vWf();
#pragma unroll
    for (int n = 0; n < 2; ++n) {
      const int fs = (job - 32) * 4 + 2 * nh + n;
      if (ms0 == 0 && lane < 16) acc[0][n][1] = 0.f;
      const int ft = fs < 84 ? fs / 7 : 12 + (fs - 84) / 6, m = fs < 84 ? fs - 7 * ft : (fs - 84) - 6 * (ft - 12);
      const u32 so = (u32)OFF_A0 + (u32)((ft * KS1 + tc) * 7 + m) * 1024u;
      bst8(rsw, vW, so, acc[0][n]); bst8(rsw, vW, so + 512u, acc[1][n]);
    }
  } else if (job < 86) {
    qloop_lin<2>(bl, 0, lane, q, acc, sum, gram, hook);
    const u32 vW = vWf();
#pragma unroll
    for (int a = 0; a < 2; ++a) {
      const int ms = ms0 + a, ct = ms / 5;
      const u32x4 id = ident_frag(a, lane);
#pragma unroll
      for (int n = 0; n < 2; ++n) {
        const f32x4 tr = mfma(row_frag(bl, n, tc, lane), id, f32x4{0.f, 0.f, 0.f, 0.f});
        const int fs = (job - 59) * 4 + 2 * nh + n;
        const u32 o = (u32)((ct * KS2 + (fs >> 1)) * 5 + (ms - 5 * ct)) * 1024u + (u32)(fs & 1) * 512u;
        bst8(rsw, vW, (u32)OFF_W2F + o, acc[a][n]);
        bst8(rsw, vW, (u32)OFF_W2T + o, tr);
      }
    }
  } else if (job == 86) {
    if (nh == 0) {
#pragma unroll
      for (int t = 0; t < KS1; ++t) { const u32x4 b = row_frag(buf, 0, t, lane); acc[0][0] = mfma(b, q[0][t], acc[0][0]); acc[1][0] = mfma(b, q[1][t], acc[1][0]); }
      if (lane < 16) { ((float*)(P.ws + OFF_B2F))[ms0 * 16 + lane] = acc[0][0][0]; ((float*)(P.ws + OFF_B2F))[ms0 * 16 + 16 + lane] = acc[1][0][0]; }
    }
  } else {
    const u32x4 id = ident_frag(nh, lane);
    const u32 vW = vWf();
    static_for<0, KS1>([&](auto T) {
      constexpr int t = T;
      const u32 fm = t < 12 ? (u32)(t * KS1 * 4) + (u32)nh : (u32)((12 + (t - 12) / 2) * KS1 * 4 + ((t - 12) & 1) * 2) + (u32)nh;
      const u32 so = (u32)OFF_A1 + (fm + (u32)tc * 4u) * 1024u;
      bst8(rsw, vW, so, mfma(q[0][t], id, f32x4{0.f, 0.f, 0.f, 0.f}));
      bst8(rsw, vW, so + 512u, mfma(q[1][t], id, f32x4{0.f, 0.f, 0.f, 0.f}));
    });
  }
}


DI u32x4 tab_frag(int fr, int lane) {
  const int c16 = lane & 15, g = lane >> 4;
  float v[8];
  if (fr < 16) {
    const int tile = fr >> 2, t = (fr >> 1) & 1, part = fr & 1;
#pragma unroll
    for (int j = 0; j < 8; ++j) {
      const int kk = 32 * t + 16 * (j >> 2) + 4 * g + (j & 3);
      const float a = (float)(((16 * tile + c16) * kk) & 63) * (1.f / 64.f);
      v[j] = part ? -sin2pi(a) : cos2pi(a);
    }
  } else {
    const int u = (fr - 16) >> 1, part = fr & 1;
#pragma unroll
    for (int j = 0; j < 8; ++j) {
      const int kk = 16 * (j >> 2) + 4 * g + (j & 3);
      const float a = (float)(((16 * u + c16) * kk) & 31) * (1.f / 32.f);
      v[j] = part ? -sin2pi(a) : cos2pi(a);
    }
  }
  return u32x4{pk2(v[0], v[1]), pk2(v[2], v[3]), pk2(v[4], v[5]), pk2(v[6], v[7])};
}

template <int LAYER, bool FRONT, class WH>
DI void col_body(const Params& P, char* smem, int b, int tid, int lane, int w, WH&& wait_hook) {
  char* ws = P.ws;
  const int oct = (b & 7) + 8 * (b >> 4), hf = (b >> 3) & 1;
  char* sA = smem;
  char* sT = smem + (FRONT ? 102400 : 16384);
  char* sE = smem + 36864;
  char* sG = smem + 53248;
  char* sY = smem + 69632;
  const int cc = w >> 2, u = (w >> 1) & 1, v = w & 1, c16 = lane & 15, g = lane >> 4;
  const int col = oct * 4 + hf * 2 + cc;
  const i32x4 rsw = mk_rsrc(ws, (u32)WS_END);
  const u32 l16 = lane * 16;
  u32x2 raw[4]; u32 st4[4]; u32x4 wv[2];
  auto issue_loads = [&]() {
    const u32 xo = (u32)(LAYER == 0 ? OFF_X0 : OFF_X2) + (u32)oct * 32768u, xv = (u32)tid * 16u + (u32)hf * 8u;
#pragma unroll
    for (int r = 0; r < 4; ++r) bld8<FRONT>(raw[r], rsw, xv, xo + r * 8192u);
#pragma unroll
    for (int r = 0; r < 4; ++r) bld4<FRONT>(st4[r], rsw, (u32)tid * 4u, (LAYER == 0 ? (u32)OFF_RSTD0 + (u32)(oct >> 4) * 8192u : (u32)OFF_SS2) + r * 2048u);
    if constexpr (!FRONT) {
      dma16<0>(rsw, l16, (u32)OFF_TAB + w * 1024u, lds_addr(sT) + w * 1024u);
      dma16<0>(rsw, l16, (u32)OFF_TAB + (8 + w) * 1024u, lds_addr(sT) + (8 + w) * 1024u);
      dma16<0>(rsw, l16, (u32)OFF_TAB + (16 + (w & 3)) * 1024u, lds_addr(sT) + (16 + (w & 3)) * 1024u);
    }
    const u32 wo = (u32)OFF_WT + (u32)((col * 4 + u * 2 + v) * 2) * 1024u;
    bld16<0, FRONT>(wv[0], rsw, l16, wo);
    bld16<1024, FRONT>(wv[1], rsw, l16, wo);
  };
  if constexpr (!FRONT) issue_loads();
  SB();
  float twc[16], tws[16], itc[8], its[8];
  {
    const float fk = (float)(16 * u + c16) * (1.f / 2048.f);
    const float c0 = cos2pi((float)(4 * g) * fk), s0 = sin2pi((float)(4 * g) * fk);
    const float cr = cos2pi(fk), sr = sin2pi(fk), ci = cos2pi(16.f * fk), si = sin2pi(16.f * fk);
    twc[0] = c0; tws[0] = s0;
#pragma unroll
    for (int r = 1; r < 4; ++r) { twc[r] = twc[r - 1] * cr - tws[r - 1] * sr; tws[r] = tws[r - 1] * cr + twc[r - 1] * sr; }
#pragma unroll
    for (int i = 1; i < 4; ++i)
#pragma unroll
      for (int r = 0; r < 4; ++r) {
        twc[i * 4 + r] = twc[(i - 1) * 4 + r] * ci - tws[(i - 1) * 4 + r] * si;
        tws[i * 4 + r] = tws[(i - 1) * 4 + r] * ci + twc[(i - 1) * 4 + r] * si;
      }
#pragma unroll
    for (int ii = 0; ii < 2; ++ii) {
      const float fm = (float)(16 * (2 * v + ii) + c16) * (1.f / 2048.f);
      const float a0 = (float)(16 * u + 4 * g) * fm;
      const float cm = cos2pi(fm), sm = sin2pi(fm);
      itc[ii * 4] = cos2pi(a0) * (1.f / 2048.f); its[ii * 4] = sin2pi(a0) * (1.f / 2048.f);
#pragma unroll
      for (int r = 1; r < 4; ++r) {
        itc[ii * 4 + r] = itc[ii * 4 + r - 1] * cm - its[ii * 4 + r - 1] * sm;
        its[ii * 4 + r] = its[ii * 4 + r - 1] * cm + itc[ii * 4 + r - 1] * sm;
      }
    }
#pragma unroll
    for (int k = 0; k < 16; ++k) asm volatile("" : "+v"(twc[k]), "+v"(tws[k]));
#pragma unroll
    for (int k = 0; k < 8; ++k) asm volatile("" : "+v"(itc[k]), "+v"(its[k]));
  }
  u32x4 f64r[2][2], f64i[2][2], f64n[2][2], f32r, f32i, f32n;
  if constexpr (FRONT) { wait_hook(); issue_loads(); }
  if constexpr (FRONT) asm volatile("s_waitcnt vmcnt(2)" : "+v"(raw[0]), "+v"(raw[1]), "+v"(raw[2]), "+v"(raw[3]), "+v"(st4[0]), "+v"(st4[1]), "+v"(st4[2]), "+v"(st4[3]) :: "memory");
  else asm volatile("s_waitcnt vmcnt(5)" : "+v"(raw[0]), "+v"(raw[1]), "+v"(raw[2]), "+v"(raw[3]), "+v"(st4[0]), "+v"(st4[1]), "+v"(st4[2]), "+v"(st4[3]) :: "memory");
  const bool special = (oct == 0 && hf == 0);
  {
    float val[4][4];
#pragma unroll
    for (int r = 0; r < 4; ++r) {
      const float sv = __builtin_bit_cast(float, st4[r]);
      const float rs = (LAYER == 0) ? sv : __builtin_amdgcn_rcpf(ln_den(sv * (1.f / 640.f)));
      val[r][0] = bflo(raw[r][0]) * rs; val[r][1] = special ? 0.f : bfhi(raw[r][0]) * rs;
      val[r][2] = bflo(raw[r][1]) * rs; val[r][3] = bfhi(raw[r][1]) * rs;
    }
    const int ti = tid >> 7, tg = (tid >> 5) & 3, tu = (tid >> 4) & 1, tc = tid & 15;
#pragma unroll
    for (int k = 0; k < 4; ++k)
      *(u32x2*)(sA + (k * 4 + ti) * 1024 + (tg * 16 + tc) * 16 + tu * 8) = u32x2{pk2(val[0][k], val[1][k]), pk2(val[2][k], val[3][k])};
  }
  asm volatile("s_waitcnt vmcnt(2) lgkmcnt(0)\n\ts_barrier" ::: "memory");
  f32r = *(const u32x4*)(sT + (16 + u * 2 + 0) * 1024 + l16);
  f32i = *(const u32x4*)(sT + (16 + u * 2 + 1) * 1024 + l16);
  f32n = f32i ^ 0x80008000u;
  const f32x4 Z4 = {0.f, 0.f, 0.f, 0.f};
  u32x4 yr[2], yi[2];
  {
    u32x4 xr[4], xi[4];
#pragma unroll
    for (int i = 0; i < 4; ++i) {
      xr[i] = *(const u32x4*)(sA + ((cc * 2 + 0) * 4 + i) * 1024 + l16);
      xi[i] = *(const u32x4*)(sA + ((cc * 2 + 1) * 4 + i) * 1024 + l16);
    }
#pragma unroll
    for (int ii = 0; ii < 2; ++ii)
#pragma unroll
      for (int t = 0; t < 2; ++t) {
        f64r[ii][t] = *(const u32x4*)(sT + (((2 * v + ii) * 2 + t) * 2 + 0) * 1024 + l16);
        f64i[ii][t] = *(const u32x4*)(sT + (((2 * v + ii) * 2 + t) * 2 + 1) * 1024 + l16);
        f64n[ii][t] = f64i[ii][t] ^ 0x80008000u;
      }
    f32x4 dr[4], di[4];
#pragma unroll
    for (int i = 0; i < 4; ++i) { dr[i] = mfma(xr[i], f32r, Z4); di[i] = mfma(xr[i], f32i, Z4); }
#pragma unroll
    for (int i = 0; i < 4; ++i) { dr[i] = mfma(xi[i], f32n, dr[i]); di[i] = mfma(xi[i], f32r, di[i]); }
    float tr[16], tq[16];
#pragma unroll
    for (int i = 0; i < 4; ++i)
#pragma unroll
      for (int r = 0; r < 4; ++r) {
        tr[i * 4 + r] = dr[i][r] * twc[i * 4 + r] + di[i][r] * tws[i * 4 + r];
        tq[i * 4 + r] = di[i][r] * twc[i * 4 + r] - dr[i][r] * tws[i * 4 + r];
      }
#pragma unroll
    for (int t = 0; t < 2; ++t) {
      yr[t] = u32x4{pk2(tr[8 * t + 0], tr[8 * t + 1]), pk2(tr[8 * t + 2], tr[8 * t + 3]), pk2(tr[8 * t + 4], tr[8 * t + 5]), pk2(tr[8 * t + 6], tr[8 * t + 7])};
      yi[t] = u32x4{pk2(tq[8 * t + 0], tq[8 * t + 1]), pk2(tq[8 * t + 2], tq[8 * t + 3]), pk2(tq[8 * t + 4], tq[8 * t + 5]), pk2(tq[8 * t + 6], tq[8 * t + 7])};
    }
  }
  u32x4 zr[2], zi[2];
  {
    f32x4 dr[2] = {Z4, Z4}, di[2] = {Z4, Z4};
#pragma unroll
    for (int t = 0; t < 2; ++t) {
#pragma unroll
      for (int ii = 0; ii < 2; ++ii) { dr[ii] = mfma(f64r[ii][t], yr[t], dr[ii]); di[ii] = mfma(f64i[ii][t], yr[t], di[ii]); }
#pragma unroll
      for (int ii = 0; ii < 2; ++ii) { dr[ii] = mfma(f64n[ii][t], yi[t], dr[ii]); di[ii] = mfma(f64r[ii][t], yi[t], di[ii]); }
    }
    asm volatile("s_waitcnt vmcnt(0)" : "+v"(wv[0]), "+v"(wv[1]) :: "memory");
    float pr[8], pi[8];
#pragma unroll
    for (int ii = 0; ii < 2; ++ii)
#pragma unroll
      for (int r = 0; r < 4; ++r) {
        const u32 wd = wv[ii][r];
        const float a = bflo(wd), bb = bfhi(wd);
        pr[ii * 4 + r] = dr[ii][r] * a - di[ii][r] * bb;
        pi[ii * 4 + r] = dr[ii][r] * bb + di[ii][r] * a;
      }
    const u32x4 mr = u32x4{pk2(pr[0], pr[1]), pk2(pr[2], pr[3]), pk2(pr[4], pr[5]), pk2(pr[6], pr[7])};
    const u32x4 mi = u32x4{pk2(pi[0], pi[1]), pk2(pi[2], pi[3]), pk2(pi[4], pi[5]), pk2(pi[6], pi[7])};
    *(u32x4*)(sE + ((((cc * 2 + u) * 2 + v) * 2 + 0) * 1024) + l16) = mr;
    *(u32x4*)(sE + ((((cc * 2 + u) * 2 + v) * 2 + 1) * 1024) + l16) = mi;
    asm volatile("s_waitcnt lgkmcnt(0)\n\ts_barrier" ::: "memory");
    const u32x4 or_ = *(const u32x4*)(sE + ((((cc * 2 + u) * 2 + (1 - v)) * 2 + 0) * 1024) + l16);
    const u32x4 oi_ = *(const u32x4*)(sE + ((((cc * 2 + u) * 2 + (1 - v)) * 2 + 1) * 1024) + l16);
    zr[0] = v ? or_ : mr; zi[0] = v ? oi_ : mi;
    zr[1] = v ? mr : or_; zi[1] = v ? mi : oi_;
  }
  {
    f32x4 dr[2] = {Z4, Z4}, di[2] = {Z4, Z4};
#pragma unroll
    for (int t = 0; t < 2; ++t) {
#pragma unroll
      for (int ii = 0; ii < 2; ++ii) { dr[ii] = mfma(zr[t], f64r[ii][t], dr[ii]); di[ii] = mfma(zr[t], f64n[ii][t], di[ii]); }
#pragma unroll
      for (int ii = 0; ii < 2; ++ii) { dr[ii] = mfma(zi[t], f64i[ii][t], dr[ii]); di[ii] = mfma(zi[t], f64r[ii][t], di[ii]); }
    }
#pragma unroll
    for (int ii = 0; ii < 2; ++ii) {
      float tr[4], tq[4];
#pragma unroll
      for (int r = 0; r < 4; ++r) {
        tr[r] = dr[ii][r] * itc[ii * 4 + r] - di[ii][r] * its[ii * 4 + r];
        tq[r] = dr[ii][r] * its[ii * 4 + r] + di[ii][r] * itc[ii * 4 + r];
      }
      *(u32x2*)(sG + ((((cc * 2 + v) * 2 + ii) * 2 + 0) * 1024) + l16 + u * 8) = u32x2{pk2(tr[0], tr[1]), pk2(tr[2], tr[3])};
      *(u32x2*)(sG + ((((cc * 2 + v) * 2 + ii) * 2 + 1) * 1024) + l16 + u * 8) = u32x2{pk2(tq[0], tq[1]), pk2(tq[2], tq[3])};
    }
  }
  asm volatile("s_waitcnt lgkmcnt(0)\n\ts_barrier" ::: "memory");
  {
    u32x4 tr[2], tq[2];
#pragma unroll
    for (int ii = 0; ii < 2; ++ii) {
      tr[ii] = *(const u32x4*)(sG + ((((cc * 2 + v) * 2 + ii) * 2 + 0) * 1024) + l16);
      tq[ii] = *(const u32x4*)(sG + ((((cc * 2 + v) * 2 + ii) * 2 + 1) * 1024) + l16);
    }
    f32x4 orr[2], oi[2];
#pragma unroll
    for (int ii = 0; ii < 2; ++ii) { orr[ii] = mfma(f32r, tr[ii], Z4); oi[ii] = mfma(f32r, tq[ii], Z4); }
#pragma unroll
    for (int ii = 0; ii < 2; ++ii) { orr[ii] = mfma(f32i, tq[ii], orr[ii]); oi[ii] = mfma(f32n, tr[ii], oi[ii]); }
#pragma unroll
    for (int ii = 0; ii < 2; ++ii) {
      const int tt = (2 * v + ii) * 128 + g * 32 + u * 16 + c16;
      *(u32x4*)(sY + (cc * 512 + tt) * 16) = u32x4{pk2(orr[ii][0], oi[ii][0]), pk2(orr[ii][1], oi[ii][1]), pk2(orr[ii][2], oi[ii][2]), pk2(orr[ii][3], oi[ii][3])};
    }
  }
  asm volatile("s_waitcnt lgkmcnt(0)\n\ts_barrier" ::: "memory");
  {
    char* Xout = ws + (LAYER == 0 ? OFF_X1 : OFF_X3);
    const u32x4 ya = *(const u32x4*)(sY + tid * 16), yb = *(const u32x4*)(sY + (512 + tid) * 16);
#pragma unroll
    for (int r = 0; r < 4; ++r) {
      const u32 a = ya[r], bq = yb[r], r0 = raw[r][0], r1 = raw[r][1];
      const float v0 = bflo(r0) + bflo(a);
      const float v1 = special ? bfhi(r0) : bfhi(r0) + bfhi(a);
      const float v2 = bflo(r1) + bflo(bq), v3 = bfhi(r1) + bfhi(bq);
      *(u32x2*)(Xout + ((size_t)oct * 2048 + r * 512 + tid) * 16 + hf * 8) = u32x2{pk2(v0, v1), pk2(v2, v3)};
    }
  }
}

__global__ void __launch_bounds__(512, 2) k_col1(Params P) {
  extern __shared__ __attribute__((aligned(16))) char smem[];
  const int b = blockIdx.x, tid = threadIdx.x, lane = tid & 63, w = __builtin_amdgcn_readfirstlane(tid >> 6);
  if (b >= 160) {
    unsigned long long pc;
    CODE_ADDR("_Z5k_mlpILi1EEv6Params", pc); touch_code(pc, MLP1_CODE_BYTES, tid);
    return;
  }
  col_body<1, false>(P, smem, b, tid, lane, w, []() {});
}

DI void wt_loads(const Params& P, int WT, int w8, int lane, u32x2 (&wv)[2][2], u32& w0r, u32& w0i) {
  const int wa = WT & 15, wcg = WT >> 4, mp = w8 & 3, nh = w8 >> 2;
  const i32x4 rwr = mk_rsrc(P.wr, S_ * DF_ * 2), rwi = mk_rsrc(P.wi, S_ * DF_ * 2);
  const u32 wvo = (u32)(lane >> 3) * (DF_ * 2) + (u32)(lane & 7) * 8u + (u32)wcg * 64u;
  const u32 wso = (u32)((16 * nh + 32 * (4 * wa + mp)) * DF_ * 2);
  bld8(wv[0][0], rwr, wvo, wso); bld8(wv[0][1], rwi, wvo, wso);
  bld8(wv[1][0], rwr, wvo, wso + 8 * DF_ * 2); bld8(wv[1][1], rwi, wvo, wso + 8 * DF_ * 2);
  bldu16(w0r, rwr, (u32)(lane & 15) * (DF_ * 2) + 640u, wso); bldu16(w0i, rwi, (u32)(lane & 15) * (DF_ * 2) + 640u, wso);
}
DI void wt_stage(u32* st, int w8, int lane, const u32x2 (&wv)[2][2]) {
  const int mp = w8 & 3, nh = w8 >> 2;
#pragma unroll
  for (int i = 0; i < 2; ++i) {
    const u32 r0 = wv[i][0][0], r1 = wv[i][0][1], i0 = wv[i][1][0], i1 = wv[i][1][1];
    u32* d = st + (4 * (lane & 7)) * 132 + (16 * nh + 8 * i + (lane >> 3)) * 4 + mp;
    d[0] = (r0 & 0xffffu) | (i0 << 16); d[132] = (r0 >> 16) | (i0 & 0xffff0000u);
    d[264] = (r1 & 0xffffu) | (i1 << 16); d[396] = (r1 >> 16) | (i1 & 0xffff0000u);
  }
}
DI void wt_store(const Params& P, const u32* st, int WT, int tid) {
  const int wa = WT & 15, wcg = WT >> 4, v = wa >> 3, ii = (wa >> 2) & 1, g = wa & 3;
  const __amdgpu_buffer_rsrc_t rsw = __builtin_amdgcn_make_buffer_rsrc((void*)P.ws, 0, (int)WS_END, 0x00020000);
#pragma unroll
  for (int k = 0; k < 2; ++k) {
    const int it = k * 512 + tid, cl = it >> 5, k1 = it & 31;
    const u32x4 o = *(const u32x4*)(st + cl * 132 + k1 * 4);
    __builtin_amdgcn_raw_buffer_store_b128(o, rsw, (u32)((((cl * 4 + (k1 >> 4) * 2) * 2) * 64 + (k1 & 15)) * 16),
                                           (u32)OFF_WT + (u32)(((32 * wcg * 4 + v) * 2 + ii) * 64 + g * 16) * 16u, 16);
  }
}

__global__ void __launch_bounds__(512, 2) k_front(Params P) {
  extern __shared__ __attribute__((aligned(16))) char smem[];
  const int b = blockIdx.x, tid = threadIdx.x, lane = tid & 63, w8 = __builtin_amdgcn_readfirstlane(tid >> 6);
  char* ws = P.ws;
  gu32* flags = (gu32*)(ws + OFF_FLG);
  gu32* epw = (gu32*)(ws + OFF_EPOCH);
  u32 tag = 0;
  const int mp = w8 & 3, nh = w8 >> 2;
  const u32 lb = lds_addr(smem);
  constexpr int GL = 4;
  JobOps J;
  u32x4 q[2][KS1];
  QGen G0;
  auto gen_second = [&](int c5) {
    QGen G1;
    gen_q_init(G1, c5 * 8 + 2 * mp + 1, lane);
#pragma unroll
    for (int t = 0; t < KS1; ++t) q[1][t] = gen_q_step(G1);
#pragma unroll
    for (int t = 0; t < KS1; ++t) asm volatile("" : "+v"(q[1][t]));
  };
  auto genrest = [&](auto T, auto N) { constexpr int t = T, n = N; if constexpr (t + GL < KS1 && n == 0) q[0][t + GL] = gen_q_step(G0); };
  auto lane_now = [&]() { return (int)__builtin_amdgcn_mbcnt_hi(~0u, __builtin_amdgcn_mbcnt_lo(~0u, 0u)); };
  auto publish = [&]() {
    asm volatile("s_waitcnt vmcnt(0)\n\ts_barrier" ::: "memory");
    if (w8 == 0 && lane_now() == 0) __hip_atomic_store(flags + b * 16, tag, __ATOMIC_RELAXED, __HIP_MEMORY_SCOPE_AGENT);
  };
  const bool isW = b >= 160;
  const int sb = b - 160;
  const int ct5 = isW ? sb % 5 : (b >> 3) % 5;
  const int j0 = isW ? 32 + sb / 5 : (b & 7) * 4 + (b >> 3) / 5;
  const bool work = !isW || sb < 95;
  if (isW) {
    if (sb >= 88) {
      unsigned long long pc;
      CODE_ADDR("_Z7k_front6Params", pc); touch_code(pc, FRONT_CODE_BYTES, tid);
      CODE_ADDR("_Z5k_mlpILi0EEv6Params", pc); touch_code(pc, MLP0_CODE_BYTES, tid);
      typedef __attribute__((address_space(1))) const u32 gu1;
      const u32 o1 = (u32)tid * 65536u, o2 = o1 + 512u * 65536u;
      const u32 a = *(gu1*)((unsigned long long)P.ws + (o1 < (u32)WS_END ? o1 : 0u));
      const u32 c = *(gu1*)((unsigned long long)P.ws + (o2 < (u32)WS_END ? o2 : 0u));
      const u32 d = *(gu1*)((unsigned long long)P.out + (o1 < (u32)(S_ * D_ * 2) ? o1 : 0u));
      asm volatile("" :: "v"(a), "v"(c), "v"(d));
    }
    if (!work) {
      for (int fr = w8; fr < 20; fr += 8) *(u32x4*)(ws + OFF_TAB + fr * 1024 + lane * 16) = tab_frag(fr, lane);
      for (int i = tid; i < 2048; i += 512) { ((float*)(ws + OFF_SS2))[i] = 0.f; ((u32*)(ws + OFF_CNT))[i] = 0u; }
      return;
    }
    job_ops(J, P, j0, w8, lane, lb);
    static_for<0, 10>([&](auto K) { job_op<K>(J); asm volatile("s_nop 0"); });
    JobOps J1;
    job_ops(J1, P, j0 + 19, w8, lane, lb + 81920);
    static_for<0, 10>([&](auto K) { job_op<K>(J1); asm volatile("s_nop 0"); });
    gen_second(ct5);
    gen_q_init(G0, ct5 * 8 + 2 * mp, lane);
    static_for<0, GL>([&](auto T) { constexpr int t = T; q[0][t] = gen_q_step(G0); });
  } else {
    u32 epv;
    asm volatile("global_load_dword %0, %1, %2 sc1" : "=v"(epv) : "v"(0u), "s"((unsigned long long)epw) : "memory");
    job_ops(J, P, j0, w8, lane, lb);
    static_for<0, 10>([&](auto K) { job_op<K>(J); asm volatile("s_nop 0"); });
    gen_second(ct5);
    gen_q_init(G0, ct5 * 8 + 2 * mp, lane);
    static_for<0, GL>([&](auto T) { constexpr int t = T; q[0][t] = gen_q_step(G0); });
    asm volatile("s_waitcnt vmcnt(0)\n\ts_barrier" : "+v"(epv) :: "memory");
    tag = RFL(epv) + 1u;
  }
  if (!isW) {
    u32x2 wv[2][2]; u32 w0r, w0i;
    wt_loads(P, b, w8, lane, wv, w0r, w0i);
    qjob_compute<1>(P, j0, ct5, mp, nh, lane, smem, q, genrest);
    asm volatile("s_waitcnt vmcnt(4)" : "+v"(wv[0][0]), "+v"(wv[0][1]), "+v"(wv[1][0]), "+v"(wv[1][1]), "+v"(w0r), "+v"(w0i) :: "memory");
    u32* st = (u32*)(smem + 81920);
    wt_stage(st, w8, lane, wv);
    if ((b >> 4) == 0) {
      asm volatile("s_waitcnt lgkmcnt(0)\n\ts_barrier" ::: "memory");
      if (lane < 16) st[(16 * nh + lane) * 4 + mp] = w0r | (w0i << 16);
    }
    asm volatile("s_waitcnt lgkmcnt(0)\n\ts_barrier" ::: "memory");
    wt_store(P, st, b, tid);
    for (int fr = w8; fr < 20; fr += 8) *(u32x4*)(smem + 102400 + fr * 1024 + lane * 16) = tab_frag(fr, lane);
    publish();
    int lane2 = lane_now();
    asm volatile("" : "+v"(lane2));
    col_body<0, true>(P, smem, b, w8 * 64 + lane2, lane2, w8, [&]() {
      const int lane = lane2;
      if (w8 == 0) {
        const int oct = (b & 7) + 8 * (b >> 4);
        const int fi = lane < 32 ? ((lane & 3) * 5 + (oct >> 4)) * 8 + (lane >> 2) : (oct >> 3) * 16 + (lane & 15);
        unsigned spins = 0;
        for (;;) {
          bool ok = __hip_atomic_load(flags + fi * 16, __ATOMIC_RELAXED, __HIP_MEMORY_SCOPE_AGENT) == tag;
          if (b == 0) {
#pragma unroll
            for (int k = 0; k < 3; ++k) { const int f2 = lane + 64 * k; ok &= __hip_atomic_load(flags + (f2 < 160 ? f2 : 0) * 16, __ATOMIC_RELAXED, __HIP_MEMORY_SCOPE_AGENT) == tag; }
          }
          if (__all(ok)) break;
          __builtin_amdgcn_s_sleep(1);
          if (++spins > (1u << 20)) { if (lane == 0) __hip_atomic_store((gu32*)(ws + OFF_EPOCH) + 16, 1u, __ATOMIC_RELAXED, __HIP_MEMORY_SCOPE_AGENT); break; }
        }
        if (b == 0 && lane == 0) __hip_atomic_store(epw, tag, __ATOMIC_RELAXED, __HIP_MEMORY_SCOPE_AGENT);
      }
      asm volatile("s_waitcnt lgkmcnt(0)\n\ts_barrier" ::: "memory");
    });
  } else {
    asm volatile("s_waitcnt vmcnt(10)\n\ts_barrier" ::: "memory");
    qjob_compute<2>(P, j0, ct5, mp, nh, lane, smem, q, genrest);
    for (int jj = 1; jj < 3; ++jj) {
      const int job = j0 + 19 * jj;
      if (job >= 88) break;
      asm volatile("s_waitcnt vmcnt(4) lgkmcnt(0)\n\ts_barrier" ::: "memory");
      const bool pf = (jj == 1 && job + 19 < 88);
      JobOps J2;
      if (pf) job_ops(J2, P, job + 19, w8, lane, lb);
      qjob_compute<0>(P, job, ct5, mp, nh, lane, smem + (jj & 1) * 81920, q, [&](auto T, auto N) {
        if constexpr (N.value == 0 && T.value < 10) { if (pf) job_op<T.value>(J2); } });
    }
  }
}

DI f32x2 gelu2(f32x2 x) {
  const float k1 = -2.3022081978378774f;
  const float k2 = k1 * 0.044715f;
  const f32x2 t = x * x;
  const f32x2 u = t * k2 + k1;
  const f32x2 z = x * u;
  f32x2 d = f32x2{__builtin_amdgcn_exp2f(z[0]), __builtin_amdgcn_exp2f(z[1])} + 1.f;
  return x * f32x2{__builtin_amdgcn_rcpf(d[0]), __builtin_amdgcn_rcpf(d[1])};
}
template <int IMM> DI void dma16_nom0(i32x4 rsrc, u32 voff, u32 soff) {
  asm volatile("buffer_load_dwordx4 %0, %1, %2 offen offset:%3 lds" ::"v"(voff), "s"(rsrc), "s"(RFL(soff)), "i"(IMM) : "memory");
}
template <int N> DI void wait_vm_bar(u32x4& a, u32x4& b, u32x4& c, u32x4& d, u32x4& e, u32x4& f, u32x4& g, u32x4& h) { wait_vm_bar8<N>(a, b, c, d, e, f, g, h); }

constexpr int REGPF = 20;
constexpr int EPIPF = 10;
template <int LAYER, int NF, int NQ, class MH, class RH, class EH>
DI void gemm1_body(const Params& P, char* smem, int ft, int tt, int lane, int w, MH&& tail_hook, RH&& reg_hook, EH&& epi_hook) {
  constexpr int NM = NF + NQ, H = (NM + 1) / 2, RING = (LAYER == 0) ? 20 : 16, BR = 12;
  const int c16 = lane & 15, g = lane >> 4;
  char* ws = P.ws;
  const i32x4 rs = mk_rsrc(ws, (u32)WS_END);
  const u32 l16 = lane * 16;
  const int p0 = tt * 128 + w * 32;
  const int f0 = (ft < 12) ? 7 * ft : 84 + 6 * (ft - 12);
  u32x4 bq[BR][2];
  const u32 xoff = (u32)(LAYER == 0 ? OFF_X1 : OFF_X3) + (u32)p0 * 16;
  const u32 bvo = (u32)g * 32768u + (u32)c16 * 16u;
  const u32 asrc = (u32)OFF_A0 + (u32)(ft * KS1 + w) * 7168u, qsrc = (u32)OFF_A1 + (u32)(ft * KS1 + w) * 4096u;
  const u32 adst = lds_addr(smem) + (u32)w * (NM * 1024);
  constexpr int PER = NM + 8;
  auto issue = [&](auto C, auto K) {
    constexpr int c = C, k = K;
    if constexpr (k < NF) {
      constexpr int k4 = k & ~3;
      const u32 so = asrc + (u32)(c * 4 * 7 + k4) * 1024u;
      if constexpr ((k & 3) == 0) dma16<0>(rs, l16, so, adst + (u32)((((c * 4) % RING) * NM + k4) * 1024));
      else dma16_nom0<(k & 3) * 1024>(rs, l16, so);
    } else if constexpr (k < NM) {
      constexpr int kq = k - NF;
      const u32 so = qsrc + (u32)(c * 4 * 4) * 1024u;
      if constexpr (kq == 0) dma16<0>(rs, l16, so, adst + (u32)((((c * 4) % RING) * NM + NF) * 1024));
      else dma16_nom0<kq * 1024>(rs, l16, so);
    } else {
      constexpr int kk = (k - NM) >> 1, t = 4 * c + kk;
      const u32 so = xoff + (u32)t * 131072u;
      if constexpr (((k - NM) & 1) == 0) bld16<0>(bq[t % BR][0], rs, bvo, so);
      else bld16<256>(bq[t % BR][1], rs, bvo, so);
    }
  };
  static_for<0, 2>([&](auto C) { static_for<0, PER>([&](auto K) { issue(C, K); }); });
  u32x2 bw[NF];
  {
    const i32x4 rb1 = mk_rsrc(P.b1, F_ * 2);
#pragma unroll
    for (int m = 0; m < NF; ++m) bld8(bw[m], rb1, (u32)g * 8u, (u32)(f0 + m) * 32u);
  }
  f32x4 acc[NM][2], gram[2];
#pragma unroll
  for (int m = 0; m < NM; ++m) { acc[m][0] = f32x4{0.f, 0.f, 0.f, 0.f}; acc[m][1] = acc[m][0]; }
  gram[0] = gram[1] = f32x4{0.f, 0.f, 0.f, 0.f};
  u32x4 af[3][H];
  auto waitchunk = [&](auto C, auto N) {
    constexpr int c = C;
    wait_vm_bar<N.value>(bq[(4 * c) % BR][0], bq[(4 * c) % BR][1], bq[(4 * c + 1) % BR][0], bq[(4 * c + 1) % BR][1], bq[(4 * c + 2) % BR][0],
                         bq[(4 * c + 2) % BR][1], bq[(4 * c + 3) % BR][0], bq[(4 * c + 3) % BR][1]);
  };
  auto rd1 = [&](auto HH, auto K) {
    constexpr int h = HH, k = K, t = h >> 1, m0 = (h & 1) ? H : 0, m1 = (h & 1) ? NM : H;
    if constexpr (t < KS1 && m0 + k < m1) af[h % 3][k] = *(const u32x4*)(smem + ((t % RING) * NM + m0 + k) * 1024 + l16);
  };
  waitchunk(IC<0>{}, IC<PER + NF>{});
  static_for<0, H>([&](auto K) { rd1(IC<0>{}, K); });
  static_for<0, H>([&](auto K) { rd1(IC<1>{}, K); });
  static_for<0, 2 * KS1>([&](auto HH) {
    constexpr int h = HH, t = h >> 1, c = t >> 2, q = t & 3, m0 = (h & 1) ? H : 0, m1 = (h & 1) ? NM : H;
    constexpr int PS = (PER + 2) / 3;
    if constexpr ((h & 1) == 0 && q == 3 && t + 1 < KS1) waitchunk(IC<c + 1>{}, IC<(c + 1 < 4 ? PER : 6)>{});
    constexpr int n1 = (h + 1 < 2 * KS1) ? (((h + 1) & 1) ? NM - H : H) : 0;
    __builtin_amdgcn_s_waitcnt(0xC07F | (n1 << 8));
    SB();
    static_for<m0, m1>([&](auto M) {
      constexpr int m = M;
      acc[m][0] = mfma(af[h % 3][m - m0], bq[t % BR][0], acc[m][0]);
      SB();
      rd1(IC<h + 2>{}, IC<m - m0>{});
      SB();
      acc[m][1] = mfma(af[h % 3][m - m0], bq[t % BR][1], acc[m][1]);
      SB();
      if constexpr (c + 2 < 5 && q < 3 && m < PS && q * PS + m < PER) issue(IC<c + 2>{}, IC<q * PS + m>{});
      if constexpr (h >= 24 && h < 39 && m == m0) tail_hook(IC<h - 24>{}, IC<((LAYER == 0) ? 0 : 4 * NM)>{});
      if constexpr (h >= 30 && m > m0 && m <= m0 + 2 && 2 * (h - 30) + (m - m0 - 1) < REGPF) reg_hook(IC<2 * (h - 30) + (m - m0 - 1)>{});
      SB();
    });
    if constexpr ((h & 1) == 0) {
      u32x4 e0 = bq[t % BR][0], e1 = bq[t % BR][1];
      if constexpr (t == 0) { e0[0] = (g == 0) ? (e0[0] & 0xffffu) : (u32)e0[0]; e1[0] = (g == 0) ? (e1[0] & 0xffffu) : (u32)e1[0]; }
      gram[0] = mfma(e0, e0, gram[0]);
      gram[1] = mfma(e1, e1, gram[1]);
      SB();
    } else if constexpr (NM - H < H) {
      rd1(IC<h + 2>{}, IC<H - 1>{});
    }
  });
  f32x2 bb[NF][2];
#pragma unroll
  for (int m = 0; m < NF; ++m) {
    asm volatile("" : "+v"(bw[m]));
    bb[m][0] = f32x2{bflo(bw[m][0]), bfhi(bw[m][0])}; bb[m][1] = f32x2{bflo(bw[m][1]), bfhi(bw[m][1])};
  }
  float a_sc[2];
#pragma unroll
  for (int n = 0; n < 2; ++n) {
    const int r = c16 & 3;
    float dv = r == 0 ? gram[n][0] : r == 1 ? gram[n][1] : r == 2 ? gram[n][2] : gram[n][3];
    dv = (g == (c16 >> 2)) ? dv : 0.f;
    const float var = red_g(dv) * (1.f / 640.f);
    const float d1 = ln_den(var);
    a_sc[n] = rcp(d1 * ln_den(var * rcp(d1 * d1)));
  }
  const auto rsb = __builtin_amdgcn_make_buffer_rsrc((void*)ws, 0, (int)WS_END, 0x00020000);
  const u32 vW = ((u32)(lane >> 5) * 16u + (u32)c16) * 16u + (u32)(g & 1) * 8u;
  const u32 hso = (u32)(LAYER == 0 ? OFF_H1 : OFF_H2) + (u32)((tt * 2 + (w >> 1)) * KS2 * 4 + 2 * (w & 1)) * 1024u;
  static_for<0, NF>([&](auto M) {
    constexpr int m = M;
    const int mg = f0 + m;
    const u32 so = hso + (u32)(mg >> 1) * 4096u + (u32)(mg & 1) * 512u;
    epi_hook(M);
#pragma unroll
    for (int n = 0; n < 2; ++n) {
      const f32x4 a = acc[m][n];
      const f32x2 h01 = gelu2(f32x2{a[0], a[1]} * a_sc[n] + bb[m][0]), h23 = gelu2(f32x2{a[2], a[3]} * a_sc[n] + bb[m][1]);
      __builtin_amdgcn_raw_buffer_store_b64(v2u_t{pk2(h01[0], h01[1]), pk2(h23[0], h23[1])}, rsb, vW, so + n * 1024u, 16);
    }
  });
  if constexpr (NQ > 0) {
    const int d0 = (ft < 12) ? 2 * ft : 24 + 4 * (ft - 12);
#pragma unroll
    for (int m = 0; m < NQ; ++m)
#pragma unroll
      for (int n = 0; n < 2; ++n) {
        const f32x4 a = acc[NF + m][n];
        __builtin_amdgcn_raw_buffer_store_b64(v2u_t{pk2(a[0], a[1]), pk2(a[2], a[3])}, rsb, (u32)lane * 8u,
                                              (u32)OFF_X3S + (u32)((d0 + m) * 128 + tt * 8 + w * 2 + n) * 512u, 16);
      }
  }
}


template <int LAYER>
__global__ void __launch_bounds__(256, 1) k_mlp(Params P) {
  extern __shared__ __attribute__((aligned(16))) char smem[];
  const int b = blockIdx.x, tid = threadIdx.x, lane = tid & 63, w = __builtin_amdgcn_readfirstlane(tid >> 6);
  char* ws = P.ws;
  const int x = b & 7, li = b >> 3;
  const int ft = (x & 1) * 8 + (li & 7), tt = (x >> 1) * 4 + (li >> 3);
  const int ct = ft >> 1, t64 = 2 * tt + (ft & 1);
  const u32 l16 = lane * 16;
  const u32 aoff = (u32)(LAYER == 0 ? OFF_W2F : OFF_W2T) + (u32)ct * (KS2 * 5 * 1024) + (u32)w * 5120u;
  const auto rs = __builtin_amdgcn_make_buffer_rsrc((void*)ws, 0, (int)WS_END, 0x00020000);
  const bool full = (w < 2);
  u32x4 rg[11][5];
  auto lda1 = [&](auto I) {
    constexpr int i = I;
    if (i < 10 || full) {
#pragma unroll
      for (int m = 0; m < 4; ++m) rg[i][m] = __builtin_amdgcn_raw_buffer_load_b128(rs, l16 + m * 1024, aoff + (u32)(3 + i) * 20480u, 0);
      rg[i][4] = __builtin_amdgcn_raw_buffer_load_b128(rs, l16, aoff + (u32)(3 + i) * 20480u + 4096u, 0);
    } else {
#pragma unroll
      for (int m = 0; m < 5; ++m) rg[i][m] = u32x4{0u, 0u, 0u, 0u};
    }
  };
  auto lda_f = [&](auto K) {
    constexpr int k = K, i = k / 5, m = k % 5;
    if constexpr (m < 4) rg[i][m] = __builtin_amdgcn_raw_buffer_load_b128(rs, l16 + m * 1024, aoff + (u32)(3 + i) * 20480u, 0);
    else rg[i][4] = __builtin_amdgcn_raw_buffer_load_b128(rs, l16, aoff + (u32)(3 + i) * 20480u + 4096u, 0);
  };
  u32 lA;
  {
    const i32x4 rsa = mk_rsrc(ws, (u32)WS_END);
    auto tail = [&](auto I, auto KB) {
      constexpr int i = I, st = i / 5, m = i % 5;
      const u32 lw = lds_addr(smem) + (u32)(KB.value * 1024) + (u32)w * 15360u;
      if constexpr (m == 0) dma16<0>(rsa, l16, aoff + st * 20480u, lw + st * 5120u);
      else if constexpr (m < 4) dma16_nom0<m * 1024>(rsa, l16, aoff + st * 20480u);
      else dma16<0>(rsa, l16, aoff + st * 20480u + 4096u, lw + st * 5120u + 4096u);
    };
        auto regh = [&](auto K) { lda_f(K); };
    auto epi = [&](auto M) { constexpr int m = M;
      if constexpr (2 * m < EPIPF) lda_f(IC<REGPF + 2 * m>{});
      if constexpr (2 * m + 1 < EPIPF) lda_f(IC<REGPF + 2 * m + 1>{}); };
    if (ft < 12) { gemm1_body<LAYER, 7, LAYER == 0 ? 0 : 2>(P, smem, ft, tt, lane, w, tail, regh, epi); lA = (LAYER == 0) ? 0 : 4 * 9 * 1024; }
    else { gemm1_body<LAYER, 6, LAYER == 0 ? 0 : 4>(P, smem, ft, tt, lane, w, tail, regh, epi); lA = (LAYER == 0) ? 0 : 4 * 10 * 1024; }
    lA += (u32)w * 15360u;
  }
  gu32* cnt = (gu32*)(ws + OFF_CNT) + (LAYER * 16 + tt) * 32;
  asm volatile("s_waitcnt vmcnt(0)\n\ts_barrier" ::: "memory");
  if (tid == 0) __hip_atomic_fetch_add(cnt, 1u, __ATOMIC_RELAXED, __HIP_MEMORY_SCOPE_AGENT);
  const u32 boff = (u32)(LAYER == 0 ? OFF_H1 : OFF_H2) + (u32)t64 * (KS2 * 4096) + (u32)w * 4096u;
  u32 bo[4];
#pragma unroll
  for (int n = 0; n < 4; ++n) bo[n] = boff + (u32)((n + w) & 3) * 1024u;
  constexpr int PD = 5, NSTEP = 14;
  const u32 vX = (u32)(lane >> 5) * 32768u + (u32)((lane >> 4) & 1) * 8u + (u32)(lane & 15) * 16u;
  const u32 xso = (u32)(10 * ct * 2048 + t64 * 64 + w * 16) * 16u;
  v2u_t rz[5]; f32x4 bz[5];
  auto ldbias = [&]() {
#pragma unroll
    for (int m = 0; m < 5; ++m) {
      if (LAYER == 0) {
        rz[m] = __builtin_amdgcn_raw_buffer_load_b64(rs, vX, (u32)OFF_X1 + xso + m * 65536u, 0);
        bz[m] = __builtin_bit_cast(f32x4, __builtin_amdgcn_raw_buffer_load_b128(rs, (u32)(lane >> 4) * 16u, (u32)OFF_B2F + (u32)(ct * 5 + m) * 64u, 0));
      } else {
        const u32x2 bw = *(const u32x2*)(P.b2 + (ct * 5 + m) * 16 + 4 * (lane >> 4));
        bz[m] = f32x4{bflo(bw[0]), bfhi(bw[0]), bflo(bw[1]), bfhi(bw[1])};
      }
    }
  };
  static_for<REGPF + EPIPF, 50>([&](auto K) { lda_f(K); });
  lda1(IC<10>{});
  ldbias();
  SB();
  if (tid == 0) {
    unsigned spins = 0;
    while (__hip_atomic_load(cnt, __ATOMIC_RELAXED, __HIP_MEMORY_SCOPE_AGENT) < 16u) {
      __builtin_amdgcn_s_sleep(1);
      if (++spins > (1u << 22)) { __hip_atomic_store((gu32*)(ws + OFF_CNT) + 1024, 1u, __ATOMIC_RELAXED, __HIP_MEMORY_SCOPE_AGENT); break; }
    }
  }
  asm volatile("s_barrier" ::: "memory");
  u32x4 rb[PD][4];
  auto ldb = [&](auto I, auto K) { constexpr int i = I, k = K; rb[i % PD][k] = __builtin_amdgcn_raw_buffer_load_b128(rs, l16, bo[k] + (u32)i * 16384u, 16); };
  static_for<0, PD - 1>([&](auto I) { static_for<0, 4>([&](auto K) { ldb(I, K); }); });
  SB();
  if (LAYER == 1) {
#pragma unroll
    for (int m = 0; m < 5; ++m) rz[m] = __builtin_amdgcn_raw_buffer_load_b64(rs, (u32)lane * 8u, (u32)OFF_X3S + (u32)((ct * 5 + m) * 128 + t64 * 4 + w) * 512u, 16);
  }
  f32x4 acc[5][4];
#pragma unroll
  for (int m = 0; m < 5; ++m)
#pragma unroll
    for (int n = 0; n < 4; ++n) acc[m][n] = f32x4{0.f, 0.f, 0.f, 0.f};
  u32x4 al[2][5];
  const char* lbase = smem + lA + l16;
#pragma unroll
  for (int m = 0; m < 5; ++m) al[0][m] = *(const u32x4*)(lbase + m * 1024);
  SB();
  static_for<0, NSTEP - 1>([&](auto I) {
    constexpr int i = I, nx = i + PD - 1;
    static_for<0, 5>([&](auto M) {
      constexpr int m = M;
#pragma unroll
      for (int n = 0; n < 4; ++n) {
        if constexpr (i < 3) acc[m][n] = mfma(al[i & 1][m], rb[i % PD][n], acc[m][n]);
        else acc[m][n] = mfma(rg[i - 3][m], rb[i % PD][n], acc[m][n]);
      }
      SB();
      if constexpr (i + 1 < 3) al[(i + 1) & 1][m] = *(const u32x4*)(lbase + ((i + 1) * 5 + m) * 1024);
      if constexpr (m < 4) {
        if constexpr (nx < NSTEP - 1) ldb(IC<nx>{}, M);
        else if constexpr (nx == NSTEP - 1) { if (full) ldb(IC<nx>{}, M); }
      }
      SB();
    });
  });
  if (full) {
    constexpr int i = NSTEP - 1;
#pragma unroll
    for (int m = 0; m < 5; ++m)
#pragma unroll
      for (int n = 0; n < 4; ++n) acc[m][n] = mfma(rg[10][m], rb[i % PD][n], acc[m][n]);
  }
  SB();
  asm volatile("s_barrier" ::: "memory");
  f32x4* red = (f32x4*)smem + lane;
#pragma unroll
  for (int n = 1; n < 4; ++n) {
    f32x4* dst = red + (((n + w) & 3) * 15 + (n - 1) * 5) * 64;
#pragma unroll
    for (int m = 0; m < 5; ++m) dst[m * 64] = acc[m][n];
  }
  asm volatile("s_waitcnt lgkmcnt(0)\n\ts_barrier" ::: "memory");
  float ssq = 0.f;
  const f32x4* src = red + w * 15 * 64;
#pragma unroll
  for (int m = 0; m < 5; ++m) {
    const f32x4 o = (acc[m][0] + src[m * 64]) + (src[(5 + m) * 64] + src[(10 + m) * 64]) +
                    (f32x4{bflo(rz[m][0]), bfhi(rz[m][0]), bflo(rz[m][1]), bfhi(rz[m][1])} + bz[m]);
    const v2u_t ov = v2u_t{pk2(o[0], o[1]), pk2(o[2], o[3])};
    if (LAYER == 0) {
      __builtin_amdgcn_raw_buffer_store_b64(ov, rs, vX, (u32)OFF_X2 + xso + m * 65536u, 0);
      f32x4 q2 = o * o;
      if (m == 0) q2[1] = (ct == 0 && lane < 16) ? 0.f : q2[1];
      ssq += (q2[0] + q2[1]) + (q2[2] + q2[3]);
    } else {
      *(v2u_t*)(P.out + (size_t)s_of_p(t64 * 64 + w * 16 + (lane & 15)) * D_ + (ct * 5 + m) * 16 + 4 * (lane >> 4)) = ov;
    }
  }
  if (LAYER == 0) {
    ssq = red_g(ssq);
    if (lane < 16) atomicAdd((float*)(ws + OFF_SS2) + t64 * 64 + w * 16 + lane, ssq);
  }
}

extern "C" void kernel_launch(void* const* d_in, const int* in_sizes, int n_in, void* d_out, int out_size, void* d_ws,
                              size_t ws_size, hipStream_t stream) {
  Params P;
  P.x = (const u16*)d_in[0]; P.wr = (const u16*)d_in[1]; P.wi = (const u16*)d_in[2];
  P.w1 = (const u16*)d_in[4]; P.b1 = (const u16*)d_in[5]; P.w2 = (const u16*)d_in[6]; P.b2 = (const u16*)d_in[7];
  P.out = (u16*)d_out; P.ws = (char*)d_ws;
  {
    hipFuncSetAttribute((const void*)k_front, hipFuncAttributeMaxDynamicSharedMemorySize, 163840);
    hipFuncSetAttribute((const void*)k_col1, hipFuncAttributeMaxDynamicSharedMemorySize, 163840);
    hipFuncSetAttribute((const void*)k_mlp<0>, hipFuncAttributeMaxDynamicSharedMemorySize, 163840);
    hipFuncSetAttribute((const void*)k_mlp<1>, hipFuncAttributeMaxDynamicSharedMemorySize, 163840);
  }
  k_front<<<256, 512, 163840, stream>>>(P);
  k_mlp<0><<<256, 256, 163840, stream>>>(P);
  k_col1<<<168, 512, 86016, stream>>>(P);
  k_mlp<1><<<256, 256, 163840, stream>>>(P);
}
```
